```python
import torch

def _softmax(x, axis=-1):
    return torch.softmax(x, dim=axis)
def _gelu(x):
    return torch.nn.functional.gelu(x, approximate="tanh")
def _silu(x):
    return torch.nn.functional.silu(x)
def _sigmoid(x):
    return torch.sigmoid(x)
def _layernorm(x, axis=-1, eps=1e-5):
    m = x.mean(dim=axis, keepdim=True)
    v = x.var(dim=axis, keepdim=True, unbiased=False)
    return (x - m) / torch.sqrt(v + eps)
def _scatter_add(tgt, idx, src, axis=0):
    return tgt.clone().index_add_(axis, idx, src)
def _segment_sum(src, seg, n):
    out = torch.zeros((n,) + src.shape[1:], dtype=src.dtype)
    return out.index_add_(0, seg, src)
def _pad(x, pw):
    flat = tuple(v for pair in reversed(pw) for v in pair)
    return torch.nn.functional.pad(x, flat)

kernel_name = 'interleave_layers__linear_attn_block__geglu_ffn__prompt'

def setup_inputs():

    _w = (lambda a: (lambda t: t.to(torch.bfloat16) if t.is_floating_point() else t)(torch.as_tensor(a)))
    def _A_setup():
        B, S, D = 1, 2048, 576
        return {
            "x": _w((lambda *s: torch.randn(*s, dtype=torch.bfloat16) * 0.05)(B, S, D)),
            "wqkv": _w((lambda *s: torch.randn(*s, dtype=torch.bfloat16) * 0.05)(D, 3 * D)),
            "wo": _w((lambda *s: torch.randn(*s, dtype=torch.bfloat16) * 0.05)(D, D)),
        }
    _A_in = _A_setup()

    def _B_setup():
        B, S, D, F = 1, 2048, 576, 4608
        return {
            "x": _w((lambda *s: torch.randn(*s, dtype=torch.bfloat16) * 0.05)(B, S, D)),
            "w_gate": _w((lambda *s: torch.randn(*s, dtype=torch.bfloat16) * 0.05)(D, F)),
            "w_up": _w((lambda *s: torch.randn(*s, dtype=torch.bfloat16) * 0.05)(D, F)),
            "w_down": _w((lambda *s: torch.randn(*s, dtype=torch.bfloat16) * 0.05)(F, D)),
        }
    _B_in = _B_setup()

    return {
        "A_x": _A_in["x"],
        "A_wqkv": _A_in["wqkv"],
        "A_wo": _A_in["wo"],
        "B_x": _B_in["x"],
        "B_w_gate": _B_in["w_gate"],
        "B_w_up": _B_in["w_up"],
        "B_w_down": _B_in["w_down"],
    }

def reference(A_x, A_wqkv, A_wo, B_x, B_w_gate, B_w_up, B_w_down):
    def _A_ref(x, wqkv, wo):
        B, S, D = x.shape
        H = 32; Dh = D // H
        qkv = (lambda x, *d: x.permute(*d))((x @ wqkv).reshape(B, S, 3, H, Dh), 2, 0, 3, 1, 4)
        q, k, v = qkv[0], qkv[1], qkv[2]
        phi = lambda t: torch.where(t > 0, t + 1, torch.exp(t)) + 1e-6
        pq, pk = phi(q), phi(k)
        kv = torch.einsum("bhsd,bhse->bhde", pk, v)
        z = torch.einsum("bhsd->bhd", pk)
        num = torch.einsum("bhqd,bhde->bhqe", pq, kv)
        den = torch.einsum("bhqd,bhd->bhq", pq, z)[..., None] + 1e-6
        o = num / den
        return (lambda x, *d: x.permute(*d))(o, 0, 2, 1, 3).reshape(B, S, D) @ wo

    def _B_ref(x, w_gate, w_up, w_down):
        return (_gelu(x @ w_gate) * (x @ w_up)) @ w_down

    x = A_x
    x = x + _A_ref(_layernorm(x, axis=-1), A_wqkv, A_wo)
    x = x + _B_ref(_layernorm(x, axis=-1), B_w_gate, B_w_up, B_w_down)
    x = x + _A_ref(_layernorm(x, axis=-1), A_wqkv, A_wo)
    x = x + _B_ref(_layernorm(x, axis=-1), B_w_gate, B_w_up, B_w_down)
    return x
```

```cpp
#include <hip/hip_runtime.h>
#include <hip/hip_bf16.h>
typedef __hip_bfloat16 bf16;
static __device__ __forceinline__ float b2f(bf16 v){ return __bfloat162float(v); }
static __device__ __forceinline__ bf16 f2b(float v){ return __float2bfloat16(v); }
constexpr int S=2048,D=576,H=32,DH=18,F=4608;

__global__ void k_cvt(const bf16* x,float* y,int n){int i=blockIdx.x*256+threadIdx.x; if(i<n) y[i]=b2f(x[i]);}
__global__ void k_ln(const float* x,bf16* h){
  int row=blockIdx.x; int t=threadIdx.x; __shared__ float red[64];
  float v[9]; float s=0; for(int i=0;i<9;++i){v[i]=x[row*D+t+64*i]; s+=v[i];}
  for(int o=32;o;o>>=1) s+=__shfl_xor(s,o);
  float m=s/D; float q=0; for(int i=0;i<9;++i){float d=v[i]-m; q+=d*d;}
  for(int o=32;o;o>>=1) q+=__shfl_xor(q,o);
  float r=rsqrtf(q/D+1e-5f);
  for(int i=0;i<9;++i) h[row*D+t+64*i]=f2b((v[i]-m)*r);
}
__global__ void k_gemm(const bf16* A,const bf16* B,float* C,int M,int N,int K){
  __shared__ float As[32][33],Bs[32][33];
  int tx=threadIdx.x&31,ty=threadIdx.x>>5;
  int bm=blockIdx.y*32,bn=blockIdx.x*32;
  float acc[4]={0,0,0,0};
  for(int k0=0;k0<K;k0+=32){
    for(int i=0;i<4;++i){int r=ty+8*i; As[r][tx]=b2f(A[(size_t)(bm+r)*K+k0+tx]); int c=bn+tx; Bs[r][tx]=c<N?b2f(B[(size_t)(k0+r)*N+c]):0.f;}
    __syncthreads();
    for(int k=0;k<32;++k){float b=Bs[k][tx]; for(int i=0;i<4;++i) acc[i]+=As[ty+8*i][k]*b;}
    __syncthreads();
  }
  for(int i=0;i<4;++i){int c=bn+tx; if(c<N) C[(size_t)(bm+ty+8*i)*N+c]=acc[i];}
}
__device__ __forceinline__ float phi(float t){return (t>0?t+1.f:__expf(t))+1e-6f;}
__global__ void k_kv(const float* qkv,float* kv,float* z){
  int h=blockIdx.x; int t=threadIdx.x;
  if(t>=DH*(DH+1)) return; int d=t/(DH+1),e=t%(DH+1);
  float s=0; for(int i=0;i<S;++i){float pk=phi(qkv[(size_t)i*3*D+D+h*DH+d]); float v=e<DH?qkv[(size_t)i*3*D+2*D+h*DH+e]:1.f; s+=pk*v;}
  if(e<DH) kv[(h*DH+d)*DH+e]=s; else z[h*DH+d]=s;
}
__global__ void k_o(const float* qkv,const float* kv,const float* z,bf16* o){
  int i=blockIdx.x; int c=threadIdx.x; if(c>=D) return; int h=c/DH,e=c%DH;
  float num=0,den=0; for(int d=0;d<DH;++d){float pq=phi(qkv[(size_t)i*3*D+h*DH+d]); num+=pq*kv[(h*DH+d)*DH+e]; den+=pq*z[h*DH+d];}
  o[(size_t)i*D+c]=f2b(num/(den+1e-6f));
}
__global__ void k_add(float* x,const float* y,int n){int i=blockIdx.x*256+threadIdx.x; if(i<n) x[i]=b2f(f2b(x[i]+y[i]));}
__global__ void k_glu(const float* g,const float* u,bf16* hh,int n){int i=blockIdx.x*256+threadIdx.x; if(i<n){float x=g[i]; float t=0.5f*x*(1.f+tanhf(0.7978845608f*(x+0.044715f*x*x*x))); hh[i]=f2b(t*u[i]);}}
__global__ void k_out(const float* x,bf16* o,int n){int i=blockIdx.x*256+threadIdx.x; if(i<n) o[i]=f2b(x[i]);}

extern "C" void kernel_launch(void* const* d_in, const int* in_sizes, int n_in, void* d_out, int out_size, void* d_ws, size_t ws_size, hipStream_t stream) {
  const bf16* x0=(const bf16*)d_in[0]; const bf16* wqkv=(const bf16*)d_in[1]; const bf16* wo=(const bf16*)d_in[2];
  const bf16* wg=(const bf16*)d_in[4]; const bf16* wu=(const bf16*)d_in[5]; const bf16* wd=(const bf16*)d_in[6];
  char* w=(char*)d_ws; size_t off=0; auto al=[&](size_t b){void* p=w+off; off+=(b+255)&~(size_t)255; return p;};
  float* x=(float*)al((size_t)S*D*4); bf16* h=(bf16*)al((size_t)S*D*2); float* qkv=(float*)al((size_t)S*3*D*4);
  float* kv=(float*)al(H*DH*DH*4); float* z=(float*)al(H*DH*4); bf16* o=(bf16*)al((size_t)S*D*2); float* y=(float*)al((size_t)S*D*4);
  float* g=(float*)al((size_t)S*F*4); float* u=(float*)al((size_t)S*F*4); bf16* hh=(bf16*)al((size_t)S*F*2);
  int n=S*D;
  k_cvt<<<n/256,256,0,stream>>>(x0,x,n);
  for(int l=0;l<2;++l){
    k_ln<<<S,64,0,stream>>>(x,h);
    k_gemm<<<dim3(3*D/32,S/32),256,0,stream>>>(h,wqkv,qkv,S,3*D,D);
    k_kv<<<H,384,0,stream>>>(qkv,kv,z);
    k_o<<<S,576,0,stream>>>(qkv,kv,z,o);
    k_gemm<<<dim3(D/32,S/32),256,0,stream>>>(o,wo,y,S,D,D);
    k_add<<<n/256,256,0,stream>>>(x,y,n);
    k_ln<<<S,64,0,stream>>>(x,h);
    k_gemm<<<dim3(F/32,S/32),256,0,stream>>>(h,wg,g,S,F,D);
    k_gemm<<<dim3(F/32,S/32),256,0,stream>>>(h,wu,u,S,F,D);
    k_glu<<<S*F/256,256,0,stream>>>(g,u,hh,S*F);
    k_gemm<<<dim3(D/32,S/32),256,0,stream>>>(hh,wd,y,S,D,F);
    k_add<<<n/256,256,0,stream>>>(x,y,n);
  }
  k_out<<<n/256,256,0,stream>>>(x,(bf16*)d_out,n);
}
```

```cpp
#include <hip/hip_runtime.h>
#include <hip/hip_bf16.h>
#include <stdint.h>

typedef __attribute__((ext_vector_type(8))) __bf16 bf16x8;
typedef __attribute__((ext_vector_type(2))) __bf16 bf16x2;
typedef __attribute__((ext_vector_type(2))) float f32x2;
typedef __attribute__((ext_vector_type(4))) float f32x4;
typedef __attribute__((ext_vector_type(16))) float f32x16;
typedef __attribute__((ext_vector_type(4))) unsigned u32x4;
typedef __attribute__((ext_vector_type(2))) unsigned u32x2;
#define DI __device__ __forceinline__
#define LDS_AS __attribute__((address_space(3)))

constexpr int S = 2048, D = 576, NH = 32, DH = 18, F = 4608;

constexpr size_t SZ_XF = (size_t)S * D * 2;
constexpr size_t OFF_XF0 = 0;
constexpr size_t OFF_XF1 = OFF_XF0 + SZ_XF;
constexpr size_t OFF_XF2 = OFF_XF1 + SZ_XF;
constexpr size_t OFF_ST0 = OFF_XF2 + SZ_XF;
constexpr size_t OFF_ST1 = OFF_ST0 + (size_t)S * 64;
constexpr size_t OFF_ST2 = OFF_ST1 + (size_t)S * 32;
constexpr size_t OFF_PQF = OFF_ST2 + (size_t)S * 64;
constexpr size_t OFF_PQL = OFF_PQF + (size_t)64 * 32 * 1024;
constexpr size_t KV_DBL = (size_t)NH * 19 * 18;
constexpr size_t OFF_KV = OFF_PQL + (size_t)64 * 4 * 1024;
constexpr size_t SZ_KV = 176128;
constexpr size_t OFF_WQKV = OFF_KV + SZ_KV;
constexpr size_t OFF_WO = OFF_WQKV + (size_t)16 * 144 * 1024;
constexpr size_t OFF_WGU = OFF_WO + (size_t)36 * 18 * 1024;
constexpr size_t OFF_WD = OFF_WGU + (size_t)32 * 324 * 1024 + 16384;
constexpr size_t OFF_HF = OFF_WD + (size_t)36 * 144 * 1024;
constexpr size_t WS_END = OFF_HF + (size_t)S * F * 2;

DI unsigned pack2(float lo, float hi) {
  f32x2 f = {lo, hi};
  bf16x2 v = __builtin_convertvector(f, bf16x2);
  return __builtin_bit_cast(unsigned, v);
}
DI float asf(unsigned u) { return __builtin_bit_cast(float, u); }
DI float bflo(unsigned u) { return __builtin_bit_cast(float, u << 16); }
DI float bfhi(unsigned u) { return __builtin_bit_cast(float, u & 0xffff0000u); }
DI float rbf(float x) { return bflo(pack2(x, 0.f) & 0xffffu); }
DI bf16x8 asbf(u32x4 v) { return __builtin_bit_cast(bf16x8, v); }
DI f32x16 mfma32(u32x4 a, u32x4 b, f32x16 c) { return __builtin_amdgcn_mfma_f32_32x32x16_bf16(asbf(a), asbf(b), c, 0, 0, 0); }
DI f32x4 mfma16(u32x4 a, u32x4 b, f32x4 c) { return __builtin_amdgcn_mfma_f32_16x16x32_bf16(asbf(a), asbf(b), c, 0, 0, 0); }
DI float phi(float t) { return (t > 0.f ? t + 1.f : __expf(t)) + 1e-6f; }
DI unsigned lds_addr(const void* p) { return (unsigned)(size_t)(LDS_AS const char*)p; }

DI void glds16(const void* sbase, unsigned voff, unsigned dst) {
  unsigned keep;
  asm volatile("s_mov_b32 %0, m0\n\ts_mov_b32 m0, %3\n\ts_nop 0\n\tglobal_load_lds_dwordx4 %1, %2\n\ts_mov_b32 m0, %0"
               : "=&s"(keep) : "v"(voff), "s"(sbase), "s"(dst) : "memory");
}
template <int IMM> DI void gload16(u32x4& dst, const void* sbase, unsigned voff) {
  asm volatile("global_load_dwordx4 %0, %1, %2 offset:%3" : "=v"(dst) : "v"(voff), "s"(sbase), "n"(IMM));
}
template <int N> DI void waitv() { asm volatile("s_waitcnt vmcnt(%0)" ::"n"(N) : "memory"); }
template <int N> DI void waitv_pin(u32x4& x) { asm volatile("s_waitcnt vmcnt(%1)" : "+v"(x) : "n"(N)); }
DI void pin(u32x4& x) { asm volatile("" : "+v"(x)); }
DI void bar() { asm volatile("" ::: "memory"); __builtin_amdgcn_s_barrier(); asm volatile("" ::: "memory"); }

DI void norm_frag(u32x4& x, float rstd, float nmr  ) {
#pragma unroll
  for (int i = 0; i < 4; ++i) {
    float lo = __builtin_fmaf(bflo(x[i]), rstd, nmr), hi = __builtin_fmaf(bfhi(x[i]), rstd, nmr);
    x[i] = pack2(lo, hi);
  }
}

__host__ __device__ constexpr int dg_start(int g) { return g < 4 ? 5 * g : 20 + 4 * (g - 4); }
__host__ __device__ constexpr int dg_count(int g) { return g < 4 ? 5 : 4; }

constexpr int PB_WGU = 648, PB_WD = 324, PB_WO = 41, PB_WQKV = 576, PB_X = 512, PB_Z = 43;
constexpr int PB_TOTAL = PB_WGU + PB_WD + PB_WO + PB_WQKV + PB_X + PB_Z;

DI void tr4x8_store(const u32x2 (&in)[8], char* dst) {
  u32x4 o0, o1, o2, o3;
#pragma unroll
  for (int t = 0; t < 4; ++t) {
    unsigned a0 = in[2 * t][0], a1 = in[2 * t + 1][0], b0 = in[2 * t][1], b1 = in[2 * t + 1][1];
    o0[t] = (a0 & 0xffffu) | (a1 << 16);
    o1[t] = (a0 >> 16) | (a1 & 0xffff0000u);
    o2[t] = (b0 & 0xffffu) | (b1 << 16);
    o3[t] = (b0 >> 16) | (b1 & 0xffff0000u);
  }
  u32x4* d = (u32x4*)dst;
  d[0] = o0; d[1] = o1; d[2] = o2; d[3] = o3;
}

__global__ __launch_bounds__(256) void k_prep(const unsigned short* __restrict__ x, const unsigned short* __restrict__ wqkv,
                                              const unsigned short* __restrict__ wo, const unsigned short* __restrict__ wg,
                                              const unsigned short* __restrict__ wu, const unsigned short* __restrict__ wd,
                                              char* __restrict__ ws) {
  int b = blockIdx.x;
  const int tid = threadIdx.x;
  if (b < PB_WGU) {
    int t = b * 256 + tid;
    int nq = t % 1152; int r = t / 1152; int h = r & 1; r >>= 1; int ks = r % 36; int m = r / 36;
    const unsigned short* w = m ? wu : wg;
    u32x2 in[8];
#pragma unroll
    for (int j = 0; j < 8; ++j) in[j] = *(const u32x2*)(w + (size_t)(16 * ks + 8 * h + j) * F + 4 * nq);
    int n0 = 4 * nq, fs = n0 / 144, rem = n0 % 144, blk = rem >> 4, qd = (rem & 15) >> 2;
    int rq = (qd >> 1) | ((qd & 1) << 1) | (m << 2);
    int idx = blk < 3 ? 3 * ks + blk : 108 + 36 * (blk - 3) + ks;
    tr4x8_store(in, ws + OFF_WGU + ((size_t)fs * 324 + idx) * 1024 + (h * 32 + 4 * rq) * 16);
    return;
  }
  b -= PB_WGU;
  if (b < PB_WD) {
    int t = b * 256 + tid;
    int nq = t % 144; int r = t / 144; int q = r & 3; int s = r >> 2;
    u32x2 in[8];
#pragma unroll
    for (int j = 0; j < 8; ++j) in[j] = *(const u32x2*)(wd + (size_t)(32 * s + 8 * q + j) * D + 4 * nq);
    int c = nq >> 2;
    int g = c < 20 ? c / 5 : 4 + (c - 20) / 4;
    int st = dg_start(g), n = dg_count(g);
    size_t frag = (size_t)st * 144 + (size_t)s * n + (c - st);
    tr4x8_store(in, ws + OFF_WD + frag * 1024 + (q * 16 + 4 * (nq & 3)) * 16);
    return;
  }
  b -= PB_WD;
  if (b < PB_WO) {
    int t = b * 256 + tid;
    if (t >= 18 * 4 * 144) return;
    int nq = t % 144; int r = t / 144; int q = r & 3; int s = r >> 2;
    u32x2 in[8];
#pragma unroll
    for (int j = 0; j < 8; ++j) {
      int row;
      if (s < 16) row = (2 * s + (q >> 1)) * 18 + 8 * (q & 1) + j;
      else { int w = 4 * (s - 16) + 2 * (j >> 2) + (q >> 1); row = (4 * w + (j & 3)) * 18 + 16 + (q & 1); }
      in[j] = *(const u32x2*)(wo + (size_t)row * D + 4 * nq);
    }
    tr4x8_store(in, ws + OFF_WO + ((size_t)(nq >> 2) * 18 + s) * 1024 + (q * 16 + 4 * (nq & 3)) * 16);
    return;
  }
  b -= PB_WO;
  if (b < PB_WQKV) {
    int t = b * 256 + tid;
    int lane = t & 63; int r = t >> 6; int blk = r & 3; r >>= 2; int ks = r % 36; int p = r / 36;
    int h = lane >> 5, rho = lane & 31;
    int col = -1;
    if (blk == 0) col = 576 + 36 * p + rho;
    else if (blk == 1) {
      if (rho < 18) col = 1152 + 36 * p + rho;
      else if (rho >= 19 && rho <= 22) col = 36 * p + 18 * ((rho - 19) >> 1) + 16 + ((rho - 19) & 1);
    } else if (blk == 2) {
      if (rho < 4) col = 576 + 36 * p + 32 + rho;
      else if (rho < 22) col = 1152 + 36 * p + 18 + (rho - 4);
    } else {
      col = 36 * p + 18 * (rho >> 4) + 8 * ((rho >> 2) & 1) + 4 * ((rho >> 3) & 1) + (rho & 3);
    }
    u32x4 o = {0, 0, 0, 0};
    if (col >= 0) {
      const unsigned short* src = wqkv + (size_t)(16 * ks + 8 * h) * (3 * D) + col;
#pragma unroll
      for (int i = 0; i < 4; ++i) o[i] = (unsigned)src[(2 * i) * (3 * D)] | ((unsigned)src[(2 * i + 1) * (3 * D)] << 16);
    }
    *(u32x4*)(ws + OFF_WQKV + ((size_t)(p * 36 + ks) * 4 + blk) * 1024 + lane * 16) = o;
    return;
  }
  b -= PB_WQKV;
  if (b < PB_X) {
    int token = b * 4 + (tid >> 6), lane = tid & 63;
    int ks = lane < 36 ? lane : 35;
    const u32x4* src = (const u32x4*)(x + (size_t)token * D + 16 * ks);
    u32x4 v0 = src[0], v1 = src[1];
    int tb = token >> 5, r = token & 31;
    if (lane < 36) {
      char* dst = ws + OFF_XF0 + ((size_t)(tb * 36 + ks) * 64 + r) * 16;
      *(u32x4*)dst = v0; *(u32x4*)(dst + 512) = v1;
    }
    float s = 0.f;
#pragma unroll
    for (int i = 0; i < 4; ++i) s += bflo(v0[i]) + bfhi(v0[i]) + bflo(v1[i]) + bfhi(v1[i]);
    float m16 = s * (1.f / 16.f), q = 0.f;
#pragma unroll
    for (int i = 0; i < 4; ++i) {
      float a = bflo(v0[i]) - m16, c = bfhi(v0[i]) - m16, d = bflo(v1[i]) - m16, e = bfhi(v1[i]) - m16;
      q += a * a + c * c + d * d + e * e;
    }
    int g = lane & 7, st = dg_start(g), n = dg_count(g);
    float ms = 0.f, mm[5], qq = 0.f;
#pragma unroll
    for (int i = 0; i < 5; ++i) {
      int sl = st + (i < n ? i : 0);
      mm[i] = __shfl(m16, sl); float qi = __shfl(q, sl);
      if (i < n) { ms += mm[i]; qq += qi; }
    }
    float mean = ms / n;
#pragma unroll
    for (int i = 0; i < 5; ++i) if (i < n) { float d = mm[i] - mean; qq += 16.f * d * d; }
    if (lane < 8) *(f32x2*)(ws + OFF_ST0 + ((size_t)token * 8 + lane) * 8) = f32x2{mean, qq};
    return;
  }
  b -= PB_X;
  {
    size_t o = ((size_t)b * 256 + tid) * 16;
    if (o < SZ_KV) *(u32x4*)(ws + OFF_KV + o) = u32x4{0, 0, 0, 0};
  }
}

__global__ __launch_bounds__(256, 1) void k_attn_a(const char* __restrict__ xf, const char* __restrict__ st, const char* __restrict__ wqkvf,
                                                   char* __restrict__ pqf, char* __restrict__ pql, double* __restrict__ kv) {
  __shared__ __attribute__((aligned(16))) char smem[147456];
  const int tid = threadIdx.x, lane = tid & 63;
  const int w = __builtin_amdgcn_readfirstlane(tid >> 6);
  const int b = blockIdx.x, xcd = b & 7, idx = b >> 3;
  const int tt = 2 * xcd + (idx & 1), p = idx >> 1;
  const int tb = 4 * tt + w;
  const unsigned sb = lds_addr(smem);
  const unsigned l16 = lane * 16;
  const int h = lane >> 5, r = lane & 31;

  u32x4 sv[4], X[36];
  {
    const char* sp = st + (size_t)(tb * 32) * 64;
    unsigned so = r * 64;
    gload16<0>(sv[0], sp, so); gload16<16>(sv[1], sp, so); gload16<32>(sv[2], sp, so); gload16<48>(sv[3], sp, so);
  }
  const char* xs = xf + (size_t)tb * 36 * 1024;
  const char* wsrc = wqkvf + (size_t)p * 144 * 1024 + w * 1024;
#pragma unroll
  for (int ks = 0; ks < 36; ++ks) {
    glds16(wsrc + ks * 4096, l16, sb + (4 * ks) * 1024 + w * 1024);
    gload16<0>(X[ks], xs + ks * 1024, l16);
  }
  float rstd, nmr;
  {
    asm volatile("s_waitcnt vmcnt(59)" : "+v"(sv[0]), "+v"(sv[1]), "+v"(sv[2]), "+v"(sv[3]));
    float m[8], q[8];
#pragma unroll
    for (int i = 0; i < 4; ++i) {
      m[2 * i] = asf(sv[i][0]); q[2 * i] = asf(sv[i][1]);
      m[2 * i + 1] = asf(sv[i][2]); q[2 * i + 1] = asf(sv[i][3]);
    }
    float mean = (80.f * (m[0] + m[1] + m[2] + m[3]) + 64.f * (m[4] + m[5] + m[6] + m[7])) * (1.f / 576.f);
    float M2 = 0.f;
#pragma unroll
    for (int i = 0; i < 8; ++i) { float d = m[i] - mean; M2 += q[i] + (i < 4 ? 80.f : 64.f) * d * d; }
    rstd = rsqrtf(M2 * (1.f / 576.f) + 1e-5f);
    nmr = -mean * rstd;
  }
  f32x16 acc[4];
#pragma unroll
  for (int i = 0; i < 4; ++i)
#pragma unroll
    for (int j = 0; j < 16; ++j) acc[i][j] = 0.f;

  const char* rd = smem + l16;
  u32x4 A[8];
#define KA_WAIT(g) do { constexpr int n_ = 56 - 8 * (g) > 0 ? 56 - 8 * (g) : 0; waitv<n_>(); bar(); } while (0)
  KA_WAIT(0);
#pragma unroll
  for (int i = 0; i < 8; ++i) A[i] = *(const u32x4*)(rd + i * 1024);
  waitv_pin<56>(X[0]);
  norm_frag(X[0], rstd, nmr);
#pragma unroll
  for (int ks = 0; ks < 36; ++ks) {
    if ((ks & 3) == 0 && ks > 0) {
      switch (ks >> 2) {
        case 1: KA_WAIT(1); break; case 2: KA_WAIT(2); break; case 3: KA_WAIT(3); break; case 4: KA_WAIT(4); break;
        case 5: KA_WAIT(5); break; case 6: KA_WAIT(6); break; case 7: KA_WAIT(7); break; default: break;
      }
    }
    if (ks + 1 < 36) { pin(X[ks + 1]); norm_frag(X[ks + 1], rstd, nmr); }
#pragma unroll
    for (int blk = 0; blk < 4; ++blk) {
      const int i = 4 * ks + blk;
      u32x4 a = A[i & 7];
      if (blk < 3) acc[blk] = mfma32(X[ks], a, acc[blk]);
      else acc[3] = mfma32(a, X[ks], acc[3]);
      if (i + 8 < 144) A[i & 7] = *(const u32x4*)(rd + (i + 8) * 1024);
    }
  }
  bar();

  const int c = r;
  u32x4 k1[2], v1[2], kv2[2];
  float* ql = (float*)(smem + 65536) + w * 128;
#pragma unroll
  for (int s = 0; s < 2; ++s)
#pragma unroll
    for (int t = 0; t < 4; ++t) {
      const int r0 = 8 * s + 2 * t;
      k1[s][t] = pack2(phi(acc[0][r0]), phi(acc[0][r0 + 1]));
      float a0 = acc[1][r0], a1 = acc[1][r0 + 1];
      if (c >= 19 && c <= 22) {
        ql[(c - 19) * 32 + ((r0 & 3) + 8 * (r0 >> 2) + 4 * h)] = phi(a0);
        ql[(c - 19) * 32 + (((r0 + 1) & 3) + 8 * ((r0 + 1) >> 2) + 4 * h)] = phi(a1);
      }
      if (c == 18) { a0 = 1.f; a1 = 1.f; }
      v1[s][t] = pack2(a0, a1);
      float b0 = acc[2][r0], b1 = acc[2][r0 + 1];
      if (c < 4) { b0 = phi(b0); b1 = phi(b1); }
      if (c == 22) { b0 = 1.f; b1 = 1.f; }
      kv2[s][t] = pack2(b0, b1);
    }
  f32x16 R1, R2, R3;
#pragma unroll
  for (int j = 0; j < 16; ++j) { R1[j] = 0.f; R2[j] = 0.f; R3[j] = 0.f; }
#pragma unroll
  for (int s = 0; s < 2; ++s) {
    R1 = mfma32(k1[s], v1[s], R1);
    R2 = mfma32(k1[s], kv2[s], R2);
    R3 = mfma32(kv2[s], kv2[s], R3);
  }
  {
    u32x4 qa, qb;
#pragma unroll
    for (int t = 0; t < 4; ++t) {
      qa[t] = pack2(phi(acc[3][2 * t]), phi(acc[3][2 * t + 1]));
      qb[t] = pack2(phi(acc[3][8 + 2 * t]), phi(acc[3][8 + 2 * t + 1]));
    }
    char* dst = pqf + ((size_t)tb * 32 + 2 * p) * 1024 + l16;
    *(u32x4*)dst = qa; *(u32x4*)(dst + 1024) = qb;
    float la = ql[h * 32 + r], lb = ql[(2 + h) * 32 + r];
    *(unsigned*)(pql + ((size_t)tb * 4 + (p >> 2)) * 1024 + l16 + 4 * (p & 3)) = pack2(la, lb);
  }
  float* red = (float*)smem + w * 1280;
#pragma unroll
  for (int j = 0; j < 16; ++j) {
    int row = (j & 3) + 8 * (j >> 2) + 4 * h;
    red[row * 33 + c] = row < 18 ? R1[j] : R2[j];
  }
  if (h == 0) {
#pragma unroll
    for (int j = 0; j < 4; ++j) red[(32 + j) * 33 + c] = R3[j];
  }
  __syncthreads();
  for (int it = tid; it < 684; it += 256) {
    int hs = it >= 342, rem = it - 342 * hs, e = rem / 18, d = rem - 18 * e;
    int o = hs == 0 ? d * 33 + e : (d < 14 ? (18 + d) * 33 + 4 + e : (32 + d - 14) * 33 + 4 + e);
    const float* rp = (const float*)smem + o;
    float sum = (rp[0] + rp[1280]) + (rp[2560] + rp[3840]);
    unsafeAtomicAdd(kv + (size_t)(2 * p) * 342 + it, (double)sum);
  }
}

__global__ __launch_bounds__(576) void k_attn_b(const char* __restrict__ pqf, const char* __restrict__ pql, const double* __restrict__ kv,
                                                const char* __restrict__ wof, const char* __restrict__ xin, char* __restrict__ xout,
                                                char* __restrict__ stout) {
  __shared__ __attribute__((aligned(16))) char smem[36 * 1024 + 9 * 32 * 8];
  const int tid = threadIdx.x, lane = tid & 63;
  const int w = __builtin_amdgcn_readfirstlane(tid >> 6);
  const int b = blockIdx.x, xcd = b & 7, idx = b >> 3;
  const int tb = 8 * xcd + (idx & 7), cg = idx >> 3;
  const int ct = 9 * cg + w;
  const int h = lane >> 5, r = lane & 31, q = lane >> 4, t = lane & 15;

  u32x4 A[18];
  const char* wp = wof + (size_t)ct * 18 * 1024 + lane * 16;
  u32x2 res[2];
  const size_t xo = ((size_t)(tb * 36 + ct) * 64 + (q >> 1) * 32 + t) * 16 + (q & 1) * 8;

  if (w < 8) {
    int e = -1;
    if (r < 16) e = 8 * ((r >> 2) & 1) + 4 * (r >> 3) + (r & 3);
    else if (r == 16) e = 16; else if (r == 20) e = 17; else if (r == 17 || r == 21) e = 18;
    const bool valid = e >= 0;
    const int ee = valid ? e : 0;
    f32x4 kd[4][4];
    double kl[4];
    u32x4 bq[4];
#pragma unroll
    for (int i = 0; i < 4; ++i) {
      const double* src = kv + ((size_t)(4 * w + i) * 19 + ee) * 18;
#pragma unroll
      for (int k = 0; k < 4; ++k) kd[i][k] = *(const f32x4*)(src + 8 * h + 2 * k);
      kl[i] = src[16 + h];
      bq[i] = *(const u32x4*)(pqf + ((size_t)tb * 32 + 4 * w + i) * 1024 + lane * 16);
    }
    u32x4 bl = *(const u32x4*)(pql + ((size_t)tb * 4 + (w >> 1)) * 1024 + lane * 16);
#pragma unroll
    for (int s = 0; s < 9; ++s) A[s] = *(const u32x4*)(wp + s * 1024);
    __builtin_amdgcn_sched_barrier(0);
    u32x4 a1v[4]; unsigned lvv[4];
#pragma unroll
    for (int i = 0; i < 4; ++i) {
#pragma unroll
      for (int k = 0; k < 4; ++k) {
        double d0 = __builtin_bit_cast(double, f32x2{kd[i][k][0], kd[i][k][1]});
        double d1 = __builtin_bit_cast(double, f32x2{kd[i][k][2], kd[i][k][3]});
        a1v[i][k] = valid ? pack2((float)d0, (float)d1) : 0u;
      }
      lvv[i] = valid ? ((i & 1) ? pack2(0.f, (float)kl[i]) : pack2((float)kl[i], 0.f)) : 0u;
    }
    __builtin_amdgcn_sched_barrier(0);
#pragma unroll
    for (int s = 9; s < 18; ++s) A[s] = *(const u32x4*)(wp + s * 1024);
    res[0] = *(const u32x2*)(xin + xo); res[1] = *(const u32x2*)(xin + xo + 256);
    __builtin_amdgcn_sched_barrier(0);
    float lo[4];
#pragma unroll
    for (int i = 0; i < 4; ++i) {
      u32x4 a1 = a1v[i], a2;
      int dw = 2 * (w & 1) + (i >> 1);
#pragma unroll
      for (int k = 0; k < 4; ++k) a2[k] = (k == dw) ? lvv[i] : 0u;
      f32x16 n;
#pragma unroll
      for (int j = 0; j < 16; ++j) n[j] = 0.f;
      n = mfma32(a1, bq[i], n);
      n = mfma32(a2, bl, n);
      float rd = __builtin_amdgcn_rcpf(n[9] + 1e-6f);
      u32x4 o;
#pragma unroll
      for (int k = 0; k < 4; ++k) o[k] = pack2(n[2 * k] * rd, n[2 * k + 1] * rd);
      lo[i] = n[8] * rd;
      const int head = 4 * w + i, s = head >> 1, qq = 2 * (head & 1) + h;
      *(u32x4*)(smem + (((r >> 4) * 18 + s) * 64 + qq * 16 + (r & 15)) * 16) = o;
    }
    {
      const int s = 16 + (w >> 2), qq = 2 * (w & 1) + h, half = (w >> 1) & 1;
      *(u32x2*)(smem + (((r >> 4) * 18 + s) * 64 + qq * 16 + (r & 15)) * 16 + half * 8) = u32x2{pack2(lo[0], lo[1]), pack2(lo[2], lo[3])};
    }
  } else {
#pragma unroll
    for (int s = 0; s < 18; ++s) A[s] = *(const u32x4*)(wp + s * 1024);
    res[0] = *(const u32x2*)(xin + xo); res[1] = *(const u32x2*)(xin + xo + 256);
  }
  __syncthreads();
  f32x4 acc[2] = {{0.f, 0.f, 0.f, 0.f}, {0.f, 0.f, 0.f, 0.f}};
#pragma unroll
  for (int s = 0; s < 18; ++s) {
    u32x4 b0 = *(const u32x4*)(smem + (s * 64 + lane) * 16);
    u32x4 b1 = *(const u32x4*)(smem + ((18 + s) * 64 + lane) * 16);
    acc[0] = mfma16(A[s], b0, acc[0]);
    acc[1] = mfma16(A[s], b1, acc[1]);
  }
  f32x2* sred = (f32x2*)(smem + 36 * 1024);
#pragma unroll
  for (int tbb = 0; tbb < 2; ++tbb) {
    float v0 = bflo(res[tbb][0]) + acc[tbb][0], v1 = bfhi(res[tbb][0]) + acc[tbb][1];
    float v2 = bflo(res[tbb][1]) + acc[tbb][2], v3 = bfhi(res[tbb][1]) + acc[tbb][3];
    u32x2 o = {pack2(v0, v1), pack2(v2, v3)};
    *(u32x2*)(xout + xo + tbb * 256) = o;
    v0 = bflo(o[0]); v1 = bfhi(o[0]); v2 = bflo(o[1]); v3 = bfhi(o[1]);
    float s = (v0 + v1) + (v2 + v3);
    s += __shfl_xor(s, 16); s += __shfl_xor(s, 32);
    float m = s * (1.f / 16.f);
    float d0 = v0 - m, d1 = v1 - m, d2 = v2 - m, d3 = v3 - m;
    float qq = d0 * d0 + d1 * d1 + d2 * d2 + d3 * d3;
    qq += __shfl_xor(qq, 16); qq += __shfl_xor(qq, 32);
    if (q == 0) sred[w * 32 + tbb * 16 + t] = f32x2{m, qq};
  }
  __syncthreads();
  if (tid < 32) {
    float mm[9], ms = 0.f, qq = 0.f;
#pragma unroll
    for (int i = 0; i < 9; ++i) { f32x2 v = sred[i * 32 + tid]; mm[i] = v[0]; ms += v[0]; qq += v[1]; }
    float mean = ms * (1.f / 9.f);
#pragma unroll
    for (int i = 0; i < 9; ++i) { float d = mm[i] - mean; qq += 16.f * d * d; }
    *(f32x2*)(stout + ((size_t)(tb * 32 + tid) * 4 + cg) * 8) = f32x2{mean, qq};
  }
}

constexpr int GU_NFRAG = 324, GU_STAGE = 16, GU_RING = 128;
__host__ __device__ constexpr int gu_wait_count(int s) {
  int need = s + 1;
  int last = s + 6 < 7 ? 7 : s + 6;
  if (last > 20) last = 20;
  if (need > 20) return 0;
  int n = 0;
  for (int k = need + 1; k <= last; ++k) n += (k == 20) ? 0 : 2;
  if (need <= 1) n += 36; else if (need <= 7) n += 36 - 6 * (need - 1);
  return n;
}
template <int R0> DI void gu_epi_elem(const f32x16& a, float& out) {
  float g = a[R0], u = a[R0 + 8];
  float t = g * g;
  float pz = __builtin_fmaf(t, -0.1029432397f, -2.3022081985f);
  float e = __builtin_amdgcn_exp2f(g * pz);
  float rr = __builtin_amdgcn_rcpf(1.f + e);
  out = g * rr * u;
}
DI void gu_epilogue(const f32x16& a, char* dst) {
  float o[8];
  gu_epi_elem<0>(a, o[0]); gu_epi_elem<1>(a, o[1]); gu_epi_elem<2>(a, o[2]); gu_epi_elem<3>(a, o[3]);
  gu_epi_elem<4>(a, o[4]); gu_epi_elem<5>(a, o[5]); gu_epi_elem<6>(a, o[6]); gu_epi_elem<7>(a, o[7]);
  u32x4 v = {pack2(o[0], o[1]), pack2(o[2], o[3]), pack2(o[4], o[5]), pack2(o[6], o[7])};
  *(u32x4*)dst = v;
}

template <int s> DI void gu_stage_sync(const char* wsrc, unsigned l16, unsigned sb, int w) {
  waitv<gu_wait_count(s)>();
  bar();
  if constexpr (s >= 1 && s + 7 <= 20) {
    constexpr int st = s + 7;
    glds16(wsrc + (st * 16) * 1024, l16, sb + ((st * 16) % GU_RING) * 1024 + w * 1024);
    if (st < 20) glds16(wsrc + (st * 16 + 8) * 1024, l16, sb + ((st * 16 + 8) % GU_RING) * 1024 + w * 1024);
  }
}
DI size_t h_off(int tb32, int fb, int h, int r) {
  return ((((size_t)(tb32 >> 1) * 144 + (fb >> 1)) * 4 + 2 * (tb32 & 1) + (r >> 4)) * 64 + (2 * (fb & 1) + h) * 16 + (r & 15)) * 16;
}

template <int I>
DI void gu_steps(const char* wsrc, unsigned l16, unsigned sb, int w, const char* rd, u32x4 (&A)[4], u32x4 (&X)[36], f32x16 (&acc)[4],
                 float rstd, float nmr, char* hb, int fb0, int tb32, int h, int r) {
  if constexpr (I % GU_STAGE == 0 && I > 0) gu_stage_sync<I / GU_STAGE>(wsrc, l16, sb, w);
  constexpr int blk = I < 108 ? I % 3 : 3 + (I - 108) / 36;
  constexpr int ks = I < 108 ? I / 3 : (I - 108) % 36;
  constexpr int slot = blk < 3 ? blk : (blk & 1 ? 3 : 0);
  if constexpr (I < 108 && I % 3 == 0 && ks + 1 < 36) {
    constexpr int kn = ks + 1;
    constexpr int s_now = I / GU_STAGE;
    constexpr int last = s_now >= 1 ? s_now + 7 : 7;
    constexpr int first_younger_stage = 2 + kn / 6;
    constexpr int n = (35 - kn) + 2 * (last - first_younger_stage + 1);
    waitv_pin<(n > 63 ? 63 : n)>(X[kn]);
    norm_frag(X[kn], rstd, nmr);
  }
  if constexpr (I >= 108 && ks == 0) {
#pragma unroll
    for (int j = 0; j < 16; ++j) acc[slot][j] = 0.f;
  }
  acc[slot] = mfma32(A[I & 3], X[ks], acc[slot]);
  if constexpr (I + 4 < GU_NFRAG) A[I & 3] = *(const u32x4*)(rd + ((I + 4) % GU_RING) * 1024);
  if constexpr (I == 108 + 2) gu_epilogue(acc[0], hb + h_off(tb32, fb0 + 0, h, r));
  if constexpr (I == 108 + 13) gu_epilogue(acc[1], hb + h_off(tb32, fb0 + 1, h, r));
  if constexpr (I == 108 + 24) gu_epilogue(acc[2], hb + h_off(tb32, fb0 + 2, h, r));
  if constexpr (I >= 144 && ks == 2) gu_epilogue(acc[(blk - 1) & 1 ? 3 : 0], hb + h_off(tb32, fb0 + blk - 1, h, r));
  if constexpr (I + 1 < GU_NFRAG) gu_steps<I + 1>(wsrc, l16, sb, w, rd, A, X, acc, rstd, nmr, hb, fb0, tb32, h, r);
}

__global__ __launch_bounds__(512, 2) void k_gu(const char* __restrict__ xf, const char* __restrict__ st, const char* __restrict__ wguf,
                                               char* __restrict__ hf) {
  __shared__ __attribute__((aligned(16))) char smem[GU_RING * 1024];
  const int tid = threadIdx.x, lane = tid & 63;
  const int w = __builtin_amdgcn_readfirstlane(tid >> 6);
  const int b = blockIdx.x, xcd = b & 7, idx = b >> 3;
  const int tt = idx & 7, fs = 4 * xcd + (idx >> 3);
  const int tb32 = 8 * tt + w;
  const unsigned sb = lds_addr(smem), l16 = lane * 16;
  const int h = lane >> 5, r = lane & 31;

  u32x4 sv[2], X[36];
  {
    const char* sp = st + (size_t)(tb32 * 32) * 32;
    gload16<0>(sv[0], sp, r * 32); gload16<16>(sv[1], sp, r * 32);
  }
  const char* wsrc = wguf + (size_t)fs * 324 * 1024 + w * 1024;
  const char* xs = xf + (size_t)tb32 * 36 * 1024;
#define GU_DMA(st_) do { glds16(wsrc + ((st_) * 16) * 1024, l16, sb + ((st_) * 16) * 1024 + w * 1024); \
                         glds16(wsrc + ((st_) * 16 + 8) * 1024, l16, sb + ((st_) * 16 + 8) * 1024 + w * 1024); } while (0)
  GU_DMA(0); GU_DMA(1);
#pragma unroll
  for (int g = 0; g < 6; ++g) {
#pragma unroll
    for (int k = 0; k < 6; ++k) gload16<0>(X[6 * g + k], xs + (6 * g + k) * 1024, l16);
    GU_DMA(2 + g);
  }
  float rstd, nmr;
  {
    asm volatile("s_waitcnt vmcnt(52)" : "+v"(sv[0]), "+v"(sv[1]));
    float m0 = asf(sv[0][0]), q0 = asf(sv[0][1]);
    float m1 = asf(sv[0][2]), q1 = asf(sv[0][3]);
    float m2 = asf(sv[1][0]), q2 = asf(sv[1][1]);
    float m3 = asf(sv[1][2]), q3 = asf(sv[1][3]);
    float mean = 0.25f * ((m0 + m1) + (m2 + m3));
    float d0 = m0 - mean, d1 = m1 - mean, d2 = m2 - mean, d3 = m3 - mean;
    float M2 = (q0 + q1) + (q2 + q3) + 144.f * (d0 * d0 + d1 * d1 + d2 * d2 + d3 * d3);
    rstd = rsqrtf(M2 * (1.f / 576.f) + 1e-5f);
    nmr = -mean * rstd;
  }
  f32x16 acc[4];
#pragma unroll
  for (int i = 0; i < 3; ++i)
#pragma unroll
    for (int j = 0; j < 16; ++j) acc[i][j] = 0.f;
  waitv<gu_wait_count(0)>();
  bar();
  const char* rd = smem + l16;
  u32x4 A[4];
#pragma unroll
  for (int i = 0; i < 4; ++i) A[i] = *(const u32x4*)(rd + i * 1024);
  waitv_pin<35 + 12>(X[0]);
  norm_frag(X[0], rstd, nmr);
  gu_steps<0>(wsrc, l16, sb, w, rd, A, X, acc, rstd, nmr, hf, 9 * fs, tb32, h, r);
  gu_epilogue(acc[0], hf + h_off(tb32, 9 * fs + 8, h, r));
}

constexpr int D_DEPTH = 6;
template <int NC, int Sx>
DI void d_load(u32x4 (&Bf)[D_DEPTH][4], u32x4 (&Af)[D_DEPTH][5], const char* hs, const char* ws_, unsigned l16) {
  constexpr int sl = Sx % D_DEPTH;
  const char* hp = hs + Sx * 4096;
  gload16<0>(Bf[sl][0], hp, l16); gload16<1024>(Bf[sl][1], hp, l16); gload16<2048>(Bf[sl][2], hp, l16); gload16<3072>(Bf[sl][3], hp, l16);
  const char* wp = ws_ + Sx * NC * 1024;
  gload16<0>(Af[sl][0], wp, l16); gload16<1024>(Af[sl][1], wp, l16); gload16<2048>(Af[sl][2], wp, l16); gload16<3072>(Af[sl][3], wp, l16);
  if constexpr (NC == 5) gload16<0>(Af[sl][4], wp + 4096, l16);
}
template <int NC, int Sx>
DI void d_steps(u32x4 (&Bf)[D_DEPTH][4], u32x4 (&Af)[D_DEPTH][5], f32x4 (&acc)[4][5], const char* hs, const char* ws_, unsigned l16) {
  constexpr int sl = Sx % D_DEPTH;
  constexpr int ahead = (36 - 1 - Sx) < (D_DEPTH - 1) ? (36 - 1 - Sx) : (D_DEPTH - 1);
  constexpr int n = ahead * (4 + NC);
  if constexpr (NC == 5)
    asm volatile("s_waitcnt vmcnt(%9)" : "+v"(Bf[sl][0]), "+v"(Bf[sl][1]), "+v"(Bf[sl][2]), "+v"(Bf[sl][3]), "+v"(Af[sl][0]), "+v"(Af[sl][1]),
                 "+v"(Af[sl][2]), "+v"(Af[sl][3]), "+v"(Af[sl][4]) : "n"(n));
  else
    asm volatile("s_waitcnt vmcnt(%8)" : "+v"(Bf[sl][0]), "+v"(Bf[sl][1]), "+v"(Bf[sl][2]), "+v"(Bf[sl][3]), "+v"(Af[sl][0]), "+v"(Af[sl][1]),
                 "+v"(Af[sl][2]), "+v"(Af[sl][3]) : "n"(n));
#pragma unroll
  for (int c = 0; c < NC; ++c)
#pragma unroll
    for (int t = 0; t < 4; ++t) acc[t][c] = mfma16(Af[sl][c], Bf[sl][t], acc[t][c]);
  if constexpr (Sx + D_DEPTH < 36) {
    __builtin_amdgcn_sched_barrier(0);
    d_load<NC, Sx + D_DEPTH>(Bf, Af, hs, ws_, l16);
  }
  if constexpr (Sx + 1 < 36) d_steps<NC, Sx + 1>(Bf, Af, acc, hs, ws_, l16);
}
constexpr int D_STRIDE = 84;
template <int NC, bool LAST>
DI void d_body(char* smem, const char* hf, const char* wdf, const char* x1, char* xout, char* stout, int tt, int g, int tid, int lane, int kq) {
  const unsigned l16 = lane * 16;
  const int st = dg_start(g);
  const char* hs = hf + ((size_t)(tt * 144 + 36 * kq) * 4) * 1024;
  const char* ws_ = wdf + ((size_t)st * 144 + (size_t)36 * kq * NC) * 1024;
  u32x4 Bf[D_DEPTH][4], Af[D_DEPTH][5];
  f32x4 acc[4][5];
#pragma unroll
  for (int t = 0; t < 4; ++t)
#pragma unroll
    for (int c = 0; c < 5; ++c) acc[t][c] = f32x4{0.f, 0.f, 0.f, 0.f};
  d_load<NC, 0>(Bf, Af, hs, ws_, l16); d_load<NC, 1>(Bf, Af, hs, ws_, l16); d_load<NC, 2>(Bf, Af, hs, ws_, l16);
  d_load<NC, 3>(Bf, Af, hs, ws_, l16); d_load<NC, 4>(Bf, Af, hs, ws_, l16); d_load<NC, 5>(Bf, Af, hs, ws_, l16);
  d_steps<NC, 0>(Bf, Af, acc, hs, ws_, l16);
  float* red = (float*)smem;
  {
    const int q = lane >> 4, t = lane & 15;
    float* rp = red + (size_t)kq * 64 * D_STRIDE + t * D_STRIDE + 4 * q;
#pragma unroll
    for (int tb = 0; tb < 4; ++tb)
#pragma unroll
      for (int c = 0; c < NC; ++c) *(f32x4*)(rp + tb * 16 * D_STRIDE + c * 16) = acc[tb][c];
  }
  __syncthreads();
  f32x2* sred = (f32x2*)(smem + 4 * 64 * D_STRIDE * 4);
  for (int it = tid; it < 64 * NC; it += 256) {
    int token, kl;
    if (LAST) { token = it / NC; kl = it - token * NC; } else { token = it & 63; kl = it >> 6; }
    const float* rp = red + token * D_STRIDE + kl * 16;
    float v[16];
#pragma unroll
    for (int i = 0; i < 4; ++i) {
      f32x4 a = *(const f32x4*)(rp + 4 * i), b = *(const f32x4*)(rp + 64 * D_STRIDE + 4 * i);
      f32x4 c = *(const f32x4*)(rp + 2 * 64 * D_STRIDE + 4 * i), d = *(const f32x4*)(rp + 3 * 64 * D_STRIDE + 4 * i);
#pragma unroll
      for (int j = 0; j < 4; ++j) v[4 * i + j] = (a[j] + b[j]) + (c[j] + d[j]);
    }
    const int gt = tt * 64 + token, ks = st + kl;
    const size_t xo = ((size_t)((gt >> 5) * 36 + ks) * 64 + (gt & 31)) * 16;
    u32x4 r0 = *(const u32x4*)(x1 + xo), r1 = *(const u32x4*)(x1 + xo + 512);
    u32x4 o0, o1;
#pragma unroll
    for (int i = 0; i < 4; ++i) {
      o0[i] = pack2(bflo(r0[i]) + v[2 * i], bfhi(r0[i]) + v[2 * i + 1]);
      o1[i] = pack2(bflo(r1[i]) + v[8 + 2 * i], bfhi(r1[i]) + v[8 + 2 * i + 1]);
    }
    if (LAST) {
      char* dst = xout + ((size_t)gt * D + 16 * ks) * 2;
      *(u32x4*)dst = o0; *(u32x4*)(dst + 16) = o1;
    } else {
      *(u32x4*)(xout + xo) = o0; *(u32x4*)(xout + xo + 512) = o1;
      float s = 0.f;
#pragma unroll
      for (int i = 0; i < 4; ++i) s += bflo(o0[i]) + bfhi(o0[i]) + bflo(o1[i]) + bfhi(o1[i]);
      float m = s * (1.f / 16.f), qq = 0.f;
#pragma unroll
      for (int i = 0; i < 4; ++i) {
        float a = bflo(o0[i]) - m, b = bfhi(o0[i]) - m, c = bflo(o1[i]) - m, d = bfhi(o1[i]) - m;
        qq += a * a + b * b + c * c + d * d;
      }
      sred[kl * 64 + token] = f32x2{m, qq};
    }
  }
  if (!LAST) {
    __syncthreads();
    if (tid < 64) {
      float mm[NC], ms = 0.f, qq = 0.f;
#pragma unroll
      for (int i = 0; i < NC; ++i) { f32x2 v = sred[i * 64 + tid]; mm[i] = v[0]; ms += v[0]; qq += v[1]; }
      float mean = ms * (1.f / NC);
#pragma unroll
      for (int i = 0; i < NC; ++i) { float d = mm[i] - mean; qq += 16.f * d * d; }
      *(f32x2*)(stout + ((size_t)(tt * 64 + tid) * 8 + g) * 8) = f32x2{mean, qq};
    }
  }
}
template <bool LAST>
__global__ __launch_bounds__(256, 1) void k_down(const char* __restrict__ hf, const char* __restrict__ wdf, const char* __restrict__ x1,
                                                 char* __restrict__ xout, char* __restrict__ stout) {
  __shared__ __attribute__((aligned(16))) char smem[4 * 64 * D_STRIDE * 4 + 5 * 64 * 8];
  const int tid = threadIdx.x, lane = tid & 63;
  const int kq = __builtin_amdgcn_readfirstlane(tid >> 6);
  const int b = blockIdx.x, xcd = b & 7, idx = b >> 3;
  const int tt = 4 * xcd + (idx & 3), g = idx >> 2;
  if (g < 4) d_body<5, LAST>(smem, hf, wdf, x1, xout, stout, tt, g, tid, lane, kq);
  else d_body<4, LAST>(smem, hf, wdf, x1, xout, stout, tt, g, tid, lane, kq);
}

#define STEP() do { } while (0)
extern "C" void kernel_launch(void* const* d_in, const int* in_sizes, int n_in, void* d_out, int out_size, void* d_ws, size_t ws_size,
                              hipStream_t stream) {
  const unsigned short* x = (const unsigned short*)d_in[0];
  const unsigned short* wqkv = (const unsigned short*)d_in[1];
  const unsigned short* wo = (const unsigned short*)d_in[2];
  const unsigned short* wg = (const unsigned short*)d_in[4];
  const unsigned short* wu = (const unsigned short*)d_in[5];
  const unsigned short* wd = (const unsigned short*)d_in[6];
  char* ws = (char*)d_ws;
  STEP();
  k_prep<<<PB_TOTAL, 256, 0, stream>>>(x, wqkv, wo, wg, wu, wd, ws);
  for (int l = 0; l < 2; ++l) {
    const char* xin = ws + (l == 0 ? OFF_XF0 : OFF_XF2);
    const char* stin = ws + (l == 0 ? OFF_ST0 : OFF_ST2);
    double* kv = (double*)(ws + OFF_KV) + (size_t)l * KV_DBL;
    STEP();
    k_attn_a<<<256, 256, 0, stream>>>(xin, stin, ws + OFF_WQKV, ws + OFF_PQF, ws + OFF_PQL, kv);
    STEP();
    k_attn_b<<<256, 576, 0, stream>>>(ws + OFF_PQF, ws + OFF_PQL, kv, ws + OFF_WO, xin, ws + OFF_XF1, ws + OFF_ST1);
    STEP();
    k_gu<<<256, 512, 0, stream>>>(ws + OFF_XF1, ws + OFF_ST1, ws + OFF_WGU, ws + OFF_HF);
    STEP();
    if (l == 0) k_down<false><<<256, 256, 0, stream>>>(ws + OFF_HF, ws + OFF_WD, ws + OFF_XF1, ws + OFF_XF2, ws + OFF_ST2);
    else k_down<true><<<256, 256, 0, stream>>>(ws + OFF_HF, ws + OFF_WD, ws + OFF_XF1, (char*)d_out, nullptr);
  }
}
```

```cpp
#include <hip/hip_runtime.h>
#include <hip/hip_bf16.h>
#include <stdint.h>

typedef __attribute__((ext_vector_type(8))) __bf16 bf16x8;
typedef __attribute__((ext_vector_type(2))) __bf16 bf16x2;
typedef __attribute__((ext_vector_type(2))) float f32x2;
typedef __attribute__((ext_vector_type(4))) float f32x4;
typedef __attribute__((ext_vector_type(16))) float f32x16;
typedef __attribute__((ext_vector_type(4))) unsigned u32x4;
typedef __attribute__((ext_vector_type(2))) unsigned u32x2;
#define DI __device__ __forceinline__
#define LDS_AS __attribute__((address_space(3)))

#define LAYARG
#define LAYVAL(l)
#define STAMP(kid, i) do { } while (0)
constexpr int S = 2048, D = 576, NH = 32, DH = 18, F = 4608;

constexpr size_t SZ_XF = (size_t)S * D * 2;
constexpr size_t OFF_XF0 = 0;
constexpr size_t OFF_XF1 = OFF_XF0 + SZ_XF;
constexpr size_t OFF_XF2 = OFF_XF1 + SZ_XF;
constexpr size_t OFF_ST0 = OFF_XF2 + SZ_XF;
constexpr size_t OFF_ST1 = OFF_ST0 + (size_t)S * 64;
constexpr size_t OFF_ST2 = OFF_ST1 + (size_t)S * 32;
constexpr size_t OFF_PQF = OFF_ST2 + (size_t)S * 64;
constexpr size_t OFF_PQL = OFF_PQF + (size_t)64 * 32 * 1024;
constexpr size_t KV_DBL = (size_t)NH * 576;
constexpr size_t OFF_KV = OFF_PQL + (size_t)64 * 4 * 1024;
constexpr size_t SZ_KV = 2 * KV_DBL * 8;
constexpr int KA_NFRAG = 148, GU_NFRAG = 333;
constexpr size_t OFF_WQKV = OFF_KV + SZ_KV;
constexpr size_t OFF_WO = OFF_WQKV + (size_t)16 * KA_NFRAG * 1024;
constexpr size_t OFF_WGU = OFF_WO + (size_t)36 * 18 * 1024;
constexpr size_t OFF_WD = OFF_WGU + (size_t)32 * GU_NFRAG * 1024 + 16384;
constexpr size_t OFF_HF = OFF_WD + (size_t)36 * 144 * 1024;
constexpr size_t WS_END = OFF_HF + (size_t)S * F * 2;

DI unsigned pack2(float lo, float hi) {
  f32x2 f = {lo, hi};
  bf16x2 v = __builtin_convertvector(f, bf16x2);
  return __builtin_bit_cast(unsigned, v);
}
DI float asf(unsigned u) { return __builtin_bit_cast(float, u); }
DI float bflo(unsigned u) { return __builtin_bit_cast(float, u << 16); }
DI float bfhi(unsigned u) { return __builtin_bit_cast(float, u & 0xffff0000u); }
DI float rbf(float x) { return bflo(pack2(x, 0.f) & 0xffffu); }
DI bf16x8 asbf(u32x4 v) { return __builtin_bit_cast(bf16x8, v); }
DI f32x16 mfma32(u32x4 a, u32x4 b, f32x16 c) { return __builtin_amdgcn_mfma_f32_32x32x16_bf16(asbf(a), asbf(b), c, 0, 0, 0); }
DI f32x4 mfma16(u32x4 a, u32x4 b, f32x4 c) { return __builtin_amdgcn_mfma_f32_16x16x32_bf16(asbf(a), asbf(b), c, 0, 0, 0); }
DI unsigned lds_addr(const void* p) { return (unsigned)(size_t)(LDS_AS const char*)p; }

DI void glds16(const void* sbase, unsigned voff, unsigned dst) {
  unsigned keep;
  asm volatile("s_mov_b32 %0, m0\n\ts_mov_b32 m0, %3\n\ts_nop 0\n\tglobal_load_lds_dwordx4 %1, %2\n\ts_mov_b32 m0, %0"
               : "=&s"(keep) : "v"(voff), "s"(sbase), "s"(dst) : "memory");
}
template <int IMM> DI void gload16(u32x4& dst, const void* sbase, unsigned voff) {
  asm volatile("global_load_dwordx4 %0, %1, %2 offset:%3" : "=v"(dst) : "v"(voff), "s"(sbase), "n"(IMM));
}
template <int G> DI void gload16_g(u32x4& dst, const void* sbase, unsigned voff, int grp) {
  asm volatile("s_cmp_lg_u32 %3, %4\n\ts_cbranch_scc1 .Lsk%=\n\tglobal_load_dwordx4 %0, %1, %2\n.Lsk%=:"
               : "+v"(dst) : "v"(voff), "s"(sbase), "s"(grp), "n"(G) : "scc");
}
template <int G> DI void glds16_g(const void* sbase, unsigned voff, unsigned dst, int grp) {
  unsigned keep;
  asm volatile("s_cmp_lg_u32 %4, %5\n\ts_cbranch_scc1 .Lsk%=\n\ts_mov_b32 %0, m0\n\ts_mov_b32 m0, %3\n\ts_nop 0\n\tglobal_load_lds_dwordx4 %1, %2\n\ts_mov_b32 m0, %0\n.Lsk%=:"
               : "=&s"(keep) : "v"(voff), "s"(sbase), "s"(dst), "s"(grp), "n"(G) : "memory", "scc");
}
template <int N> DI void waitv() { asm volatile("s_waitcnt vmcnt(%0)" ::"n"(N) : "memory"); }
template <int N> DI void waitv_pin(u32x4& x) { asm volatile("s_waitcnt vmcnt(%1)" : "+v"(x) : "n"(N)); }
DI void pin(u32x4& x) { asm volatile("" : "+v"(x)); }
DI u32x4 lds_rd(unsigned addr) { return *(const LDS_AS u32x4*)(size_t)addr; }
DI void bar() { asm volatile("" ::: "memory"); __builtin_amdgcn_s_barrier(); asm volatile("" ::: "memory"); }

__host__ __device__ constexpr int dg_start(int g) { return g < 4 ? 5 * g : 20 + 4 * (g - 4); }
__host__ __device__ constexpr int dg_count(int g) { return g < 4 ? 5 : 4; }

typedef __attribute__((ext_vector_type(4))) short s16x4;
DI u32x2 lds_tr(unsigned addr) {
  return __builtin_bit_cast(u32x2, __builtin_amdgcn_ds_read_tr16_b64_v4i16((LDS_AS s16x4*)(size_t)addr));
}
DI float dot_ones(unsigned u, float acc) {
  return __builtin_amdgcn_fdot2_f32_bf16(__builtin_bit_cast(bf16x2, u), __builtin_bit_cast(bf16x2, 0x3f803f80u), acc, false);
}
DI void put_colsum(char* chunk0, int kq, float cs) {
  unsigned p = pack2(-cs, 0.f) & 0xffffu;
  float hi = bflo(p);
  unsigned lo = pack2(-cs - hi, 0.f) & 0xffffu;
#pragma unroll
  for (int t = 0; t < 3; ++t) {
    int sl = 3 * kq + t;
    *(unsigned short*)(chunk0 + (sl >> 3) * 512 + (sl & 7) * 2) = (unsigned short)(t == 1 ? lo : p);
  }
  if (kq == 3) *(u32x2*)(chunk0 + 512 + 8) = u32x2{0u, 0u};
}
__host__ __device__ constexpr int gu_frag_index(int blk, int ks) { return blk < 3 ? 3 * ks + blk : 111 + 37 * (blk - 3) + ks; }

constexpr int PB_WGU = 576, PB_WD = 324, PB_WO = 36, PB_WQKV = 64, PB_X = 256, PB_Z = 18;
constexpr int PB_TOTAL = PB_WGU + PB_WD + PB_WO + PB_WQKV + PB_X + PB_Z;
constexpr int P_RS = 144;

__global__ __launch_bounds__(256) void k_prep(const unsigned short* __restrict__ x, const unsigned short* __restrict__ wqkv,
                                              const unsigned short* __restrict__ wo, const unsigned short* __restrict__ wg,
                                              const unsigned short* __restrict__ wu, const unsigned short* __restrict__ wd,
                                              char* __restrict__ ws) {
  __shared__ __attribute__((aligned(16))) char smem[576 * 40];
  int b = blockIdx.x;
  const int tid = threadIdx.x, lane = tid & 63, w = tid >> 6;
  const unsigned sb = lds_addr(smem);
  if (b < PB_WQKV) {
    constexpr int RS = 224;
    const int p = b >> 2, kq = b & 3;
    const int blk = w, h = lane >> 5, rho = lane & 31;
    int col = 108;
    if (blk == 0) col = 36 + rho;
    else if (blk == 1) {
      if (rho < 18) col = 72 + rho;
      else if (rho >= 19 && rho <= 22) col = 36 + 32 + (rho - 19);
      else if (rho >= 23 && rho <= 26) col = 18 * ((rho - 23) >> 1) + 16 + ((rho - 23) & 1);
    } else if (blk == 2) { if (rho < 18) col = 72 + 18 + rho; }
    else col = 18 * (rho >> 4) + 8 * ((rho >> 2) & 1) + 4 * ((rho >> 3) & 1) + (rho & 3);
    const unsigned short* src = (const unsigned short*)(smem + 8 * h * RS) + col;
    char* dst = ws + OFF_WQKV + (size_t)p * KA_NFRAG * 1024 + blk * 1024 + lane * 16;
    u32x2 v[16];
#pragma unroll
    for (int i = 0; i < 16; ++i) {
      int ch = tid + 256 * i; if (ch > 3887) ch = 3887;
      int row = ch / 27, rem = ch - 27 * row, m = rem / 9, c4 = rem - 9 * m;
      v[i] = *(const u32x2*)(wqkv + (size_t)(144 * kq + row) * (3 * D) + 576 * m + 36 * p + 4 * c4);
    }
    float cs = 0.f;
#pragma unroll
    for (int pass = 0; pass < 2; ++pass) {
      const int r0 = pass * 80, r1 = pass ? 144 : 80;
      if (pass) __syncthreads();
#pragma unroll
      for (int i = 0; i < 16; ++i) {
        int ch = tid + 256 * i;
        int row = ch / 27, rem = ch - 27 * row;
        if (ch < 3888 && row >= r0 && row < r1) *(u32x2*)(smem + (row - r0) * RS + rem * 8) = v[i];
      }
      if (tid < 80) *(u32x2*)(smem + tid * RS + 216) = u32x2{0u, 0u};
      __syncthreads();
#pragma unroll
      for (int kl = 0; kl < 5; ++kl) {
        if (pass * 5 + kl < 9) {
          u32x4 o;
#pragma unroll
          for (int i = 0; i < 4; ++i)
            o[i] = (unsigned)src[(kl * 16 + 2 * i) * (RS / 2)] | ((unsigned)src[(kl * 16 + 2 * i + 1) * (RS / 2)] << 16);
          cs = dot_ones(o[0], cs); cs = dot_ones(o[1], cs); cs = dot_ones(o[2], cs); cs = dot_ones(o[3], cs);
          *(u32x4*)(dst + (size_t)(9 * kq + pass * 5 + kl) * 4096) = o;
        }
      }
    }
    cs += __shfl_xor(cs, 32);
    if (lane < 32) put_colsum(ws + OFF_WQKV + ((size_t)p * KA_NFRAG + 144 + blk) * 1024 + lane * 16, kq, cs);
    return;
  }
  b -= PB_WQKV;
  if (b < PB_X) {
    const int ks = lane < 36 ? lane : 35;
    u32x4 v0[2], v1[2];
#pragma unroll
    for (int i = 0; i < 2; ++i) {
      const u32x4* src = (const u32x4*)(x + (size_t)(b * 8 + w * 2 + i) * D + 16 * ks);
      v0[i] = src[0]; v1[i] = src[1];
    }
#pragma unroll
    for (int i = 0; i < 2; ++i) {
      const int token = b * 8 + w * 2 + i, tb = token >> 5, r = token & 31;
      if (lane < 36) {
        char* dst = ws + OFF_XF0 + ((size_t)(tb * 36 + ks) * 64 + r) * 16;
        *(u32x4*)dst = v0[i]; *(u32x4*)(dst + 512) = v1[i];
      }
      float s = 0.f;
#pragma unroll
      for (int j = 0; j < 4; ++j) s += bflo(v0[i][j]) + bfhi(v0[i][j]) + bflo(v1[i][j]) + bfhi(v1[i][j]);
      float m16 = s * (1.f / 16.f), q = 0.f;
#pragma unroll
      for (int j = 0; j < 4; ++j) {
        float a = bflo(v0[i][j]) - m16, c = bfhi(v0[i][j]) - m16, d = bflo(v1[i][j]) - m16, e = bfhi(v1[i][j]) - m16;
        q += a * a + c * c + d * d + e * e;
      }
      int g = lane & 7, st = dg_start(g), n = dg_count(g);
      float ms = 0.f, mm[5], qq = 0.f;
#pragma unroll
      for (int j = 0; j < 5; ++j) {
        int sl = st + (j < n ? j : 0);
        mm[j] = __shfl(m16, sl); float qi = __shfl(q, sl);
        if (j < n) { ms += mm[j]; qq += qi; }
      }
      float mean = ms / n;
#pragma unroll
      for (int j = 0; j < 5; ++j) if (j < n) { float d = mm[j] - mean; qq += 16.f * d * d; }
      if (lane < 8) *(f32x2*)(ws + OFF_ST0 + ((size_t)token * 8 + lane) * 8) = f32x2{mean, qq};
    }
    return;
  }
  b -= PB_X;
  if (b < PB_WO) {
    constexpr int RS = 40;
    u32x4 v[5];
#pragma unroll
    for (int i = 0; i < 5; ++i) {
      int ch = tid + 256 * i; if (ch > 1151) ch = 1151;
      v[i] = *(const u32x4*)(wo + (size_t)(ch >> 1) * D + 16 * b + 8 * (ch & 1));
    }
#pragma unroll
    for (int i = 0; i < 5; ++i) {
      int ch = tid + 256 * i;
      if (ch < 1152) { char* d = smem + (ch >> 1) * RS + (ch & 1) * 16; *(u32x2*)d = u32x2{v[i][0], v[i][1]}; *(u32x2*)(d + 8) = u32x2{v[i][2], v[i][3]}; }
    }
    __syncthreads();
    const int q = lane >> 4, qq = (lane >> 2) & 3, pp = lane & 3;
    for (int s = w; s < 18; s += 4) {
      int r0, r1;
      if (s < 16) { r0 = (2 * s + (q >> 1)) * 18 + 8 * (q & 1) + qq; r1 = r0 + 4; }
      else { int w0 = 4 * (s - 16) + (q >> 1); r0 = (4 * w0 + qq) * 18 + 16 + (q & 1); r1 = (4 * (w0 + 2) + qq) * 18 + 16 + (q & 1); }
      u32x2 lo = lds_tr(sb + r0 * RS + pp * 8), hi = lds_tr(sb + r1 * RS + pp * 8);
      *(u32x4*)(ws + OFF_WO + ((size_t)b * 18 + s) * 1024 + lane * 16) = u32x4{lo[0], lo[1], hi[0], hi[1]};
    }
    return;
  }
  b -= PB_WO;
  if (b < PB_Z) {
#pragma unroll
    for (int i = 0; i < 4; ++i) {
      size_t o = (((size_t)b * 4 + i) * 256 + tid) * 16;
      if (o < SZ_KV) *(u32x4*)(ws + OFF_KV + o) = u32x4{0, 0, 0, 0};
    }
    return;
  }
  b -= PB_Z;
  if (b < PB_WGU) {
    const int m = b & 1, kq = (b >> 1) & 3, fq = b >> 3;
    const unsigned short* wsrc = m ? wu : wg;
    u32x4 v[5];
#pragma unroll
    for (int i = 0; i < 5; ++i) {
      int ch = tid + 256 * i; if (ch > 1151) ch = 1151;
      v[i] = *(const u32x4*)(wsrc + (size_t)(144 * kq + (ch >> 3)) * F + 64 * fq + 8 * (ch & 7));
    }
#pragma unroll
    for (int i = 0; i < 5; ++i) {
      int ch = tid + 256 * i;
      if (ch < 1152) *(u32x4*)(smem + (ch >> 3) * P_RS + (ch & 7) * 16) = v[i];
    }
    __syncthreads();
    const int fb = 4 * fq + w, fs = fb / 9, blk = fb - 9 * fs;
    const int g = lane >> 4, h = g & 1, kpar = g >> 1, qq = (lane >> 2) & 3, pp = lane & 3, i16 = lane & 15;
    const int pq = (pp >> 1) | ((pp & 1) << 1);
    const unsigned rd = sb + (16 * kpar + 8 * h + qq) * P_RS + w * 32 + pq * 8;
    char* dst = ws + OFF_WGU + (size_t)fs * GU_NFRAG * 1024 + (h * 32 + m * 16 + i16) * 16;
    float cs = 0.f;
#pragma unroll
    for (int k2 = 0; k2 < 5; ++k2) {
      const int kl = 2 * k2 + kpar;
      if (kl < 9) {
      }
      const int klc = kl < 9 ? kl : 8;
      u32x2 lo = lds_tr(rd + (2 * k2 * 16 - (kl < 9 ? 0 : 16)) * P_RS), hi = lds_tr(rd + (2 * k2 * 16 + 4 - (kl < 9 ? 0 : 16)) * P_RS);
      u32x4 o = {lo[0], lo[1], hi[0], hi[1]};
      if (kl < 9) {
        cs = dot_ones(o[0], cs); cs = dot_ones(o[1], cs); cs = dot_ones(o[2], cs); cs = dot_ones(o[3], cs);
        *(u32x4*)(dst + (size_t)gu_frag_index(blk, 9 * kq + klc) * 1024) = o;
      }
    }
    cs += __shfl_xor(cs, 16); cs += __shfl_xor(cs, 32);
    if (lane < 16) put_colsum(ws + OFF_WGU + ((size_t)fs * GU_NFRAG + gu_frag_index(blk, 36)) * 1024 + (m * 16 + lane) * 16, kq, cs);
    return;
  }
  b -= PB_WGU;
  {
    const int cgp = b % 9, sg = b / 9;
    u32x4 v[4];
#pragma unroll
    for (int i = 0; i < 4; ++i) {
      int ch = tid + 256 * i, row = ch >> 3, c8 = ch & 7;
      v[i] = *(const u32x4*)(wd + (size_t)(128 * sg + row) * D + 64 * cgp + 8 * c8);
    }
#pragma unroll
    for (int i = 0; i < 4; ++i) {
      int ch = tid + 256 * i, row = ch >> 3, c8 = ch & 7;
      *(u32x4*)(smem + row * P_RS + c8 * 16) = v[i];
    }
    __syncthreads();
    const int c = 4 * cgp + w;
    const int g = c < 20 ? c / 5 : 4 + (c - 20) / 4;
    const int st = dg_start(g), n = dg_count(g);
    const int q = lane >> 4, qq = (lane >> 2) & 3, pp = lane & 3;
    const unsigned rd = sb + (8 * q + qq) * P_RS + w * 32 + pp * 8;
#pragma unroll
    for (int sl = 0; sl < 4; ++sl) {
      u32x2 lo = lds_tr(rd + sl * 32 * P_RS), hi = lds_tr(rd + (sl * 32 + 4) * P_RS);
      size_t frag = (size_t)st * 144 + (size_t)(4 * sg + sl) * n + (c - st);
      *(u32x4*)(ws + OFF_WD + frag * 1024 + lane * 16) = u32x4{lo[0], lo[1], hi[0], hi[1]};
    }
  }
}

DI u32x4 mean_frag(float mean, int h) {
  unsigned mh = pack2(mean, 0.f) & 0xffffu;
  unsigned ml = pack2(mean - bflo(mh), 0.f) & 0xffffu;
  unsigned hh = mh | (mh << 16), lh = ml | (mh << 16), hl = mh | (ml << 16);
  return h == 0 ? u32x4{hh, lh, hl, hh} : u32x4{lh, hl, 0u, 0u};
}

DI float phi2(float a, float rs2) {
  const float t = a * rs2;
  return __builtin_fmaf(fmaxf(t, 0.f), 0.6931471805599453f, __builtin_amdgcn_exp2f(fminf(t, 0.f)) + 1e-6f);
}
constexpr int KA_PRO = 12;
constexpr int ka_kv_row(int e) { return e < 16 ? (e & 3) + 4 * (e >> 3) + 8 * ((e >> 2) & 1) : (e == 16 ? 16 : (e == 17 ? 20 : 17)); }
template <int KS>
DI void ka_steps(u32x4 (&X)[37], u32x4 (&A)[8], f32x16 (&acc)[4], const char* xs, unsigned l16, unsigned rd0, unsigned rd1, unsigned rd2) {
  if constexpr (KS % 4 == 0 && KS > 0 && KS <= 32) bar();
  if constexpr (KS < 36) {
    constexpr int issued = KS + KA_PRO < 36 ? KS + KA_PRO : 36;
    waitv_pin<issued - KS - 1>(X[KS]);
    if constexpr (KS + KA_PRO < 36) gload16<0>(X[KS + KA_PRO], xs + (KS + KA_PRO) * 1024, l16);
  }
#pragma unroll
  for (int blk = 0; blk < 4; ++blk) {
    const int i = 4 * KS + blk;
    u32x4 a = A[i & 7];
    if (blk < 3) acc[blk] = mfma32(X[KS], a, acc[blk]);
    else acc[3] = mfma32(a, X[KS], acc[3]);
    if (i + 8 < KA_NFRAG) { const int f = i + 8; A[i & 7] = lds_rd((f < 64 ? rd0 : (f < 128 ? rd1 : rd2)) + (f & 63) * 1024); }
    __builtin_amdgcn_sched_barrier(0);
  }
  if constexpr (KS + 1 < 37) ka_steps<KS + 1>(X, A, acc, xs, l16, rd0, rd1, rd2);
}
constexpr int KA_LEAD = 16;
template <int G> DI void ka_loader(const char* wsrc, unsigned l16, unsigned dst) {
  constexpr int need = 4 * G + 7 < 36 ? 4 * G + 7 : 36;
  constexpr int from = G == 0 ? 0 : (G == 1 ? 9 : (4 * (G - 1) + KA_LEAD < 37 ? 4 * (G - 1) + KA_LEAD : 37));
  constexpr int to = G == 0 ? 9 : (4 * G + KA_LEAD < 37 ? 4 * G + KA_LEAD : 37);
#pragma unroll
  for (int ks = from; ks < to; ++ks) glds16(wsrc + ks * 4096, l16, dst + (4 * ks) * 1024);
  waitv<to - 1 - need>();
  bar();
  if constexpr (G < 8) ka_loader<G + 1>(wsrc, l16, dst);
}

__global__ __launch_bounds__(512, 2) void k_attn_a(const char* __restrict__ xf, const char* __restrict__ st, const char* __restrict__ wqkvf,
                                                   char* __restrict__ pqf, char* __restrict__ pql, double* __restrict__ kv LAYARG) {
  __shared__ __attribute__((aligned(16))) char smem[KA_NFRAG * 1024];
  const int tid = threadIdx.x, lane = tid & 63;
  const int w8 = __builtin_amdgcn_readfirstlane(tid >> 6);
  const int w = w8 & 3;
  const int b = blockIdx.x, xcd = b & 7, idx = b >> 3;
  const int tt = 2 * xcd + (idx & 1), p = idx >> 1;
  const int tb = 4 * tt + w;
  const unsigned sb = lds_addr(smem);
  const unsigned l16 = lane * 16;
  const int h = lane >> 5, r = lane & 31;
  STAMP(1, 0);
  if (w8 >= 4) {
    const char* wsrc = wqkvf + (size_t)p * KA_NFRAG * 1024 + w * 1024;
    ka_loader<0>(wsrc, l16, sb + w * 1024);
    bar();
    bar();
  } else {
    u32x4 sv[4], X[37];
    const char* xs = xf + (size_t)tb * 36 * 1024;
    gload16<0>(X[0], xs, l16); gload16<0>(X[1], xs + 1024, l16);
    {
      const char* sp = st + (size_t)(tb * 32) * 64;
      unsigned so = r * 64;
      gload16<0>(sv[0], sp, so); gload16<16>(sv[1], sp, so); gload16<32>(sv[2], sp, so); gload16<48>(sv[3], sp, so);
    }
#pragma unroll
    for (int k = 2; k < KA_PRO; ++k) gload16<0>(X[k], xs + k * 1024, l16);
    STAMP(1, 1);
    float rstd;
    {
      asm volatile("s_waitcnt vmcnt(%4)" : "+v"(sv[0]), "+v"(sv[1]), "+v"(sv[2]), "+v"(sv[3]) : "n"(KA_PRO - 2));
      float m[8], q[8];
#pragma unroll
      for (int i = 0; i < 4; ++i) {
        m[2 * i] = asf(sv[i][0]); q[2 * i] = asf(sv[i][1]);
        m[2 * i + 1] = asf(sv[i][2]); q[2 * i + 1] = asf(sv[i][3]);
      }
      float mean = (80.f * (m[0] + m[1] + m[2] + m[3]) + 64.f * (m[4] + m[5] + m[6] + m[7])) * (1.f / 576.f);
      float M2 = 0.f;
#pragma unroll
      for (int i = 0; i < 8; ++i) { float d = m[i] - mean; M2 += q[i] + (i < 4 ? 80.f : 64.f) * d * d; }
      rstd = rsqrtf(M2 * (1.f / 576.f) + 1e-5f);
      X[36] = mean_frag(mean, h);
    }
    f32x16 acc[4];
#pragma unroll
    for (int i = 0; i < 4; ++i)
#pragma unroll
      for (int j = 0; j < 16; ++j) acc[i][j] = 0.f;
    unsigned rd0 = sb + l16, rd1 = rd0 + 65536, rd2 = rd0 + 131072;
    asm volatile("" : "+v"(rd0), "+v"(rd1), "+v"(rd2));
    bar();
    STAMP(1, 2);
    u32x4 A[8];
#pragma unroll
    for (int i = 0; i < 8; ++i) A[i] = lds_rd(rd0 + i * 1024);
    __builtin_amdgcn_sched_barrier(0);
    ka_steps<0>(X, A, acc, xs, l16, rd0, rd1, rd2);
    STAMP(1, 3);
    bar();
    STAMP(1, 6);

    const int c = r;
    float* rs = (float*)(smem + 65536) + w * 160;
    float* ql = rs + 32;
    if (h == 0) rs[r] = rstd;
    f32x4 rr[4];
#pragma unroll
    for (int g = 0; g < 4; ++g) rr[g] = *(const f32x4*)(rs + 8 * g + 4 * h);
    u32x4 k1[2], v1[2], v2[2];
    f32x4 pv[4];
    const bool ph = c >= 19 && c <= 26, one = c == 18;
#pragma unroll
    for (int s = 0; s < 2; ++s)
#pragma unroll
      for (int t = 0; t < 4; ++t) {
        const int r0 = 8 * s + 2 * t;
        const float s0 = rr[r0 >> 2][r0 & 3], s1 = rr[r0 >> 2][(r0 & 3) + 1];
        const float e0 = s0 * 1.4426950408889634f, e1 = s1 * 1.4426950408889634f;
        k1[s][t] = pack2(phi2(acc[0][r0], e0), phi2(acc[0][r0 + 1], e1));
        float a0 = acc[1][r0] * s0, a1 = acc[1][r0 + 1] * s1;
        float p0 = phi2(acc[1][r0], e0), p1 = phi2(acc[1][r0 + 1], e1);
        a0 = ph ? p0 : (one ? 1.f : a0); a1 = ph ? p1 : (one ? 1.f : a1);
        pv[r0 >> 2][r0 & 3] = p0; pv[r0 >> 2][(r0 & 3) + 1] = p1;
        v1[s][t] = pack2(a0, a1);
        v2[s][t] = one ? 0x3f803f80u : pack2(acc[2][r0] * s0, acc[2][r0 + 1] * s1);
      }
    if (c >= 23 && c <= 26) {
#pragma unroll
      for (int g = 0; g < 4; ++g) *(f32x4*)(ql + (c - 23) * 32 + 8 * g + 4 * h) = pv[g];
    }
    STAMP(1, 7);
    f32x16 R1, R2, R3;
#pragma unroll
    for (int j = 0; j < 16; ++j) { R1[j] = 0.f; R2[j] = 0.f; R3[j] = 0.f; }
#pragma unroll
    for (int s = 0; s < 2; ++s) {
      R1 = mfma32(k1[s], v1[s], R1);
      R2 = mfma32(k1[s], v2[s], R2);
      R3 = mfma32(v1[s], v2[s], R3);
    }
    {
      u32x4 qa, qb;
      const float rse = rstd * 1.4426950408889634f;
#pragma unroll
      for (int t = 0; t < 4; ++t) {
        qa[t] = pack2(phi2(acc[3][2 * t], rse), phi2(acc[3][2 * t + 1], rse));
        qb[t] = pack2(phi2(acc[3][8 + 2 * t], rse), phi2(acc[3][8 + 2 * t + 1], rse));
      }
      char* dst = pqf + ((size_t)tb * 32 + 2 * p) * 1024 + l16;
      *(u32x4*)dst = qa; *(u32x4*)(dst + 1024) = qb;
      float la = ql[h * 32 + r], lb = ql[(2 + h) * 32 + r];
      *(unsigned*)(pql + ((size_t)tb * 4 + (p >> 2)) * 1024 + l16 + 4 * (p & 3)) = pack2(la, lb);
    }
    float* red = (float*)smem + w * 1280;
#pragma unroll
    for (int j = 0; j < 16; ++j) {
      int row = (j & 3) + 8 * (j >> 2) + 4 * h;
      red[row * 33 + c] = row < 18 ? R1[j] : R2[j];
    }
    if (h == 0) red[32 * 33 + c] = R3[11];
    else { red[33 * 33 + c] = R3[8]; red[34 * 33 + c] = R3[9]; red[35 * 33 + c] = R3[10]; }
    STAMP(1, 4);
    asm volatile("s_waitcnt lgkmcnt(0)" ::: "memory");
    bar();
  }
  for (int it = tid; it < 1152; it += 512) {
    int hs = it >= 576, sl = it - 576 * hs;
    int row, d;
    if (sl < 512) { row = (sl >> 1) & 31; d = 8 * ((sl >> 6) & 1) + 2 * (sl >> 7) + (sl & 1); }
    else { row = sl & 31; d = 16 + ((sl >> 5) & 1); }
    int e = row < 16 ? (row & 3) + 4 * (row >> 3) + 8 * ((row >> 2) & 1) : (row == 16 ? 16 : (row == 20 ? 17 : (row == 17 ? 18 : -1)));
    if (e >= 0) {
      int o = hs == 0 ? d * 33 + e : (d < 14 ? (18 + d) * 33 + e : (32 + d - 14) * 33 + e);
      const float* rp = (const float*)smem + o;
      float sum = (rp[0] + rp[1280]) + (rp[2560] + rp[3840]);
      unsafeAtomicAdd(kv + (size_t)(2 * p) * 576 + it, (double)sum);
    }
  }
  STAMP(1, 5);
}

__global__ __launch_bounds__(576) void k_attn_b(const char* __restrict__ pqf, const char* __restrict__ pql, const double* __restrict__ kv,
                                                const char* __restrict__ wof, const char* __restrict__ xin, char* __restrict__ xout,
                                                char* __restrict__ stout LAYARG) {
  __shared__ __attribute__((aligned(16))) char smem[36 * 1024 + 9 * 32 * 8];
  const int tid = threadIdx.x, lane = tid & 63;
  const int w = __builtin_amdgcn_readfirstlane(tid >> 6);
  const int b = blockIdx.x, xcd = b & 7, idx = b >> 3;
  const int tb = 8 * xcd + (idx & 7), cg = idx >> 3;
  const int ct = 9 * cg + w;
  const int h = lane >> 5, r = lane & 31, q = lane >> 4, t = lane & 15;

  STAMP(2, 0);
  u32x4 A[18];
  const char* wp = wof + (size_t)ct * 18 * 1024 + lane * 16;
  u32x2 res[2];
  const size_t xo = ((size_t)(tb * 36 + ct) * 64 + (q >> 1) * 32 + t) * 16 + (q & 1) * 8;

  if (w < 8) {
    const bool valid = r < 18 || r == 20 || r == 21;
    const int rr = valid ? (r == 21 ? 17 : r) : 0;
    f32x4 kd[4][4];
    double kl[4];
    u32x4 bq[4];
#pragma unroll
    for (int i = 0; i < 4; ++i) {
      const double* src = kv + (size_t)(4 * w + i) * 576;
#pragma unroll
      for (int k = 0; k < 4; ++k) kd[i][k] = *(const f32x4*)(src + (k * 64 + h * 32 + rr) * 2);
      kl[i] = src[512 + h * 32 + rr];
      bq[i] = *(const u32x4*)(pqf + ((size_t)tb * 32 + 4 * w + i) * 1024 + lane * 16);
    }
    u32x4 bl = *(const u32x4*)(pql + ((size_t)tb * 4 + (w >> 1)) * 1024 + lane * 16);
#pragma unroll
    for (int s = 0; s < 9; ++s) A[s] = *(const u32x4*)(wp + s * 1024);
    __builtin_amdgcn_sched_barrier(0);
    u32x4 a1v[4]; unsigned lvv[4];
#pragma unroll
    for (int i = 0; i < 4; ++i) {
#pragma unroll
      for (int k = 0; k < 4; ++k) {
        double d0 = __builtin_bit_cast(double, f32x2{kd[i][k][0], kd[i][k][1]});
        double d1 = __builtin_bit_cast(double, f32x2{kd[i][k][2], kd[i][k][3]});
        a1v[i][k] = valid ? pack2((float)d0, (float)d1) : 0u;
      }
      lvv[i] = valid ? ((i & 1) ? pack2(0.f, (float)kl[i]) : pack2((float)kl[i], 0.f)) : 0u;
    }
    __builtin_amdgcn_sched_barrier(0);
#pragma unroll
    for (int s = 9; s < 18; ++s) A[s] = *(const u32x4*)(wp + s * 1024);
    res[0] = *(const u32x2*)(xin + xo); res[1] = *(const u32x2*)(xin + xo + 256);
    __builtin_amdgcn_sched_barrier(0);
    STAMP(2, 1);
    float lo[4];
#pragma unroll
    for (int i = 0; i < 4; ++i) {
      u32x4 a1 = a1v[i], a2;
      int dw = 2 * (w & 1) + (i >> 1);
#pragma unroll
      for (int k = 0; k < 4; ++k) a2[k] = (k == dw) ? lvv[i] : 0u;
      f32x16 n;
#pragma unroll
      for (int j = 0; j < 16; ++j) n[j] = 0.f;
      n = mfma32(a1, bq[i], n);
      n = mfma32(a2, bl, n);
      float rd = __builtin_amdgcn_rcpf(n[9] + 1e-6f);
      u32x4 o;
#pragma unroll
      for (int k = 0; k < 4; ++k) o[k] = pack2(n[2 * k] * rd, n[2 * k + 1] * rd);
      lo[i] = n[8] * rd;
      const int head = 4 * w + i, s = head >> 1, qq = 2 * (head & 1) + h;
      *(u32x4*)(smem + (((r >> 4) * 18 + s) * 64 + qq * 16 + (r & 15)) * 16) = o;
    }
    {
      const int s = 16 + (w >> 2), qq = 2 * (w & 1) + h, half = (w >> 1) & 1;
      *(u32x2*)(smem + (((r >> 4) * 18 + s) * 64 + qq * 16 + (r & 15)) * 16 + half * 8) = u32x2{pack2(lo[0], lo[1]), pack2(lo[2], lo[3])};
    }
  } else {
#pragma unroll
    for (int s = 0; s < 18; ++s) A[s] = *(const u32x4*)(wp + s * 1024);
    res[0] = *(const u32x2*)(xin + xo); res[1] = *(const u32x2*)(xin + xo + 256);
  }
  STAMP(2, 2);
  asm volatile("s_waitcnt lgkmcnt(0)" ::: "memory");
  bar();
  STAMP(2, 3);
  f32x4 acc[2] = {{0.f, 0.f, 0.f, 0.f}, {0.f, 0.f, 0.f, 0.f}};
#pragma unroll
  for (int s = 0; s < 18; ++s) {
    u32x4 b0 = *(const u32x4*)(smem + (s * 64 + lane) * 16);
    u32x4 b1 = *(const u32x4*)(smem + ((18 + s) * 64 + lane) * 16);
    acc[0] = mfma16(A[s], b0, acc[0]);
    acc[1] = mfma16(A[s], b1, acc[1]);
  }
  STAMP(2, 4);
  f32x2* sred = (f32x2*)(smem + 36 * 1024);
#pragma unroll
  for (int tbb = 0; tbb < 2; ++tbb) {
    float v0 = bflo(res[tbb][0]) + acc[tbb][0], v1 = bfhi(res[tbb][0]) + acc[tbb][1];
    float v2 = bflo(res[tbb][1]) + acc[tbb][2], v3 = bfhi(res[tbb][1]) + acc[tbb][3];
    u32x2 o = {pack2(v0, v1), pack2(v2, v3)};
    *(u32x2*)(xout + xo + tbb * 256) = o;
    v0 = bflo(o[0]); v1 = bfhi(o[0]); v2 = bflo(o[1]); v3 = bfhi(o[1]);
    float s = (v0 + v1) + (v2 + v3);
    s += __shfl_xor(s, 16); s += __shfl_xor(s, 32);
    float m = s * (1.f / 16.f);
    float d0 = v0 - m, d1 = v1 - m, d2 = v2 - m, d3 = v3 - m;
    float qq = d0 * d0 + d1 * d1 + d2 * d2 + d3 * d3;
    qq += __shfl_xor(qq, 16); qq += __shfl_xor(qq, 32);
    if (q == 0) sred[w * 32 + tbb * 16 + t] = f32x2{m, qq};
  }
  __syncthreads();
  if (tid < 32) {
    float mm[9], ms = 0.f, qq = 0.f;
#pragma unroll
    for (int i = 0; i < 9; ++i) { f32x2 v = sred[i * 32 + tid]; mm[i] = v[0]; ms += v[0]; qq += v[1]; }
    float mean = ms * (1.f / 9.f);
#pragma unroll
    for (int i = 0; i < 9; ++i) { float d = mm[i] - mean; qq += 16.f * d * d; }
    *(f32x2*)(stout + ((size_t)(tb * 32 + tid) * 4 + cg) * 8) = f32x2{mean, qq};
  }
  STAMP(2, 5);
}

#define GU_STAGE_ 32
#define GU_RING_ 128
constexpr int GU_STAGE = GU_STAGE_, GU_RING = GU_RING_, GU_P1 = 111;
constexpr int GU_PD = 5;
constexpr int GU_NSLOT = GU_RING / GU_STAGE, GU_NPIECE = (GU_NFRAG + 7) / 8;
constexpr int GU_NST = (GU_NFRAG + GU_STAGE - 1) / GU_STAGE;
struct GuSched { int n; int kind[96]; int arg[96]; int step[96]; };
#define GU_PRO_X_ 10
#define GU_PRO_P_ (GU_STAGE_ / 8 + 2)
#define GU_PACE 5
#define GU_LAG_ 1
constexpr int GU_PRO_X = GU_PRO_X_;
constexpr int GU_PRO_P = GU_PRO_P_;
constexpr int gu_pstage(int p) { return 8 * p / GU_STAGE; }
constexpr int gu_pdead(int p) { int st = gu_pstage(p); return st == 0 ? 0 : st * GU_STAGE - GU_PD; }
constexpr int gu_pearly(int p) { int st = gu_pstage(p); return st < GU_NSLOT ? 0 : (st - GU_NSLOT + 2) * GU_STAGE - GU_PD + 2; }
constexpr GuSched gu_make_sched() {
  GuSched s{};
  int n = 0;
  auto push = [&](int k, int a, int st) { s.kind[n] = k; s.arg[n] = a; s.step[n] = st; ++n; };
  push(2, 0, -1); push(2, 1, -1);
  int kx = 0, pc = 0;
  while (kx < GU_PRO_X || pc < GU_PRO_P) {
    bool takep = pc < GU_PRO_P && (kx >= GU_PRO_X || (pc < GU_STAGE / 8 ? 8 * pc <= 12 * kx : 8 * pc <= 3 * kx));
    if (takep) push(1, pc++, -1); else push(0, kx++, -1);
  }
  int m = 0, last = 0;
  while (kx < 36 || pc < GU_NPIECE) {
    int dx = kx < 36 ? 3 * kx : 100000, ds = pc < GU_NPIECE ? gu_pdead(pc) : 100000;
    bool takep = ds <= dx;
    int st = kx < 36 ? (m * GU_PACE) / 2 : last + 5;
    if (takep && st < gu_pearly(pc)) { if (kx < 36) { takep = false; } else st = gu_pearly(pc); }
    if (st < last) st = last;
    if (takep) push(1, pc++, st); else push(0, kx++, st);
    last = st; ++m;
  }
  s.n = n;
  return s;
}
constexpr GuSched GUS = gu_make_sched();
constexpr int GU_LAG = GU_LAG_;
constexpr int gu_cnt_raw(int step) { int c = 0; for (int i = 0; i < GUS.n; ++i) if (GUS.step[i] < step) ++c; return c; }
constexpr int gu_cnt_before(int step) { return gu_cnt_raw(step - GU_LAG < 0 ? 0 : step - GU_LAG); }
constexpr int gu_idx(int kind, int arg) { for (int i = 0; i < GUS.n; ++i) if (GUS.kind[i] == kind && GUS.arg[i] == arg) return i; return -1; }
constexpr int gu_clamp(int n) { return n > 63 ? 63 : (n < 0 ? 0 : n); }
constexpr int gu_wait_for(int idx, int step) { return gu_clamp(gu_cnt_before(step) - idx - 1); }
constexpr int gu_last_piece(int st) { int p = (st + 1) * GU_STAGE / 8 - 1; return gu_idx(1, p < GU_NPIECE ? p : GU_NPIECE - 1); }
constexpr bool gu_sched_ok() {
  for (int i = 0; i < GUS.n; ++i) {
    if (GUS.kind[i] == 1) { if (GUS.step[i] + GU_LAG >= gu_pdead(GUS.arg[i]) && gu_pdead(GUS.arg[i]) > 0) return false;
                            if (gu_pdead(GUS.arg[i]) == 0 && GUS.step[i] >= 0) return false;
                            if (gu_pearly(GUS.arg[i]) > 0 && GUS.step[i] < gu_pearly(GUS.arg[i]) - 2) return false; }
    if (GUS.kind[i] == 0 && GUS.step[i] >= 0 && GUS.step[i] + GU_LAG >= 3 * GUS.arg[i]) return false;
    if (i > 0 && GUS.step[i] < GUS.step[i - 1]) return false;
  }
  return true;
}
static_assert(gu_sched_ok(), "GU schedule");
static_assert(GU_PRO_P >= GU_STAGE / 8, "stage 0 must be requested in the prologue");

DI void gstore16_wt(char* sbase, unsigned voff, u32x4 v) {
  asm volatile("global_store_dwordx4 %0, %1, %2 sc1\n\ts_nop 1" ::"v"(voff), "v"(v), "s"(sbase) : "memory");
}

struct GuCtx {
  const char* wsrc; const char* xs; const char* sp; unsigned l16, sb, so; int w, grp, h; unsigned rd0, rd1, rd2; float c1, c3, cinv; char* hbase; unsigned hvoff; int fb0;
};
template <int N, int G> DI void gu_issue(const GuCtx& c, u32x4 (&X)[37], u32x4 (&sv)[2]) {
  constexpr int kind = GUS.kind[N], arg = GUS.arg[N];
  if constexpr (kind == 0) {
    if constexpr (G < 0) gload16<0>(X[arg], c.xs + arg * 1024, c.l16);
    else gload16_g<G>(X[arg], c.xs + arg * 1024, c.l16, c.grp);
  }
  if constexpr (kind == 1) {
    constexpr int piece = arg * 8;
    if constexpr (G < 0) glds16(c.wsrc + piece * 1024, c.l16, c.sb + (piece % GU_RING) * 1024 + c.w * 1024);
    else glds16_g<G>(c.wsrc + piece * 1024, c.l16, c.sb + (piece % GU_RING) * 1024 + c.w * 1024, c.grp);
  }
  if constexpr (kind == 2) gload16<16 * arg>(sv[arg], c.sp, c.so);
}
template <int N, int STEP, int G> DI void gu_issue_step(const GuCtx& c, u32x4 (&X)[37], u32x4 (&sv)[2]) {
  if constexpr (N < GUS.n) {
    if constexpr (GUS.step[N] == STEP) gu_issue<N, G>(c, X, sv);
    if constexpr (GUS.step[N] <= STEP) gu_issue_step<N + 1, STEP, G>(c, X, sv);
  }
}
constexpr int gu_epi_start(int j) { int a = GU_P1 + 2 + 24 * j, e = (j < 3 ? GU_P1 : GU_P1 + 37 * (j - 2)) + 2; return a > e ? a : e; }
constexpr int gu_epi_block(int I) { for (int j = 0; j < 8; ++j) if (I >= gu_epi_start(j) && I < gu_epi_start(j) + 24) return j; return -1; }
struct GuEpi { float x, ab; unsigned pk[4]; float olo; };
template <int E> DI void gu_epi_0(const f32x16& a, const GuCtx& c, GuEpi& e) {
  float g = a[E];
  e.x = g * __builtin_fmaf(g * g, c.c3, c.c1);
}
template <int E> DI void gu_epi_1(const f32x16& a, const GuCtx& c, GuEpi& e) {
  float ex = __builtin_amdgcn_exp2f(e.x);
  e.ab = a[E] * a[E + 8];
  e.x = __builtin_fmaf(ex, c.cinv, c.cinv);
}
template <int E> DI void gu_epi_2(const GuCtx& c, GuEpi& e, int fb) {
  float o = e.ab * __builtin_amdgcn_rcpf(e.x);
  if constexpr ((E & 1) == 0) e.olo = o; else e.pk[E >> 1] = pack2(e.olo, o);
  if constexpr (E == 7) gstore16_wt(c.hbase + (size_t)(fb >> 1) * 4096 + (fb & 1) * 512, c.hvoff, u32x4{e.pk[0], e.pk[1], e.pk[2], e.pk[3]});
}
template <int I>
DI void gu_steps(GuCtx& c, u32x4 (&A)[GU_PD], u32x4 (&X)[37], u32x4 (&sv)[2], f32x16 (&acc)[4], GuEpi& ep) {
  if constexpr ((I + GU_PD) % GU_STAGE == 0 && (I + GU_PD) / GU_STAGE < GU_NST) {
    constexpr int s = (I + GU_PD) / GU_STAGE;
    waitv<gu_wait_for(gu_last_piece(s), I)>();
    bar();
  }
  constexpr int blk = I < GU_P1 ? I % 3 : 3 + (I - GU_P1) / 37;
  constexpr int ks = I < GU_P1 ? I / 3 : (I - GU_P1) % 37;
  constexpr int slot = blk & 3;
  if constexpr (I < 108 && I % 3 == 0 && ks >= 2) waitv_pin<gu_wait_for(gu_idx(0, ks), I)>(X[ks]);
  gu_issue_step<gu_cnt_raw(I), I, 0>(c, X, sv);
  if constexpr (I >= GU_LAG) gu_issue_step<gu_cnt_raw(I - GU_LAG), I - GU_LAG, 1>(c, X, sv);
  if constexpr (I >= GU_P1 && ks == 0) {
    f32x16 z;
#pragma unroll
    for (int j = 0; j < 16; ++j) z[j] = 0.f;
    acc[slot] = mfma32(A[I % GU_PD], X[ks], z);
  } else {
    acc[slot] = mfma32(A[I % GU_PD], X[ks], acc[slot]);
  }
  if constexpr (I + GU_PD < GU_NFRAG) {
    constexpr int fr = (I + GU_PD) % GU_RING;
    A[I % GU_PD] = lds_rd((fr < 64 ? c.rd0 : (fr < 128 ? c.rd1 : c.rd2)) + (fr & 63) * 1024);
  }
  __builtin_amdgcn_sched_barrier(0);
  {
    constexpr int j = gu_epi_block(I);
    if constexpr (j >= 0) {
      constexpr int d = I - gu_epi_start(j);
      if constexpr (d % 3 == 0) gu_epi_0<d / 3>(acc[j & 3], c, ep);
      if constexpr (d % 3 == 1) gu_epi_1<d / 3>(acc[j & 3], c, ep);
      if constexpr (d % 3 == 2) gu_epi_2<d / 3>(c, ep, c.fb0 + j);
    }
  }
  __builtin_amdgcn_sched_barrier(0);
  if constexpr (I + 1 < GU_NFRAG) gu_steps<I + 1>(c, A, X, sv, acc, ep);
}

__global__ __launch_bounds__(512, 2) void k_gu(const char* __restrict__ xf, const char* __restrict__ st, const char* __restrict__ wguf,
                                               char* __restrict__ hf LAYARG) {
  __shared__ __attribute__((aligned(16))) char smem[GU_RING * 1024];
  const int tid = threadIdx.x, lane = tid & 63;
  const int w = __builtin_amdgcn_readfirstlane(tid >> 6);
  const int b = blockIdx.x, xcd = b & 7, idx = b >> 3;
  const int tt = idx & 7, fs = 4 * xcd + (idx >> 3);
  const int tb32 = 8 * tt + w;
  const unsigned sb = lds_addr(smem), l16 = lane * 16;
  const int h = lane >> 5, r = lane & 31;

  STAMP(3, 0);
  GuCtx c;
  c.wsrc = wguf + (size_t)fs * GU_NFRAG * 1024 + w * 1024;
  c.xs = xf + (size_t)tb32 * 36 * 1024;
  c.sp = st + (size_t)(tb32 * 32) * 32; c.so = r * 32;
  c.l16 = l16; c.sb = sb; c.w = w; c.grp = w >> 2; c.h = h; c.fb0 = 9 * fs;
  c.c1 = 0.f; c.c3 = 0.f; c.cinv = 0.f;
  u32x4 sv[2], X[37];
  gu_issue_step<0, -1, -1>(c, X, sv);
#pragma unroll
  for (int k = GU_PRO_X; k < 36; ++k) asm volatile("" : "=v"(X[k]));
  STAMP(3, 1);
  f32x16 acc[4];
#pragma unroll
  for (int i = 0; i < 3; ++i)
#pragma unroll
    for (int j = 0; j < 16; ++j) acc[i][j] = 0.f;
  {
    asm volatile("s_waitcnt vmcnt(%2)" : "+v"(sv[0]), "+v"(sv[1]) : "n"(gu_wait_for(gu_idx(2, 1), 0)));
    float m0 = asf(sv[0][0]), q0 = asf(sv[0][1]), m1 = asf(sv[0][2]), q1 = asf(sv[0][3]);
    float m2 = asf(sv[1][0]), q2 = asf(sv[1][1]), m3 = asf(sv[1][2]), q3 = asf(sv[1][3]);
    float mean = 0.25f * ((m0 + m1) + (m2 + m3));
    float d0 = m0 - mean, d1 = m1 - mean, d2 = m2 - mean, d3 = m3 - mean;
    float M2 = (q0 + q1) + (q2 + q3) + 144.f * (d0 * d0 + d1 * d1 + d2 * d2 + d3 * d3);
    c.cinv = M2 * (1.f / 576.f) + 1e-5f;
    float rstd = rsqrtf(c.cinv);
    c.c1 = -2.3022081985f * rstd;
    c.c3 = -0.1029432397f * rstd * rstd * rstd;
    X[36] = mean_frag(mean, h);
  }
  waitv<gu_wait_for(gu_last_piece(0) > gu_idx(0, 1) ? gu_last_piece(0) : gu_idx(0, 1), 0)>();
  pin(X[0]); pin(X[1]);
  bar();
  STAMP(3, 2);
  c.rd0 = sb + l16; c.rd1 = sb + l16 + 65536; c.rd2 = sb + l16 + 131072;
  asm volatile("" : "+v"(c.rd0), "+v"(c.rd1));
  if (GU_RING > 128) asm volatile("" : "+v"(c.rd2));
  c.hbase = hf + (size_t)(tb32 >> 1) * 144 * 4096;
  c.hvoff = (2 * (tb32 & 1) + (r >> 4)) * 1024 + h * 256 + (r & 15) * 16;
  u32x4 A[GU_PD];
#pragma unroll
  for (int i = 0; i < GU_PD; ++i) A[i] = lds_rd(c.rd0 + i * 1024);
  GuEpi ep;
  __builtin_amdgcn_sched_barrier(0);
  gu_steps<0>(c, A, X, sv, acc, ep);
  STAMP(3, 3);
  {
    float o[8];
#pragma unroll
    for (int e = 0; e < 8; ++e) {
      float g = acc[0][e];
      float ex = __builtin_amdgcn_exp2f(g * __builtin_fmaf(g * g, c.c3, c.c1));
      o[e] = g * acc[0][e + 8] * __builtin_amdgcn_rcpf(__builtin_fmaf(ex, c.cinv, c.cinv));
    }
    const int fb = c.fb0 + 8;
    gstore16_wt(c.hbase + (size_t)(fb >> 1) * 4096 + (fb & 1) * 512, c.hvoff,
                u32x4{pack2(o[0], o[1]), pack2(o[2], o[3]), pack2(o[4], o[5]), pack2(o[6], o[7])});
  }
  STAMP(3, 4);
}

constexpr int D_DEPTH = 6;
template <int NC, int Sx>
DI void d_load(u32x4 (&Bf)[D_DEPTH][4], u32x4 (&Af)[D_DEPTH][5], const char* hs, const char* ws_, unsigned l16) {
  constexpr int sl = Sx % D_DEPTH;
  const char* hp = hs + Sx * 4096;
  gload16<0>(Bf[sl][0], hp, l16); gload16<1024>(Bf[sl][1], hp, l16); gload16<2048>(Bf[sl][2], hp, l16); gload16<3072>(Bf[sl][3], hp, l16);
  const char* wp = ws_ + Sx * NC * 1024;
  gload16<0>(Af[sl][0], wp, l16); gload16<1024>(Af[sl][1], wp, l16); gload16<2048>(Af[sl][2], wp, l16); gload16<3072>(Af[sl][3], wp, l16);
  if constexpr (NC == 5) gload16<0>(Af[sl][4], wp + 4096, l16);
}
template <int NC, int Sx>
DI void d_steps(u32x4 (&Bf)[D_DEPTH][4], u32x4 (&Af)[D_DEPTH][5], f32x4 (&acc)[4][5], const char* hs, const char* ws_, unsigned l16) {
  constexpr int sl = Sx % D_DEPTH;
  constexpr int ahead = (36 - 1 - Sx) < (D_DEPTH - 1) ? (36 - 1 - Sx) : (D_DEPTH - 1);
  constexpr int n = ahead * (4 + NC);
  if constexpr (NC == 5)
    asm volatile("s_waitcnt vmcnt(%9)" : "+v"(Bf[sl][0]), "+v"(Bf[sl][1]), "+v"(Bf[sl][2]), "+v"(Bf[sl][3]), "+v"(Af[sl][0]), "+v"(Af[sl][1]),
                 "+v"(Af[sl][2]), "+v"(Af[sl][3]), "+v"(Af[sl][4]) : "n"(n));
  else
    asm volatile("s_waitcnt vmcnt(%8)" : "+v"(Bf[sl][0]), "+v"(Bf[sl][1]), "+v"(Bf[sl][2]), "+v"(Bf[sl][3]), "+v"(Af[sl][0]), "+v"(Af[sl][1]),
                 "+v"(Af[sl][2]), "+v"(Af[sl][3]) : "n"(n));
#pragma unroll
  for (int c = 0; c < NC; ++c)
#pragma unroll
    for (int t = 0; t < 4; ++t) acc[t][c] = mfma16(Af[sl][c], Bf[sl][t], acc[t][c]);
  if constexpr (Sx + D_DEPTH < 36) {
    __builtin_amdgcn_sched_barrier(0);
    d_load<NC, Sx + D_DEPTH>(Bf, Af, hs, ws_, l16);
  }
  if constexpr (Sx + 1 < 36) d_steps<NC, Sx + 1>(Bf, Af, acc, hs, ws_, l16);
}
constexpr int D_STRIDE = 84;
template <int NC, bool LAST>
DI void d_body(char* smem, const char* hf, const char* wdf, const char* x1, char* xout, char* stout, int tt, int g, int tid, int lane, int kq LAYARG) {
  const unsigned l16 = lane * 16;
  const int st = dg_start(g);
  const char* hs = hf + ((size_t)(tt * 144 + 36 * kq) * 4) * 1024;
  const char* ws_ = wdf + ((size_t)st * 144 + (size_t)36 * kq * NC) * 1024;
  u32x4 Bf[D_DEPTH][4], Af[D_DEPTH][5];
  f32x4 acc[4][5];
#pragma unroll
  for (int t = 0; t < 4; ++t)
#pragma unroll
    for (int c = 0; c < 5; ++c) acc[t][c] = f32x4{0.f, 0.f, 0.f, 0.f};
  STAMP(4, 0);
  d_load<NC, 0>(Bf, Af, hs, ws_, l16); d_load<NC, 1>(Bf, Af, hs, ws_, l16); d_load<NC, 2>(Bf, Af, hs, ws_, l16);
  d_load<NC, 3>(Bf, Af, hs, ws_, l16); d_load<NC, 4>(Bf, Af, hs, ws_, l16); d_load<NC, 5>(Bf, Af, hs, ws_, l16);
  STAMP(4, 1);
  d_steps<NC, 0>(Bf, Af, acc, hs, ws_, l16);
  STAMP(4, 2);
  float* red = (float*)smem;
  {
    const int q = lane >> 4, t = lane & 15;
    float* rp = red + (size_t)kq * 64 * D_STRIDE + t * D_STRIDE + 4 * q;
#pragma unroll
    for (int tb = 0; tb < 4; ++tb)
#pragma unroll
      for (int c = 0; c < NC; ++c) *(f32x4*)(rp + tb * 16 * D_STRIDE + c * 16) = acc[tb][c];
  }
  __syncthreads();
  STAMP(4, 3);
  f32x2* sred = (f32x2*)(smem + 4 * 64 * D_STRIDE * 4);
  for (int it = tid; it < 64 * NC; it += 256) {
    int token, kl;
    if (LAST) { token = it / NC; kl = it - token * NC; } else { token = it & 63; kl = it >> 6; }
    const float* rp = red + token * D_STRIDE + kl * 16;
    float v[16];
#pragma unroll
    for (int i = 0; i < 4; ++i) {
      f32x4 a = *(const f32x4*)(rp + 4 * i), b = *(const f32x4*)(rp + 64 * D_STRIDE + 4 * i);
      f32x4 c = *(const f32x4*)(rp + 2 * 64 * D_STRIDE + 4 * i), d = *(const f32x4*)(rp + 3 * 64 * D_STRIDE + 4 * i);
#pragma unroll
      for (int j = 0; j < 4; ++j) v[4 * i + j] = (a[j] + b[j]) + (c[j] + d[j]);
    }
    const int gt = tt * 64 + token, ks = st + kl;
    const size_t xo = ((size_t)((gt >> 5) * 36 + ks) * 64 + (gt & 31)) * 16;
    u32x4 r0 = *(const u32x4*)(x1 + xo), r1 = *(const u32x4*)(x1 + xo + 512);
    u32x4 o0, o1;
#pragma unroll
    for (int i = 0; i < 4; ++i) {
      o0[i] = pack2(bflo(r0[i]) + v[2 * i], bfhi(r0[i]) + v[2 * i + 1]);
      o1[i] = pack2(bflo(r1[i]) + v[8 + 2 * i], bfhi(r1[i]) + v[8 + 2 * i + 1]);
    }
    if (LAST) {
      char* dst = xout + ((size_t)gt * D + 16 * ks) * 2;
      *(u32x4*)dst = o0; *(u32x4*)(dst + 16) = o1;
    } else {
      *(u32x4*)(xout + xo) = o0; *(u32x4*)(xout + xo + 512) = o1;
      float s = 0.f;
#pragma unroll
      for (int i = 0; i < 4; ++i) s += bflo(o0[i]) + bfhi(o0[i]) + bflo(o1[i]) + bfhi(o1[i]);
      float m = s * (1.f / 16.f), qq = 0.f;
#pragma unroll
      for (int i = 0; i < 4; ++i) {
        float a = bflo(o0[i]) - m, b = bfhi(o0[i]) - m, c = bflo(o1[i]) - m, d = bfhi(o1[i]) - m;
        qq += a * a + b * b + c * c + d * d;
      }
      sred[kl * 64 + token] = f32x2{m, qq};
    }
  }
  STAMP(4, 4);
  if (!LAST) {
    __syncthreads();
    if (tid < 64) {
      float mm[NC], ms = 0.f, qq = 0.f;
#pragma unroll
      for (int i = 0; i < NC; ++i) { f32x2 v = sred[i * 64 + tid]; mm[i] = v[0]; ms += v[0]; qq += v[1]; }
      float mean = ms * (1.f / NC);
#pragma unroll
      for (int i = 0; i < NC; ++i) { float d = mm[i] - mean; qq += 16.f * d * d; }
      *(f32x2*)(stout + ((size_t)(tt * 64 + tid) * 8 + g) * 8) = f32x2{mean, qq};
    }
  }
}
template <bool LAST>
__global__ __launch_bounds__(256, 1) void k_down(const char* __restrict__ hf, const char* __restrict__ wdf, const char* __restrict__ x1,
                                                 char* __restrict__ xout, char* __restrict__ stout LAYARG) {
  __shared__ __attribute__((aligned(16))) char smem[4 * 64 * D_STRIDE * 4 + 5 * 64 * 8];
  const int tid = threadIdx.x, lane = tid & 63;
  const int kq = __builtin_amdgcn_readfirstlane(tid >> 6);
  const int b = blockIdx.x, xcd = b & 7, idx = b >> 3;
  const int tt = 4 * xcd + (idx & 3), g = idx >> 2;
  if (g < 4) d_body<5, LAST>(smem, hf, wdf, x1, xout, stout, tt, g, tid, lane, kq LAYVAL(lay_));
  else d_body<4, LAST>(smem, hf, wdf, x1, xout, stout, tt, g, tid, lane, kq LAYVAL(lay_));
}

#define STEP() do { } while (0)
extern "C" void kernel_launch(void* const* d_in, const int* in_sizes, int n_in, void* d_out, int out_size, void* d_ws, size_t ws_size,
                              hipStream_t stream) {
  const unsigned short* x = (const unsigned short*)d_in[0];
  const unsigned short* wqkv = (const unsigned short*)d_in[1];
  const unsigned short* wo = (const unsigned short*)d_in[2];
  const unsigned short* wg = (const unsigned short*)d_in[4];
  const unsigned short* wu = (const unsigned short*)d_in[5];
  const unsigned short* wd = (const unsigned short*)d_in[6];
  char* ws = (char*)d_ws;
  STEP();
  k_prep<<<PB_TOTAL, 256, 0, stream>>>(x, wqkv, wo, wg, wu, wd, ws);
  for (int l = 0; l < 2; ++l) {
    const char* xin = ws + (l == 0 ? OFF_XF0 : OFF_XF2);
    const char* stin = ws + (l == 0 ? OFF_ST0 : OFF_ST2);
    double* kv = (double*)(ws + OFF_KV) + (size_t)l * KV_DBL;
    STEP();
    k_attn_a<<<256, 512, 0, stream>>>(xin, stin, ws + OFF_WQKV, ws + OFF_PQF, ws + OFF_PQL, kv LAYVAL(l));
    STEP();
    k_attn_b<<<256, 576, 0, stream>>>(ws + OFF_PQF, ws + OFF_PQL, kv, ws + OFF_WO, xin, ws + OFF_XF1, ws + OFF_ST1 LAYVAL(l));
    STEP();
    k_gu<<<256, 512, 0, stream>>>(ws + OFF_XF1, ws + OFF_ST1, ws + OFF_WGU, ws + OFF_HF LAYVAL(l));
    STEP();
    if (l == 0) k_down<false><<<256, 256, 0, stream>>>(ws + OFF_HF, ws + OFF_WD, ws + OFF_XF1, ws + OFF_XF2, ws + OFF_ST2 LAYVAL(l));
    else k_down<true><<<256, 256, 0, stream>>>(ws + OFF_HF, ws + OFF_WD, ws + OFF_XF1, (char*)d_out, nullptr LAYVAL(l));
  }
}
```

```cpp
#include <hip/hip_runtime.h>
#include <hip/hip_bf16.h>
#include <stdint.h>

typedef __attribute__((ext_vector_type(8))) __bf16 bf16x8;
typedef __attribute__((ext_vector_type(2))) __bf16 bf16x2;
typedef __attribute__((ext_vector_type(2))) float f32x2;
typedef __attribute__((ext_vector_type(4))) float f32x4;
typedef __attribute__((ext_vector_type(16))) float f32x16;
typedef __attribute__((ext_vector_type(4))) unsigned u32x4;
typedef __attribute__((ext_vector_type(2))) unsigned u32x2;
#define DI __device__ __forceinline__
#define LDS_AS __attribute__((address_space(3)))

#define LAYARG
#define LAYVAL(l)
#define STAMP(kid, i) do { } while (0)
constexpr int S = 2048, D = 576, NH = 32, DH = 18, F = 4608;

constexpr size_t SZ_XF = (size_t)S * D * 2;
constexpr size_t OFF_XF0 = 0;
constexpr size_t OFF_XF1 = OFF_XF0 + SZ_XF;
constexpr size_t OFF_XF2 = OFF_XF1 + SZ_XF;
constexpr size_t OFF_ST0 = OFF_XF2 + SZ_XF;
constexpr size_t OFF_ST1 = OFF_ST0 + (size_t)S * 64;
constexpr size_t OFF_ST2 = OFF_ST1 + (size_t)S * 32;
constexpr size_t OFF_PQF = OFF_ST2 + (size_t)S * 64;
constexpr size_t OFF_PQL = OFF_PQF + (size_t)64 * 32 * 1024;
constexpr size_t KV_DBL = (size_t)NH * 576;
constexpr size_t OFF_KV = OFF_PQL + (size_t)64 * 4 * 1024;
constexpr size_t SZ_KV = 2 * KV_DBL * 8;
constexpr int KA_NFRAG = 148, GU_NFRAG = 333;
constexpr size_t OFF_WQKV = OFF_KV + SZ_KV;
constexpr size_t OFF_WO = OFF_WQKV + (size_t)16 * KA_NFRAG * 1024;
constexpr size_t OFF_WGU = OFF_WO + (size_t)36 * 18 * 1024;
constexpr size_t OFF_WD = OFF_WGU + (size_t)32 * GU_NFRAG * 1024 + 16384;
constexpr size_t OFF_HF = OFF_WD + (size_t)36 * 144 * 1024;
constexpr size_t WS_END = OFF_HF + (size_t)S * F * 2;

DI unsigned pack2(float lo, float hi) {
  f32x2 f = {lo, hi};
  bf16x2 v = __builtin_convertvector(f, bf16x2);
  return __builtin_bit_cast(unsigned, v);
}
DI float asf(unsigned u) { return __builtin_bit_cast(float, u); }
DI float bflo(unsigned u) { return __builtin_bit_cast(float, u << 16); }
DI float bfhi(unsigned u) { return __builtin_bit_cast(float, u & 0xffff0000u); }
DI float rbf(float x) { return bflo(pack2(x, 0.f) & 0xffffu); }
DI bf16x8 asbf(u32x4 v) { return __builtin_bit_cast(bf16x8, v); }
DI f32x16 mfma32(u32x4 a, u32x4 b, f32x16 c) { return __builtin_amdgcn_mfma_f32_32x32x16_bf16(asbf(a), asbf(b), c, 0, 0, 0); }
DI f32x4 mfma16(u32x4 a, u32x4 b, f32x4 c) { return __builtin_amdgcn_mfma_f32_16x16x32_bf16(asbf(a), asbf(b), c, 0, 0, 0); }
DI unsigned lds_addr(const void* p) { return (unsigned)(size_t)(LDS_AS const char*)p; }

DI void glds16(const void* sbase, unsigned voff, unsigned dst) {
  unsigned keep;
  asm volatile("s_mov_b32 %0, m0\n\ts_mov_b32 m0, %3\n\ts_nop 0\n\tglobal_load_lds_dwordx4 %1, %2\n\ts_mov_b32 m0, %0"
               : "=&s"(keep) : "v"(voff), "s"(sbase), "s"(dst) : "memory");
}
template <int IMM> DI void gload16(u32x4& dst, const void* sbase, unsigned voff) {
  asm volatile("global_load_dwordx4 %0, %1, %2 offset:%3" : "=v"(dst) : "v"(voff), "s"(sbase), "n"(IMM));
}
template <int G> DI void gload16_g(u32x4& dst, const void* sbase, unsigned voff, int grp) {
  asm volatile("s_cmp_lg_u32 %3, %4\n\ts_cbranch_scc1 .Lsk%=\n\tglobal_load_dwordx4 %0, %1, %2\n.Lsk%=:"
               : "+v"(dst) : "v"(voff), "s"(sbase), "s"(grp), "n"(G) : "scc");
}
template <int G> DI void glds16_g(const void* sbase, unsigned voff, unsigned dst, int grp) {
  unsigned keep;
  asm volatile("s_cmp_lg_u32 %4, %5\n\ts_cbranch_scc1 .Lsk%=\n\ts_mov_b32 %0, m0\n\ts_mov_b32 m0, %3\n\ts_nop 0\n\tglobal_load_lds_dwordx4 %1, %2\n\ts_mov_b32 m0, %0\n.Lsk%=:"
               : "=&s"(keep) : "v"(voff), "s"(sbase), "s"(dst), "s"(grp), "n"(G) : "memory", "scc");
}
template <int IMM> DI void gload8(u32x2& dst, const void* sbase, unsigned voff) {
  asm volatile("global_load_dwordx2 %0, %1, %2 offset:%3" : "=v"(dst) : "v"(voff), "s"(sbase), "n"(IMM));
}
DI float sum_x16(float x) {
  auto r = __builtin_amdgcn_permlane16_swap(__builtin_bit_cast(unsigned, x), __builtin_bit_cast(unsigned, x), false, false);
  return __builtin_bit_cast(float, (unsigned)r[0]) + __builtin_bit_cast(float, (unsigned)r[1]);
}
DI float sum_x32(float x) {
  auto r = __builtin_amdgcn_permlane32_swap(__builtin_bit_cast(unsigned, x), __builtin_bit_cast(unsigned, x), false, false);
  return __builtin_bit_cast(float, (unsigned)r[0]) + __builtin_bit_cast(float, (unsigned)r[1]);
}
template <int N> DI void waitv() { asm volatile("s_waitcnt vmcnt(%0)" ::"n"(N) : "memory"); }
template <int N> DI void waitv_pin(u32x4& x) { asm volatile("s_waitcnt vmcnt(%1)" : "+v"(x) : "n"(N)); }
DI void pin(u32x4& x) { asm volatile("" : "+v"(x)); }
DI u32x4 lds_rd(unsigned addr) { return *(const LDS_AS u32x4*)(size_t)addr; }
DI void bar() { asm volatile("" ::: "memory"); __builtin_amdgcn_s_barrier(); asm volatile("" ::: "memory"); }

__host__ __device__ constexpr int dg_start(int g) { return g < 4 ? 5 * g : 20 + 4 * (g - 4); }
__host__ __device__ constexpr int dg_count(int g) { return g < 4 ? 5 : 4; }

#define PLD4(p) __builtin_nontemporal_load(p)
#define PLD2(p) __builtin_nontemporal_load(p)
DI void st16_wt(void* p, u32x4 v) { asm volatile("global_store_dwordx4 %0, %1, off sc1\n\ts_nop 1" ::"v"(p), "v"(v) : "memory"); }
DI void st16_nt(void* p, u32x4 v) { __builtin_nontemporal_store(v, (u32x4*)p); }
#define PST4(p, v) (*(u32x4*)(p) = (v))
typedef __attribute__((ext_vector_type(4))) short s16x4;
DI u32x2 lds_tr(unsigned addr) {
  return __builtin_bit_cast(u32x2, __builtin_amdgcn_ds_read_tr16_b64_v4i16((LDS_AS s16x4*)(size_t)addr));
}
DI float dot_ones(unsigned u, float acc) {
  return __builtin_amdgcn_fdot2_f32_bf16(__builtin_bit_cast(bf16x2, u), __builtin_bit_cast(bf16x2, 0x3f803f80u), acc, false);
}
DI void put_colsum(char* chunk0, int kq, float cs) {
  unsigned p = pack2(-cs, 0.f) & 0xffffu;
  float hi = bflo(p);
  unsigned lo = pack2(-cs - hi, 0.f) & 0xffffu;
#pragma unroll
  for (int t = 0; t < 3; ++t) {
    int sl = 3 * kq + t;
    *(unsigned short*)(chunk0 + (sl >> 3) * 512 + (sl & 7) * 2) = (unsigned short)(t == 1 ? lo : p);
  }
  if (kq == 3) *(u32x2*)(chunk0 + 512 + 8) = u32x2{0u, 0u};
}
__host__ __device__ constexpr int gu_frag_index(int blk, int ks) { return blk < 3 ? 3 * ks + blk : 111 + 37 * (blk - 3) + ks; }

constexpr int P_RS = 144;
struct RtRegs { u32x4 v[5]; };
DI void rt_load(int u, int tid, const unsigned short* wg, const unsigned short* wu, const unsigned short* wd, RtRegs& R) {
  if (u < 576) {
    const int m = u & 1, kq = (u >> 1) & 3, fq = u >> 3;
    const unsigned short* wsrc = m ? wu : wg;
#pragma unroll
    for (int i = 0; i < 5; ++i) {
      int ch = tid + 256 * i; if (ch > 1151) ch = 1151;
      R.v[i] = PLD4((const u32x4*)(wsrc + (size_t)(144 * kq + (ch >> 3)) * F + 64 * fq + 8 * (ch & 7)));
    }
  } else {
    const int ub = u - 576, cgp = ub % 9, sg = ub / 9;
#pragma unroll
    for (int i = 0; i < 4; ++i) {
      int ch = tid + 256 * i;
      R.v[i] = PLD4((const u32x4*)(wd + (size_t)(128 * sg + (ch >> 3)) * D + 64 * cgp + 8 * (ch & 7)));
    }
    R.v[4] = R.v[3];
  }
}
DI void rt_stage(int u, int tid, char* sm, const RtRegs& R) {
  const int nch = u < 576 ? 1152 : 1024;
#pragma unroll
  for (int i = 0; i < 5; ++i) {
    int ch = tid + 256 * i;
    if (ch < nch) *(u32x4*)(sm + (ch >> 3) * P_RS + (ch & 7) * 16) = R.v[i];
  }
}
DI void rt_emit(int u, int tid, char* sm, char* ws) {
  const int lane = tid & 63, w = tid >> 6;
  const unsigned sb = lds_addr(sm);
  if (u < 576) {
    const int m = u & 1, kq = (u >> 1) & 3, fq = u >> 3;
    const int fb = 4 * fq + w, fs = fb / 9, blk = fb - 9 * fs;
    const int g = lane >> 4, h = g & 1, kpar = g >> 1, qq = (lane >> 2) & 3, pp = lane & 3, i16 = lane & 15;
    const int pq = (pp >> 1) | ((pp & 1) << 1);
    const unsigned rd = sb + (16 * kpar + 8 * h + qq) * P_RS + w * 32 + pq * 8;
    char* dst = ws + OFF_WGU + (size_t)fs * GU_NFRAG * 1024 + (h * 32 + m * 16 + i16) * 16;
    float cs = 0.f;
#pragma unroll
    for (int k2 = 0; k2 < 5; ++k2) {
      const int kl = 2 * k2 + kpar, back = kl < 9 ? 0 : 16;
      u32x2 lo = lds_tr(rd + (2 * k2 * 16 - back) * P_RS), hi = lds_tr(rd + (2 * k2 * 16 + 4 - back) * P_RS);
      u32x4 o = {lo[0], lo[1], hi[0], hi[1]};
      if (kl < 9) {
        cs = dot_ones(o[0], cs); cs = dot_ones(o[1], cs); cs = dot_ones(o[2], cs); cs = dot_ones(o[3], cs);
        PST4(dst + (size_t)gu_frag_index(blk, 9 * kq + kl) * 1024, o);
      }
    }
    cs += __shfl_xor(cs, 16); cs += __shfl_xor(cs, 32);
    if (lane < 16) put_colsum(ws + OFF_WGU + ((size_t)fs * GU_NFRAG + gu_frag_index(blk, 36)) * 1024 + (m * 16 + lane) * 16, kq, cs);
  } else {
    const int ub = u - 576, cgp = ub % 9, sg = ub / 9;
    const int c = 4 * cgp + w;
    const int g = c < 20 ? c / 5 : 4 + (c - 20) / 4;
    const int st = dg_start(g), n = dg_count(g);
    const int q = lane >> 4, qq = (lane >> 2) & 3, pp = lane & 3;
    const unsigned rd = sb + (8 * q + qq) * P_RS + w * 32 + pp * 8;
#pragma unroll
    for (int sl = 0; sl < 4; ++sl) {
      u32x2 lo = lds_tr(rd + sl * 32 * P_RS), hi = lds_tr(rd + (sl * 32 + 4) * P_RS);
      size_t frag = (size_t)st * 144 + (size_t)(4 * sg + sl) * n + (c - st);
      PST4(ws + OFF_WD + frag * 1024 + lane * 16, (u32x4{lo[0], lo[1], hi[0], hi[1]}));
    }
  }
}

constexpr int PB_WQKV = 64, PB_RT = 450, PB_X = 128, PB_WO = 18, PB_Z = 9;
constexpr int PB_TOTAL = PB_WQKV + PB_RT + PB_X + PB_WO + PB_Z;

__global__ __launch_bounds__(512) void k_prep(const unsigned short* __restrict__ x, const unsigned short* __restrict__ wqkv,
                                              const unsigned short* __restrict__ wo, const unsigned short* __restrict__ wg,
                                              const unsigned short* __restrict__ wu, const unsigned short* __restrict__ wd,
                                              char* __restrict__ ws) {
  __shared__ __attribute__((aligned(16))) char smem[2 * 576 * 40];
  int b = blockIdx.x;
  const int tid = threadIdx.x, lane = tid & 63, w8 = tid >> 6;
  if (b < PB_WQKV) {
    constexpr int RS = 224;
    const int p = b >> 2, kq = b & 3;
    u32x2 v[8];
#pragma unroll
    for (int i = 0; i < 8; ++i) {
      int ch = tid + 512 * i; if (ch > 3887) ch = 3887;
      int row = ch / 27, rem = ch - 27 * row, m = rem / 9, c4 = rem - 9 * m;
      v[i] = PLD2((const u32x2*)(wqkv + (size_t)(144 * kq + row) * (3 * D) + 576 * m + 36 * p + 4 * c4));
    }
    const int blk = w8 & 3, kh = w8 >> 2, h = lane >> 5, rho = lane & 31;
    int col = 108;
    if (blk == 0) col = 36 + rho;
    else if (blk == 1) {
      if (rho < 18) col = 72 + rho;
      else if (rho >= 19 && rho <= 22) col = 36 + 32 + (rho - 19);
      else if (rho >= 23 && rho <= 26) col = 18 * ((rho - 23) >> 1) + 16 + ((rho - 23) & 1);
    } else if (blk == 2) { if (rho < 18) col = 72 + 18 + rho; }
    else col = 18 * (rho >> 4) + 8 * ((rho >> 2) & 1) + 4 * ((rho >> 3) & 1) + (rho & 3);
#pragma unroll
    for (int i = 0; i < 8; ++i) {
      int ch = tid + 512 * i;
      if (ch < 3888) { int row = ch / 27, rem = ch - 27 * row; *(u32x2*)(smem + row * RS + rem * 8) = v[i]; }
    }
    if (tid < 144) *(u32x2*)(smem + tid * RS + 216) = u32x2{0u, 0u};
    __syncthreads();
    const unsigned short* src = (const unsigned short*)(smem + (80 * kh + 8 * h) * RS) + col;
    char* dst = ws + OFF_WQKV + (size_t)p * KA_NFRAG * 1024 + blk * 1024 + lane * 16;
    float cs = 0.f;
#pragma unroll
    for (int kl = 0; kl < 5; ++kl) {
      if (5 * kh + kl < 9) {
        u32x4 o;
#pragma unroll
        for (int i = 0; i < 4; ++i)
          o[i] = (unsigned)src[(kl * 16 + 2 * i) * (RS / 2)] | ((unsigned)src[(kl * 16 + 2 * i + 1) * (RS / 2)] << 16);
        cs = dot_ones(o[0], cs); cs = dot_ones(o[1], cs); cs = dot_ones(o[2], cs); cs = dot_ones(o[3], cs);
        PST4(dst + (size_t)(9 * kq + 5 * kh + kl) * 4096, o);
      }
    }
    cs += __shfl_xor(cs, 32);
    float* cx = (float*)(smem + 144 * RS);
    if (kh == 1 && lane < 32) cx[blk * 32 + lane] = cs;
    __syncthreads();
    if (kh == 0 && lane < 32) put_colsum(ws + OFF_WQKV + ((size_t)p * KA_NFRAG + 144 + blk) * 1024 + lane * 16, kq, cs + cx[blk * 32 + lane]);
    return;
  }
  b -= PB_WQKV;
  if (b < PB_RT) {
    const int half = tid >> 8, t = tid & 255, u = 2 * b + half;
    char* sm = smem + half * (144 * P_RS);
    RtRegs R;
    rt_load(u, t, wg, wu, wd, R);
    rt_stage(u, t, sm, R);
    __syncthreads();
    rt_emit(u, t, sm, ws);
    return;
  }
  b -= PB_RT;
  if (b < PB_X) {
    const int ks = lane < 36 ? lane : 35;
    u32x4 v0[2], v1[2];
#pragma unroll
    for (int i = 0; i < 2; ++i) {
      const u32x4* src = (const u32x4*)(x + (size_t)(b * 16 + w8 * 2 + i) * D + 16 * ks);
      v0[i] = PLD4(src); v1[i] = PLD4(src + 1);
    }
#pragma unroll
    for (int i = 0; i < 2; ++i) {
      const int token = b * 16 + w8 * 2 + i, tb = token >> 5, r = token & 31;
      if (lane < 36) {
        char* dst = ws + OFF_XF0 + ((size_t)(tb * 36 + ks) * 64 + r) * 16;
        PST4(dst, v0[i]); PST4(dst + 512, v1[i]);
      }
      float s = 0.f;
#pragma unroll
      for (int j = 0; j < 4; ++j) s += bflo(v0[i][j]) + bfhi(v0[i][j]) + bflo(v1[i][j]) + bfhi(v1[i][j]);
      float m16 = s * (1.f / 16.f), q = 0.f;
#pragma unroll
      for (int j = 0; j < 4; ++j) {
        float a = bflo(v0[i][j]) - m16, c = bfhi(v0[i][j]) - m16, d = bflo(v1[i][j]) - m16, e = bfhi(v1[i][j]) - m16;
        q += a * a + c * c + d * d + e * e;
      }
      int g = lane & 7, st = dg_start(g), n = dg_count(g);
      float ms = 0.f, mm[5], qq = 0.f;
#pragma unroll
      for (int j = 0; j < 5; ++j) {
        int sl = st + (j < n ? j : 0);
        mm[j] = __shfl(m16, sl); float qi = __shfl(q, sl);
        if (j < n) { ms += mm[j]; qq += qi; }
      }
      float mean = ms / n;
#pragma unroll
      for (int j = 0; j < 5; ++j) if (j < n) { float d = mm[j] - mean; qq += 16.f * d * d; }
      if (lane < 8) *(f32x2*)(ws + OFF_ST0 + ((size_t)token * 8 + lane) * 8) = f32x2{mean, qq};
    }
    return;
  }
  b -= PB_X;
  if (b < PB_WO) {
    constexpr int RS = 40;
    const int half = tid >> 8, t = tid & 255, w = w8 & 3, c = 2 * b + half;
    char* sm = smem + half * (576 * RS);
    u32x4 v[5];
#pragma unroll
    for (int i = 0; i < 5; ++i) {
      int ch = t + 256 * i; if (ch > 1151) ch = 1151;
      v[i] = PLD4((const u32x4*)(wo + (size_t)(ch >> 1) * D + 16 * c + 8 * (ch & 1)));
    }
#pragma unroll
    for (int i = 0; i < 5; ++i) {
      int ch = t + 256 * i;
      if (ch < 1152) { char* d = sm + (ch >> 1) * RS + (ch & 1) * 16; *(u32x2*)d = u32x2{v[i][0], v[i][1]}; *(u32x2*)(d + 8) = u32x2{v[i][2], v[i][3]}; }
    }
    __syncthreads();
    const unsigned sb = lds_addr(sm);
    const int q = lane >> 4, qq = (lane >> 2) & 3, pp = lane & 3;
    for (int s = w; s < 18; s += 4) {
      int r0, r1;
      if (s < 16) { r0 = (2 * s + (q >> 1)) * 18 + 8 * (q & 1) + qq; r1 = r0 + 4; }
      else { int w0 = 4 * (s - 16) + (q >> 1); r0 = (4 * w0 + qq) * 18 + 16 + (q & 1); r1 = (4 * (w0 + 2) + qq) * 18 + 16 + (q & 1); }
      u32x2 lo = lds_tr(sb + r0 * RS + pp * 8), hi = lds_tr(sb + r1 * RS + pp * 8);
      PST4(ws + OFF_WO + ((size_t)c * 18 + s) * 1024 + lane * 16, (u32x4{lo[0], lo[1], hi[0], hi[1]}));
    }
    return;
  }
  b -= PB_WO;
#pragma unroll
  for (int i = 0; i < 4; ++i) {
    size_t o = (((size_t)b * 4 + i) * 512 + tid) * 16;
    if (o < SZ_KV) PST4(ws + OFF_KV + o, (u32x4{0, 0, 0, 0}));
  }
}

DI u32x4 mean_frag(float mean, int h) {
  unsigned mh = pack2(mean, 0.f) & 0xffffu;
  unsigned ml = pack2(mean - bflo(mh), 0.f) & 0xffffu;
  unsigned hh = mh | (mh << 16), lh = ml | (mh << 16), hl = mh | (ml << 16);
  return h == 0 ? u32x4{hh, lh, hl, hh} : u32x4{lh, hl, 0u, 0u};
}

DI float phi2(float a, float rs2) {
  const float t = a * rs2;
  return __builtin_fmaf(fmaxf(t, 0.f), 0.6931471805599453f, __builtin_amdgcn_exp2f(fminf(t, 0.f)) + 1e-6f);
}
constexpr int KA_PRO = 12;
constexpr int KA_RING = 37;
constexpr int KA_PD = 8;
constexpr int ka_kv_row(int e) { return e < 16 ? (e & 3) + 4 * (e >> 3) + 8 * ((e >> 2) & 1) : (e == 16 ? 16 : (e == 17 ? 20 : 17)); }
template <int KS>
DI void ka_steps(u32x4 (&X)[37], u32x4 (&A)[KA_PD], f32x16 (&acc)[4], const char* xs, unsigned l16, unsigned rd0, unsigned rd1, float mean, int h) {
  if constexpr (KS == 36) X[36] = mean_frag(mean, h);
  if constexpr (KS % 4 == 0 && KS > 0 && KS <= 32) bar();
  if constexpr (KS < 36) {
    constexpr int issued = KS + KA_PRO < 36 ? KS + KA_PRO : 36;
    waitv_pin<issued - KS - 1>(X[KS]);
    if constexpr (KS + KA_PRO < 36) gload16<0>(X[KS + KA_PRO], xs + (KS + KA_PRO) * 1024, l16);
  }
#pragma unroll
  for (int blk = 0; blk < 4; ++blk) {
    const int i = 4 * KS + blk;
    u32x4 a = A[i % KA_PD];
    if (blk < 3) acc[blk] = mfma32(X[KS], a, acc[blk]);
    else acc[3] = mfma32(a, X[KS], acc[3]);
    if (i + KA_PD < KA_NFRAG) { const int f = (i + KA_PD) % (4 * KA_RING); A[i % KA_PD] = lds_rd((f < 64 ? rd0 : (f < 128 ? rd1 : rd1 + 65536)) + (f & 63) * 1024); }
    __builtin_amdgcn_sched_barrier(0);
  }
  if constexpr (KS + 1 < 37) ka_steps<KS + 1>(X, A, acc, xs, l16, rd0, rd1, mean, h);
}
constexpr int ka_to(int G) { int t = G == 0 ? 9 : 4 * G + 16; if (G > 0 && t > 4 * (G - 1) + KA_RING) t = 4 * (G - 1) + KA_RING; return t < 37 ? t : 37; }
template <int G> DI void ka_loader(const char* wsrc, unsigned l16, unsigned dst) {
  constexpr int need = 4 * G + 7 < 36 ? 4 * G + 7 : 36;
  constexpr int from = G == 0 ? 0 : ka_to(G - 1);
  constexpr int to = ka_to(G);
  static_assert(to > need, "loader schedule");
#pragma unroll
  for (int ks = from; ks < to; ++ks) glds16(wsrc + ks * 4096, l16, dst + (4 * (ks % KA_RING)) * 1024);
  waitv<to - 1 - need>();
  bar();
  if constexpr (G < 8) ka_loader<G + 1>(wsrc, l16, dst);
}

__global__ __launch_bounds__(512, 2) void k_attn_a(const char* xf, const char* st, const char* wqkvf, char* pqf, char* pql, double* kv LAYARG) {
  __shared__ __attribute__((aligned(16))) char smem[KA_RING * 4096];
  asm volatile("" : "+s"(xf), "+s"(st), "+s"(wqkvf), "+s"(pqf), "+s"(pql), "+s"(kv));
  const int tid = threadIdx.x, lane = tid & 63;
  const int w8 = __builtin_amdgcn_readfirstlane(tid >> 6);
  const int w = w8 & 3;
  const int b = blockIdx.x, xcd = b & 7, idx = b >> 3;
  const int tt = 2 * xcd + (idx & 1), p = idx >> 1;
  const int tb = 4 * tt + w;
  const unsigned sb = lds_addr(smem);
  const unsigned l16 = lane * 16;
  const int h = lane >> 5, r = lane & 31;
  STAMP(1, 0);
  if (w8 >= 4) {
    const char* wsrc = wqkvf + (size_t)p * KA_NFRAG * 1024 + w * 1024;
    ka_loader<0>(wsrc, l16, sb + w * 1024);
    int fo[5];
#pragma unroll
    for (int i = 0; i < 5; ++i) {
      const int it = (tid - 256) + 256 * i;
      const int hs = it >= 576, sl = it - 576 * hs;
      int row, d;
      if (sl < 512) { row = (sl >> 1) & 31; d = 8 * ((sl >> 6) & 1) + 2 * (sl >> 7) + (sl & 1); }
      else { row = sl & 31; d = 16 + ((sl >> 5) & 1); }
      const int e = row < 16 ? (row & 3) + 4 * (row >> 3) + 8 * ((row >> 2) & 1) : (row == 16 ? 16 : (row == 20 ? 17 : (row == 17 ? 18 : -1)));
      const int o = hs == 0 ? d * 33 + e : (d < 14 ? (18 + d) * 33 + e : (32 + d - 14) * 33 + e);
      fo[i] = (it < 1152 && e >= 0) ? o : -1;
    }
    bar();
    bar();
    float sum[5];
#pragma unroll
    for (int i = 0; i < 5; ++i) {
      const float* rp = (const float*)smem + (fo[i] >= 0 ? fo[i] : 0);
      sum[i] = (rp[0] + rp[1280]) + (rp[2560] + rp[3840]);
    }
#pragma unroll
    for (int i = 0; i < 5; ++i)
      if (fo[i] >= 0) unsafeAtomicAdd(kv + (size_t)(2 * p) * 576 + (tid - 256) + 256 * i, (double)sum[i]);
  } else {
    u32x4 sv[4], X[37];
    const char* xs = xf + (size_t)tb * 36 * 1024;
    gload16<0>(X[0], xs, l16); gload16<0>(X[1], xs + 1024, l16);
    {
      const char* sp = st + (size_t)(tb * 32) * 64;
      unsigned so = r * 64;
      gload16<0>(sv[0], sp, so); gload16<16>(sv[1], sp, so); gload16<32>(sv[2], sp, so); gload16<48>(sv[3], sp, so);
    }
#pragma unroll
    for (int k = 2; k < KA_PRO; ++k) gload16<0>(X[k], xs + k * 1024, l16);
    STAMP(1, 1);
    float rstd, mean;
    {
      asm volatile("s_waitcnt vmcnt(%4)" : "+v"(sv[0]), "+v"(sv[1]), "+v"(sv[2]), "+v"(sv[3]) : "n"(KA_PRO - 2));
      float m[8], q[8];
#pragma unroll
      for (int i = 0; i < 4; ++i) {
        m[2 * i] = asf(sv[i][0]); q[2 * i] = asf(sv[i][1]);
        m[2 * i + 1] = asf(sv[i][2]); q[2 * i + 1] = asf(sv[i][3]);
      }
      mean = (80.f * (m[0] + m[1] + m[2] + m[3]) + 64.f * (m[4] + m[5] + m[6] + m[7])) * (1.f / 576.f);
      float M2 = 0.f;
#pragma unroll
      for (int i = 0; i < 8; ++i) { float d = m[i] - mean; M2 += q[i] + (i < 4 ? 80.f : 64.f) * d * d; }
      rstd = rsqrtf(M2 * (1.f / 576.f) + 1e-5f);
    }
    f32x16 acc[4];
#pragma unroll
    for (int i = 0; i < 4; ++i)
#pragma unroll
      for (int j = 0; j < 16; ++j) acc[i][j] = 0.f;
    unsigned rd0 = sb + l16, rd1 = rd0 + 65536;
    asm volatile("" : "+v"(rd0), "+v"(rd1));
    bar();
    STAMP(1, 2);
    u32x4 A[KA_PD];
#pragma unroll
    for (int i = 0; i < KA_PD; ++i) A[i] = lds_rd(rd0 + i * 1024);
    __builtin_amdgcn_sched_barrier(0);
    ka_steps<0>(X, A, acc, xs, l16, rd0, rd1, mean, h);
    STAMP(1, 3);
    bar();
    STAMP(1, 6);

    const int c = r;
    float* rs = (float*)(smem + 32768) + w * 160;
    float* ql = rs + 32;
    if (h == 0) rs[r] = rstd;
    f32x4 rr[4];
#pragma unroll
    for (int g = 0; g < 4; ++g) rr[g] = *(const f32x4*)(rs + 8 * g + 4 * h);
    u32x4 k1[2], v1[2], v2[2];
    f32x4 pv[4];
    const bool ph = c >= 19 && c <= 26, one = c == 18;
#pragma unroll
    for (int s = 0; s < 2; ++s)
#pragma unroll
      for (int t = 0; t < 4; ++t) {
        const int r0 = 8 * s + 2 * t;
        const float s0 = rr[r0 >> 2][r0 & 3], s1 = rr[r0 >> 2][(r0 & 3) + 1];
        const float e0 = s0 * 1.4426950408889634f, e1 = s1 * 1.4426950408889634f;
        k1[s][t] = pack2(phi2(acc[0][r0], e0), phi2(acc[0][r0 + 1], e1));
        float a0 = acc[1][r0] * s0, a1 = acc[1][r0 + 1] * s1;
        float p0 = phi2(acc[1][r0], e0), p1 = phi2(acc[1][r0 + 1], e1);
        a0 = ph ? p0 : (one ? 1.f : a0); a1 = ph ? p1 : (one ? 1.f : a1);
        pv[r0 >> 2][r0 & 3] = p0; pv[r0 >> 2][(r0 & 3) + 1] = p1;
        v1[s][t] = pack2(a0, a1);
        v2[s][t] = one ? 0x3f803f80u : pack2(acc[2][r0] * s0, acc[2][r0 + 1] * s1);
      }
    if (c >= 23 && c <= 26) {
#pragma unroll
      for (int g = 0; g < 4; ++g) *(f32x4*)(ql + (c - 23) * 32 + 8 * g + 4 * h) = pv[g];
    }
    STAMP(1, 7);
    f32x16 R1, R2, R3;
#pragma unroll
    for (int j = 0; j < 16; ++j) { R1[j] = 0.f; R2[j] = 0.f; R3[j] = 0.f; }
#pragma unroll
    for (int s = 0; s < 2; ++s) {
      R1 = mfma32(k1[s], v1[s], R1);
      R2 = mfma32(k1[s], v2[s], R2);
      R3 = mfma32(v1[s], v2[s], R3);
    }
    float* red = (float*)smem + w * 1280;
#pragma unroll
    for (int j = 0; j < 16; ++j) {
      int row = (j & 3) + 8 * (j >> 2) + 4 * h;
      red[row * 33 + c] = row < 18 ? R1[j] : R2[j];
    }
    if (h == 0) red[32 * 33 + c] = R3[11];
    else { red[33 * 33 + c] = R3[8]; red[34 * 33 + c] = R3[9]; red[35 * 33 + c] = R3[10]; }
    STAMP(1, 4);
    asm volatile("s_waitcnt lgkmcnt(0)" ::: "memory");
    bar();
    {
      u32x4 qa, qb;
      const float rse = rstd * 1.4426950408889634f;
#pragma unroll
      for (int t = 0; t < 4; ++t) {
        qa[t] = pack2(phi2(acc[3][2 * t], rse), phi2(acc[3][2 * t + 1], rse));
        qb[t] = pack2(phi2(acc[3][8 + 2 * t], rse), phi2(acc[3][8 + 2 * t + 1], rse));
      }
      char* dst = pqf + ((size_t)tb * 32 + 2 * p) * 1024 + l16;
      *(u32x4*)dst = qa; *(u32x4*)(dst + 1024) = qb;
      float la = ql[h * 32 + r], lb = ql[(2 + h) * 32 + r];
      *(unsigned*)(pql + ((size_t)tb * 4 + (p >> 2)) * 1024 + l16 + 4 * (p & 3)) = pack2(la, lb);
    }
  }
}

__global__ __launch_bounds__(576) void k_attn_b(const char* pqf, const char* pql, const double* kv, const char* wof, const char* xin, char* xout,
                                                char* stout LAYARG) {
  __shared__ __attribute__((aligned(16))) char smem[36 * 1024 + 9 * 32 * 8];
  asm volatile("" : "+s"(pqf), "+s"(pql), "+s"(kv), "+s"(wof), "+s"(xin), "+s"(xout), "+s"(stout));
  const int tid = threadIdx.x, lane = tid & 63;
  const int w = __builtin_amdgcn_readfirstlane(tid >> 6);
  const int b = blockIdx.x, xcd = b & 7, idx = b >> 3;
  const int tb = 8 * xcd + (idx & 7), cg = idx >> 3;
  const int ct = 9 * cg + w;
  const int h = lane >> 5, r = lane & 31, q = lane >> 4, t = lane & 15;

  STAMP(2, 0);
  u32x4 A[18];
  const char* wp = wof + (size_t)ct * 18 * 1024;
  const unsigned l16 = lane * 16;
  u32x2 res[2];
  const unsigned xo = ((tb * 36 + ct) * 64 + (q >> 1) * 32 + t) * 16 + (q & 1) * 8;
#define KB_LDA(s_) gload16<((s_) & 3) * 1024>(A[s_], wp + ((s_) >> 2) * 4096, l16)

  if (w < 8) {
    const bool valid = r < 18 || r == 20 || r == 21;
    const int rr = valid ? (r == 21 ? 17 : r) : 0;
    const unsigned kvo = (h * 32 + rr) * 16, klo = (h * 32 + rr) * 8;
    u32x4 kd[4][4];
    u32x2 kl[4];
    u32x4 bq[4], bl;
    __builtin_amdgcn_s_setprio(3);
    gload16<0>(bl, pql + ((size_t)tb * 4 + (w >> 1)) * 1024, l16);
#pragma unroll
    for (int i = 0; i < 4; ++i) {
      const char* src = (const char*)(kv + (size_t)(4 * w + i) * 576);
      gload16<0>(kd[i][0], src, kvo); gload16<1024>(kd[i][1], src, kvo); gload16<2048>(kd[i][2], src, kvo); gload16<3072>(kd[i][3], src, kvo);
      gload8<0>(kl[i], src + 4096, klo);
      gload16<0>(bq[i], pqf + ((size_t)tb * 32 + 4 * w + i) * 1024, l16);
    }
    __builtin_amdgcn_s_setprio(0);
    STAMP(2, 1);
    float lo[4];
#pragma unroll
    for (int i = 0; i < 4; ++i) {
      if (i == 0) asm volatile("s_waitcnt vmcnt(18)" : "+v"(bl), "+v"(kd[0][0]), "+v"(kd[0][1]), "+v"(kd[0][2]), "+v"(kd[0][3]), "+v"(kl[0]), "+v"(bq[0]));
      if (i == 1) asm volatile("s_waitcnt vmcnt(17)" : "+v"(kd[1][0]), "+v"(kd[1][1]), "+v"(kd[1][2]), "+v"(kd[1][3]), "+v"(kl[1]), "+v"(bq[1]));
      if (i == 2) asm volatile("s_waitcnt vmcnt(16)" : "+v"(kd[2][0]), "+v"(kd[2][1]), "+v"(kd[2][2]), "+v"(kd[2][3]), "+v"(kl[2]), "+v"(bq[2]));
      if (i == 3) asm volatile("s_waitcnt vmcnt(14)" : "+v"(kd[3][0]), "+v"(kd[3][1]), "+v"(kd[3][2]), "+v"(kd[3][3]), "+v"(kl[3]), "+v"(bq[3]));
      u32x4 a1, a2;
#pragma unroll
      for (int k = 0; k < 4; ++k) {
        double d0 = __builtin_bit_cast(double, u32x2{kd[i][k][0], kd[i][k][1]});
        double d1 = __builtin_bit_cast(double, u32x2{kd[i][k][2], kd[i][k][3]});
        a1[k] = valid ? pack2((float)d0, (float)d1) : 0u;
      }
      const float klf = (float)__builtin_bit_cast(double, kl[i]);
      const unsigned lv = valid ? ((i & 1) ? pack2(0.f, klf) : pack2(klf, 0.f)) : 0u;
      __builtin_amdgcn_sched_barrier(0);
      if (i == 0) { KB_LDA(0); KB_LDA(1); KB_LDA(2); KB_LDA(3); KB_LDA(4); }
      if (i == 1) { KB_LDA(5); KB_LDA(6); KB_LDA(7); KB_LDA(8); KB_LDA(9); }
      if (i == 2) { KB_LDA(10); KB_LDA(11); KB_LDA(12); KB_LDA(13); }
      if (i == 3) { KB_LDA(14); KB_LDA(15); KB_LDA(16); KB_LDA(17); gload8<0>(res[0], xin, xo); gload8<256>(res[1], xin, xo); }
      const int dw = 2 * (w & 1) + (i >> 1);
#pragma unroll
      for (int k = 0; k < 4; ++k) a2[k] = (k == dw) ? lv : 0u;
      f32x16 n;
#pragma unroll
      for (int j = 0; j < 16; ++j) n[j] = 0.f;
      n = mfma32(a1, bq[i], n);
      n = mfma32(a2, bl, n);
      float rd = __builtin_amdgcn_rcpf(n[9] + 1e-6f);
      u32x4 o;
#pragma unroll
      for (int k = 0; k < 4; ++k) o[k] = pack2(n[2 * k] * rd, n[2 * k + 1] * rd);
      lo[i] = n[8] * rd;
      const int head = 4 * w + i, s = head >> 1, qq = 2 * (head & 1) + h;
      *(u32x4*)(smem + (((r >> 4) * 18 + s) * 64 + qq * 16 + (r & 15)) * 16) = o;
    }
    {
      const int s = 16 + (w >> 2), qq = 2 * (w & 1) + h, half = (w >> 1) & 1;
      *(u32x2*)(smem + (((r >> 4) * 18 + s) * 64 + qq * 16 + (r & 15)) * 16 + half * 8) = u32x2{pack2(lo[0], lo[1]), pack2(lo[2], lo[3])};
    }
  } else {
    KB_LDA(0); KB_LDA(1); KB_LDA(2); KB_LDA(3); KB_LDA(4); KB_LDA(5); KB_LDA(6); KB_LDA(7); KB_LDA(8);
    KB_LDA(9); KB_LDA(10); KB_LDA(11); KB_LDA(12); KB_LDA(13); KB_LDA(14); KB_LDA(15); KB_LDA(16); KB_LDA(17);
    gload8<0>(res[0], xin, xo); gload8<256>(res[1], xin, xo);
  }
  STAMP(2, 2);
  asm volatile("s_waitcnt lgkmcnt(0)" ::: "memory");
  bar();
  STAMP(2, 3);
  f32x4 acc[2] = {{0.f, 0.f, 0.f, 0.f}, {0.f, 0.f, 0.f, 0.f}};
#pragma unroll
  for (int s = 0; s < 18; ++s) {
    if (s == 0) asm volatile("s_waitcnt vmcnt(15)" : "+v"(A[0]), "+v"(A[1]), "+v"(A[2]), "+v"(A[3]), "+v"(A[4]));
    if (s == 5) asm volatile("s_waitcnt vmcnt(10)" : "+v"(A[5]), "+v"(A[6]), "+v"(A[7]), "+v"(A[8]), "+v"(A[9]));
    if (s == 10) asm volatile("s_waitcnt vmcnt(6)" : "+v"(A[10]), "+v"(A[11]), "+v"(A[12]), "+v"(A[13]));
    if (s == 14) asm volatile("s_waitcnt vmcnt(2)" : "+v"(A[14]), "+v"(A[15]), "+v"(A[16]), "+v"(A[17]));
    u32x4 b0 = *(const u32x4*)(smem + (s * 64 + lane) * 16);
    u32x4 b1 = *(const u32x4*)(smem + ((18 + s) * 64 + lane) * 16);
    acc[0] = mfma16(A[s], b0, acc[0]);
    acc[1] = mfma16(A[s], b1, acc[1]);
  }
  STAMP(2, 4);
  asm volatile("s_waitcnt vmcnt(0)" : "+v"(res[0]), "+v"(res[1]));
  f32x2* sred = (f32x2*)(smem + 36 * 1024);
#pragma unroll
  for (int tbb = 0; tbb < 2; ++tbb) {
    float v0 = bflo(res[tbb][0]) + acc[tbb][0], v1 = bfhi(res[tbb][0]) + acc[tbb][1];
    float v2 = bflo(res[tbb][1]) + acc[tbb][2], v3 = bfhi(res[tbb][1]) + acc[tbb][3];
    u32x2 o = {pack2(v0, v1), pack2(v2, v3)};
    *(u32x2*)(xout + xo + tbb * 256) = o;
    v0 = bflo(o[0]); v1 = bfhi(o[0]); v2 = bflo(o[1]); v3 = bfhi(o[1]);
    float s = sum_x32(sum_x16((v0 + v1) + (v2 + v3)));
    float m = s * (1.f / 16.f);
    float d0 = v0 - m, d1 = v1 - m, d2 = v2 - m, d3 = v3 - m;
    float qq = sum_x32(sum_x16(d0 * d0 + d1 * d1 + d2 * d2 + d3 * d3));
    if (q == 0) sred[w * 32 + tbb * 16 + t] = f32x2{m, qq};
  }
  asm volatile("s_waitcnt lgkmcnt(0)" ::: "memory");
  bar();
  if (tid < 32) {
    float mm[9], ms = 0.f, qq = 0.f;
#pragma unroll
    for (int i = 0; i < 9; ++i) { f32x2 v = sred[i * 32 + tid]; mm[i] = v[0]; ms += v[0]; qq += v[1]; }
    float mean = ms * (1.f / 9.f);
#pragma unroll
    for (int i = 0; i < 9; ++i) { float d = mm[i] - mean; qq += 16.f * d * d; }
    *(f32x2*)(stout + ((size_t)(tb * 32 + tid) * 4 + cg) * 8) = f32x2{mean, qq};
  }
  STAMP(2, 5);
}

#define GU_STAGE_ 32
#define GU_RING_ 128
constexpr int GU_STAGE = GU_STAGE_, GU_RING = GU_RING_, GU_P1 = 111;
constexpr int GU_PD = 5;
constexpr int GU_NSLOT = GU_RING / GU_STAGE, GU_NPIECE = (GU_NFRAG + 7) / 8;
constexpr int GU_NST = (GU_NFRAG + GU_STAGE - 1) / GU_STAGE;
struct GuSched { int n; int kind[96]; int arg[96]; int step[96]; };
#define GU_PRO_X_ 10
#define GU_PRO_P_ (GU_STAGE_ / 8 + 2)
#define GU_PACE 5
#define GU_LAG_ 1
constexpr int GU_PRO_X = GU_PRO_X_;
constexpr int GU_PRO_P = GU_PRO_P_;
constexpr int gu_pstage(int p) { return 8 * p / GU_STAGE; }
constexpr int gu_pdead(int p) { int st = gu_pstage(p); return st == 0 ? 0 : st * GU_STAGE - GU_PD; }
constexpr int gu_pearly(int p) { int st = gu_pstage(p); return st < GU_NSLOT ? 0 : (st - GU_NSLOT + 2) * GU_STAGE - GU_PD + 2; }
constexpr GuSched gu_make_sched() {
  GuSched s{};
  int n = 0;
  auto push = [&](int k, int a, int st) { s.kind[n] = k; s.arg[n] = a; s.step[n] = st; ++n; };
  push(2, 0, -1); push(2, 1, -1);
  int kx = 0, pc = 0;
  while (kx < GU_PRO_X || pc < GU_PRO_P) {
    bool takep = pc < GU_PRO_P && (kx >= GU_PRO_X || (pc < GU_STAGE / 8 ? 8 * pc <= 12 * kx : 8 * pc <= 3 * kx));
    if (takep) push(1, pc++, -1); else push(0, kx++, -1);
  }
  int m = 0, last = 0;
  while (kx < 36 || pc < GU_NPIECE) {
    int dx = kx < 36 ? 3 * kx : 100000, ds = pc < GU_NPIECE ? gu_pdead(pc) : 100000;
    bool takep = ds <= dx;
    int st = kx < 36 ? (m * GU_PACE) / 2 : last + 5;
    if (takep && st < gu_pearly(pc)) { if (kx < 36) { takep = false; } else st = gu_pearly(pc); }
    if (st < last) st = last;
    if (takep) push(1, pc++, st); else push(0, kx++, st);
    last = st; ++m;
  }
  s.n = n;
  return s;
}
constexpr GuSched GUS = gu_make_sched();
constexpr int GU_LAG = GU_LAG_;
constexpr int gu_cnt_raw(int step) { int c = 0; for (int i = 0; i < GUS.n; ++i) if (GUS.step[i] < step) ++c; return c; }
constexpr int gu_cnt_before(int step) { return gu_cnt_raw(step - GU_LAG < 0 ? 0 : step - GU_LAG); }
constexpr int gu_idx(int kind, int arg) { for (int i = 0; i < GUS.n; ++i) if (GUS.kind[i] == kind && GUS.arg[i] == arg) return i; return -1; }
constexpr int gu_clamp(int n) { return n > 63 ? 63 : (n < 0 ? 0 : n); }
constexpr int gu_wait_for(int idx, int step) { return gu_clamp(gu_cnt_before(step) - idx - 1); }
constexpr int gu_last_piece(int st) { int p = (st + 1) * GU_STAGE / 8 - 1; return gu_idx(1, p < GU_NPIECE ? p : GU_NPIECE - 1); }
constexpr bool gu_sched_ok() {
  for (int i = 0; i < GUS.n; ++i) {
    if (GUS.kind[i] == 1) { if (GUS.step[i] + GU_LAG >= gu_pdead(GUS.arg[i]) && gu_pdead(GUS.arg[i]) > 0) return false;
                            if (gu_pdead(GUS.arg[i]) == 0 && GUS.step[i] >= 0) return false;
                            if (gu_pearly(GUS.arg[i]) > 0 && GUS.step[i] < gu_pearly(GUS.arg[i]) - 2) return false; }
    if (GUS.kind[i] == 0 && GUS.step[i] >= 0 && GUS.step[i] + GU_LAG >= 3 * GUS.arg[i]) return false;
    if (i > 0 && GUS.step[i] < GUS.step[i - 1]) return false;
  }
  return true;
}
static_assert(gu_sched_ok(), "GU schedule");
static_assert(GU_PRO_P >= GU_STAGE / 8, "stage 0 must be requested in the prologue");

DI void gstore16_wt(char* sbase, unsigned voff, u32x4 v) {
  asm volatile("global_store_dwordx4 %0, %1, %2 sc1\n\ts_nop 1" ::"v"(voff), "v"(v), "s"(sbase) : "memory");
}

struct GuCtx {
  const char* wsrc; const char* xs; const char* sp; unsigned l16, sb, so; int w, grp, h; unsigned rd0, rd1, rd2; float c1, c3, cinv; char* hbase; unsigned hvoff; int fb0;
};
template <int N, int G> DI void gu_issue(const GuCtx& c, u32x4 (&X)[37], u32x4 (&sv)[2]) {
  constexpr int kind = GUS.kind[N], arg = GUS.arg[N];
  if constexpr (kind == 0) {
    if constexpr (G < 0) gload16<0>(X[arg], c.xs + arg * 1024, c.l16);
    else gload16_g<G>(X[arg], c.xs + arg * 1024, c.l16, c.grp);
  }
  if constexpr (kind == 1) {
    constexpr int piece = arg * 8;
    if constexpr (G < 0) glds16(c.wsrc + piece * 1024, c.l16, c.sb + (piece % GU_RING) * 1024 + c.w * 1024);
    else glds16_g<G>(c.wsrc + piece * 1024, c.l16, c.sb + (piece % GU_RING) * 1024 + c.w * 1024, c.grp);
  }
  if constexpr (kind == 2) gload16<16 * arg>(sv[arg], c.sp, c.so);
}
template <int N, int STEP, int G> DI void gu_issue_step(const GuCtx& c, u32x4 (&X)[37], u32x4 (&sv)[2]) {
  if constexpr (N < GUS.n) {
    if constexpr (GUS.step[N] == STEP) gu_issue<N, G>(c, X, sv);
    if constexpr (GUS.step[N] <= STEP) gu_issue_step<N + 1, STEP, G>(c, X, sv);
  }
}
constexpr int gu_epi_start(int j) { int a = GU_P1 + 2 + 24 * j, e = (j < 3 ? GU_P1 : GU_P1 + 37 * (j - 2)) + 2; return a > e ? a : e; }
constexpr int gu_epi_block(int I) { for (int j = 0; j < 8; ++j) if (I >= gu_epi_start(j) && I < gu_epi_start(j) + 24) return j; return -1; }
struct GuEpi { float x, ab; unsigned pk[4]; float olo; };
template <int E> DI void gu_epi_0(const f32x16& a, const GuCtx& c, GuEpi& e) {
  float g = a[E];
  e.x = g * __builtin_fmaf(g * g, c.c3, c.c1);
}
template <int E> DI void gu_epi_1(const f32x16& a, const GuCtx& c, GuEpi& e) {
  float ex = __builtin_amdgcn_exp2f(e.x);
  e.ab = a[E] * a[E + 8];
  e.x = __builtin_fmaf(ex, c.cinv, c.cinv);
}
template <int E> DI void gu_epi_2(const GuCtx& c, GuEpi& e, int fb) {
  float o = e.ab * __builtin_amdgcn_rcpf(e.x);
  if constexpr ((E & 1) == 0) e.olo = o; else e.pk[E >> 1] = pack2(e.olo, o);
  if constexpr (E == 7) gstore16_wt(c.hbase + (size_t)(fb >> 1) * 4096 + (fb & 1) * 512, c.hvoff, u32x4{e.pk[0], e.pk[1], e.pk[2], e.pk[3]});
}
template <int I>
DI void gu_steps(GuCtx& c, u32x4 (&A)[GU_PD], u32x4 (&X)[37], u32x4 (&sv)[2], f32x16 (&acc)[4], GuEpi& ep) {
  if constexpr ((I + GU_PD) % GU_STAGE == 0 && (I + GU_PD) / GU_STAGE < GU_NST) {
    constexpr int s = (I + GU_PD) / GU_STAGE;
    waitv<gu_wait_for(gu_last_piece(s), I)>();
    bar();
  }
  constexpr int blk = I < GU_P1 ? I % 3 : 3 + (I - GU_P1) / 37;
  constexpr int ks = I < GU_P1 ? I / 3 : (I - GU_P1) % 37;
  constexpr int slot = blk & 3;
  if constexpr (I < 108 && I % 3 == 0 && ks >= 2) waitv_pin<gu_wait_for(gu_idx(0, ks), I)>(X[ks]);
  gu_issue_step<gu_cnt_raw(I), I, 0>(c, X, sv);
  if constexpr (I >= GU_LAG) gu_issue_step<gu_cnt_raw(I - GU_LAG), I - GU_LAG, 1>(c, X, sv);
  if constexpr (I >= GU_P1 && ks == 0) {
    f32x16 z;
#pragma unroll
    for (int j = 0; j < 16; ++j) z[j] = 0.f;
    acc[slot] = mfma32(A[I % GU_PD], X[ks], z);
  } else {
    acc[slot] = mfma32(A[I % GU_PD], X[ks], acc[slot]);
  }
  if constexpr (I + GU_PD < GU_NFRAG) {
    constexpr int fr = (I + GU_PD) % GU_RING;
    A[I % GU_PD] = lds_rd((fr < 64 ? c.rd0 : (fr < 128 ? c.rd1 : c.rd2)) + (fr & 63) * 1024);
  }
  __builtin_amdgcn_sched_barrier(0);
  {
    constexpr int j = gu_epi_block(I);
    if constexpr (j >= 0) {
      constexpr int d = I - gu_epi_start(j);
      if constexpr (d % 3 == 0) gu_epi_0<d / 3>(acc[j & 3], c, ep);
      if constexpr (d % 3 == 1) gu_epi_1<d / 3>(acc[j & 3], c, ep);
      if constexpr (d % 3 == 2) gu_epi_2<d / 3>(c, ep, c.fb0 + j);
    }
  }
  __builtin_amdgcn_sched_barrier(0);
  if constexpr (I + 1 < GU_NFRAG) gu_steps<I + 1>(c, A, X, sv, acc, ep);
}

__global__ __launch_bounds__(512, 2) void k_gu(const char* __restrict__ xf, const char* __restrict__ st, const char* __restrict__ wguf,
                                               char* __restrict__ hf LAYARG) {
  __shared__ __attribute__((aligned(16))) char smem[GU_RING * 1024];
  const int tid = threadIdx.x, lane = tid & 63;
  const int w = __builtin_amdgcn_readfirstlane(tid >> 6);
  const int b = blockIdx.x, xcd = b & 7, idx = b >> 3;
  const int tt = idx & 7, fs = 4 * xcd + (idx >> 3);
  const int tb32 = 8 * tt + w;
  const unsigned sb = lds_addr(smem), l16 = lane * 16;
  const int h = lane >> 5, r = lane & 31;

  STAMP(3, 0);
  GuCtx c;
  c.wsrc = wguf + (size_t)fs * GU_NFRAG * 1024 + w * 1024;
  c.xs = xf + (size_t)tb32 * 36 * 1024;
  c.sp = st + (size_t)(tb32 * 32) * 32; c.so = r * 32;
  c.l16 = l16; c.sb = sb; c.w = w; c.grp = w >> 2; c.h = h; c.fb0 = 9 * fs;
  c.c1 = 0.f; c.c3 = 0.f; c.cinv = 0.f;
  u32x4 sv[2], X[37];
  gu_issue_step<0, -1, -1>(c, X, sv);
#pragma unroll
  for (int k = GU_PRO_X; k < 36; ++k) asm volatile("" : "=v"(X[k]));
  STAMP(3, 1);
  f32x16 acc[4];
#pragma unroll
  for (int i = 0; i < 3; ++i)
#pragma unroll
    for (int j = 0; j < 16; ++j) acc[i][j] = 0.f;
  {
    asm volatile("s_waitcnt vmcnt(%2)" : "+v"(sv[0]), "+v"(sv[1]) : "n"(gu_wait_for(gu_idx(2, 1), 0)));
    float m0 = asf(sv[0][0]), q0 = asf(sv[0][1]), m1 = asf(sv[0][2]), q1 = asf(sv[0][3]);
    float m2 = asf(sv[1][0]), q2 = asf(sv[1][1]), m3 = asf(sv[1][2]), q3 = asf(sv[1][3]);
    float mean = 0.25f * ((m0 + m1) + (m2 + m3));
    float d0 = m0 - mean, d1 = m1 - mean, d2 = m2 - mean, d3 = m3 - mean;
    float M2 = (q0 + q1) + (q2 + q3) + 144.f * (d0 * d0 + d1 * d1 + d2 * d2 + d3 * d3);
    c.cinv = M2 * (1.f / 576.f) + 1e-5f;
    float rstd = rsqrtf(c.cinv);
    c.c1 = -2.3022081985f * rstd;
    c.c3 = -0.1029432397f * rstd * rstd * rstd;
    X[36] = mean_frag(mean, h);
  }
  waitv<gu_wait_for(gu_last_piece(0) > gu_idx(0, 1) ? gu_last_piece(0) : gu_idx(0, 1), 0)>();
  pin(X[0]); pin(X[1]);
  bar();
  STAMP(3, 2);
  c.rd0 = sb + l16; c.rd1 = sb + l16 + 65536; c.rd2 = sb + l16 + 131072;
  asm volatile("" : "+v"(c.rd0), "+v"(c.rd1));
  if (GU_RING > 128) asm volatile("" : "+v"(c.rd2));
  c.hbase = hf + (size_t)(tb32 >> 1) * 144 * 4096;
  c.hvoff = (2 * (tb32 & 1) + (r >> 4)) * 1024 + h * 256 + (r & 15) * 16;
  u32x4 A[GU_PD];
#pragma unroll
  for (int i = 0; i < GU_PD; ++i) A[i] = lds_rd(c.rd0 + i * 1024);
  GuEpi ep;
  __builtin_amdgcn_sched_barrier(0);
  gu_steps<0>(c, A, X, sv, acc, ep);
  STAMP(3, 3);
  {
    float o[8];
#pragma unroll
    for (int e = 0; e < 8; ++e) {
      float g = acc[0][e];
      float ex = __builtin_amdgcn_exp2f(g * __builtin_fmaf(g * g, c.c3, c.c1));
      o[e] = g * acc[0][e + 8] * __builtin_amdgcn_rcpf(__builtin_fmaf(ex, c.cinv, c.cinv));
    }
    const int fb = c.fb0 + 8;
    gstore16_wt(c.hbase + (size_t)(fb >> 1) * 4096 + (fb & 1) * 512, c.hvoff,
                u32x4{pack2(o[0], o[1]), pack2(o[2], o[3]), pack2(o[4], o[5]), pack2(o[6], o[7])});
  }
  STAMP(3, 4);
}

constexpr int D_DEPTH = 6;
template <int NC, int Sx>
DI void d_load(u32x4 (&Bf)[D_DEPTH][4], u32x4 (&Af)[D_DEPTH][5], const char* hs, const char* ws_, unsigned l16) {
  constexpr int sl = Sx % D_DEPTH;
  const char* hp = hs + Sx * 4096;
  gload16<0>(Bf[sl][0], hp, l16); gload16<1024>(Bf[sl][1], hp, l16); gload16<2048>(Bf[sl][2], hp, l16); gload16<3072>(Bf[sl][3], hp, l16);
  const char* wp = ws_ + Sx * NC * 1024;
  gload16<0>(Af[sl][0], wp, l16); gload16<1024>(Af[sl][1], wp, l16); gload16<2048>(Af[sl][2], wp, l16); gload16<3072>(Af[sl][3], wp, l16);
  if constexpr (NC == 5) gload16<0>(Af[sl][4], wp + 4096, l16);
}
struct DRes { const char* x1; unsigned off[2]; u32x4 v[2][2]; };
template <int NC, int Sx>
DI void d_steps(u32x4 (&Bf)[D_DEPTH][4], u32x4 (&Af)[D_DEPTH][5], f32x4 (&acc)[4][5], const char* hs, const char* ws_, unsigned l16, DRes& rs) {
  constexpr int sl = Sx % D_DEPTH;
  constexpr int ahead = (36 - 1 - Sx) < (D_DEPTH - 1) ? (36 - 1 - Sx) : (D_DEPTH - 1);
  constexpr int n = ahead * (4 + NC);
  if constexpr (NC == 5)
    asm volatile("s_waitcnt vmcnt(%9)" : "+v"(Bf[sl][0]), "+v"(Bf[sl][1]), "+v"(Bf[sl][2]), "+v"(Bf[sl][3]), "+v"(Af[sl][0]), "+v"(Af[sl][1]),
                 "+v"(Af[sl][2]), "+v"(Af[sl][3]), "+v"(Af[sl][4]) : "n"(n));
  else
    asm volatile("s_waitcnt vmcnt(%8)" : "+v"(Bf[sl][0]), "+v"(Bf[sl][1]), "+v"(Bf[sl][2]), "+v"(Bf[sl][3]), "+v"(Af[sl][0]), "+v"(Af[sl][1]),
                 "+v"(Af[sl][2]), "+v"(Af[sl][3]) : "n"(n));
#pragma unroll
  for (int c = 0; c < NC; ++c)
#pragma unroll
    for (int t = 0; t < 4; ++t) acc[t][c] = mfma16(Af[sl][c], Bf[sl][t], acc[t][c]);
  if constexpr (Sx + D_DEPTH < 36) {
    __builtin_amdgcn_sched_barrier(0);
    d_load<NC, Sx + D_DEPTH>(Bf, Af, hs, ws_, l16);
  }
  if constexpr (Sx == 36 - D_DEPTH + 1) {
    __builtin_amdgcn_sched_barrier(0);
    gload16<0>(rs.v[0][0], rs.x1, rs.off[0]); gload16<512>(rs.v[0][1], rs.x1, rs.off[0]);
    gload16<0>(rs.v[1][0], rs.x1, rs.off[1]); gload16<512>(rs.v[1][1], rs.x1, rs.off[1]);
  }
  if constexpr (Sx + 1 < 36) d_steps<NC, Sx + 1>(Bf, Af, acc, hs, ws_, l16, rs);
}
constexpr int D_STRIDE = 84;
template <int NC, bool LAST>
DI void d_body(char* smem, const char* hf, const char* wdf, const char* x1, char* xout, char* stout, int tt, int g, int tid, int lane, int kq LAYARG) {
  const unsigned l16 = lane * 16;
  const int st = dg_start(g);
  const char* hs = hf + ((size_t)(tt * 144 + 36 * kq) * 4) * 1024;
  const char* ws_ = wdf + ((size_t)st * 144 + (size_t)36 * kq * NC) * 1024;
  u32x4 Bf[D_DEPTH][4], Af[D_DEPTH][5];
  f32x4 acc[4][5];
#pragma unroll
  for (int t = 0; t < 4; ++t)
#pragma unroll
    for (int c = 0; c < 5; ++c) acc[t][c] = f32x4{0.f, 0.f, 0.f, 0.f};
  DRes rs;
  rs.x1 = x1;
#pragma unroll
  for (int i = 0; i < 2; ++i) {
    int it = tid + 256 * i; if (it >= 64 * NC) it = tid;
    int token, kl;
    if (LAST) { token = it / NC; kl = it - token * NC; } else { token = it & 63; kl = it >> 6; }
    const int gt = tt * 64 + token, ks = st + kl;
    rs.off[i] = (((gt >> 5) * 36 + ks) * 64 + (gt & 31)) * 16;
  }
  STAMP(4, 0);
  d_load<NC, 0>(Bf, Af, hs, ws_, l16); d_load<NC, 1>(Bf, Af, hs, ws_, l16); d_load<NC, 2>(Bf, Af, hs, ws_, l16);
  d_load<NC, 3>(Bf, Af, hs, ws_, l16); d_load<NC, 4>(Bf, Af, hs, ws_, l16); d_load<NC, 5>(Bf, Af, hs, ws_, l16);
  STAMP(4, 1);
  d_steps<NC, 0>(Bf, Af, acc, hs, ws_, l16, rs);
  STAMP(4, 2);
  float* red = (float*)smem;
  {
    const int q = lane >> 4, t = lane & 15;
    float* rp = red + (size_t)kq * 64 * D_STRIDE + t * D_STRIDE + 4 * q;
#pragma unroll
    for (int tb = 0; tb < 4; ++tb)
#pragma unroll
      for (int c = 0; c < NC; ++c) *(f32x4*)(rp + tb * 16 * D_STRIDE + c * 16) = acc[tb][c];
  }
  __syncthreads();
  STAMP(4, 3);
  f32x2* sred = (f32x2*)(smem + 4 * 64 * D_STRIDE * 4);
  asm volatile("" : "+v"(rs.v[0][0]), "+v"(rs.v[0][1]), "+v"(rs.v[1][0]), "+v"(rs.v[1][1]));
#pragma unroll
  for (int ii = 0; ii < 2; ++ii) {
    const int it = tid + 256 * ii;
    if (it >= 64 * NC) break;
    int token, kl;
    if (LAST) { token = it / NC; kl = it - token * NC; } else { token = it & 63; kl = it >> 6; }
    const float* rp = red + token * D_STRIDE + kl * 16;
    float v[16];
#pragma unroll
    for (int i = 0; i < 4; ++i) {
      f32x4 a = *(const f32x4*)(rp + 4 * i), b = *(const f32x4*)(rp + 64 * D_STRIDE + 4 * i);
      f32x4 c = *(const f32x4*)(rp + 2 * 64 * D_STRIDE + 4 * i), d = *(const f32x4*)(rp + 3 * 64 * D_STRIDE + 4 * i);
#pragma unroll
      for (int j = 0; j < 4; ++j) v[4 * i + j] = (a[j] + b[j]) + (c[j] + d[j]);
    }
    const int gt = tt * 64 + token, ks = st + kl;
    const size_t xo = rs.off[ii];
    const u32x4 r0 = rs.v[ii][0], r1 = rs.v[ii][1];
    u32x4 o0, o1;
#pragma unroll
    for (int i = 0; i < 4; ++i) {
      o0[i] = pack2(bflo(r0[i]) + v[2 * i], bfhi(r0[i]) + v[2 * i + 1]);
      o1[i] = pack2(bflo(r1[i]) + v[8 + 2 * i], bfhi(r1[i]) + v[8 + 2 * i + 1]);
    }
    if (LAST) {
      char* dst = xout + ((size_t)gt * D + 16 * ks) * 2;
      *(u32x4*)dst = o0; *(u32x4*)(dst + 16) = o1;
    } else {
      *(u32x4*)(xout + xo) = o0; *(u32x4*)(xout + xo + 512) = o1;
      float s = 0.f;
#pragma unroll
      for (int i = 0; i < 4; ++i) s += bflo(o0[i]) + bfhi(o0[i]) + bflo(o1[i]) + bfhi(o1[i]);
      float m = s * (1.f / 16.f), qq = 0.f;
#pragma unroll
      for (int i = 0; i < 4; ++i) {
        float a = bflo(o0[i]) - m, b = bfhi(o0[i]) - m, c = bflo(o1[i]) - m, d = bfhi(o1[i]) - m;
        qq += a * a + b * b + c * c + d * d;
      }
      sred[kl * 64 + token] = f32x2{m, qq};
    }
  }
  STAMP(4, 4);
  if (!LAST) {
    __syncthreads();
    if (tid < 64) {
      float mm[NC], ms = 0.f, qq = 0.f;
#pragma unroll
      for (int i = 0; i < NC; ++i) { f32x2 v = sred[i * 64 + tid]; mm[i] = v[0]; ms += v[0]; qq += v[1]; }
      float mean = ms * (1.f / NC);
#pragma unroll
      for (int i = 0; i < NC; ++i) { float d = mm[i] - mean; qq += 16.f * d * d; }
      *(f32x2*)(stout + ((size_t)(tt * 64 + tid) * 8 + g) * 8) = f32x2{mean, qq};
    }
  }
}
template <bool LAST>
__global__ __launch_bounds__(256, 1) void k_down(const char* __restrict__ hf, const char* __restrict__ wdf, const char* __restrict__ x1,
                                                 char* __restrict__ xout, char* __restrict__ stout LAYARG) {
  __shared__ __attribute__((aligned(16))) char smem[4 * 64 * D_STRIDE * 4 + 5 * 64 * 8];
  const int tid = threadIdx.x, lane = tid & 63;
  const int kq = __builtin_amdgcn_readfirstlane(tid >> 6);
  const int b = blockIdx.x, xcd = b & 7, idx = b >> 3;
  const int tt = 4 * xcd + (idx & 3), g = idx >> 2;
  if (g < 4) d_body<5, LAST>(smem, hf, wdf, x1, xout, stout, tt, g, tid, lane, kq LAYVAL(lay_));
  else d_body<4, LAST>(smem, hf, wdf, x1, xout, stout, tt, g, tid, lane, kq LAYVAL(lay_));
}

#define STEP() do { } while (0)
extern "C" void kernel_launch(void* const* d_in, const int* in_sizes, int n_in, void* d_out, int out_size, void* d_ws, size_t ws_size,
                              hipStream_t stream) {
  const unsigned short* x = (const unsigned short*)d_in[0];
  const unsigned short* wqkv = (const unsigned short*)d_in[1];
  const unsigned short* wo = (const unsigned short*)d_in[2];
  const unsigned short* wg = (const unsigned short*)d_in[4];
  const unsigned short* wu = (const unsigned short*)d_in[5];
  const unsigned short* wd = (const unsigned short*)d_in[6];
  char* ws = (char*)d_ws;
  STEP();
  k_prep<<<PB_TOTAL, 512, 0, stream>>>(x, wqkv, wo, wg, wu, wd, ws);
  for (int l = 0; l < 2; ++l) {
    const char* xin = ws + (l == 0 ? OFF_XF0 : OFF_XF2);
    const char* stin = ws + (l == 0 ? OFF_ST0 : OFF_ST2);
    double* kv = (double*)(ws + OFF_KV) + (size_t)l * KV_DBL;
    STEP();
    k_attn_a<<<256, 512, 0, stream>>>(xin, stin, ws + OFF_WQKV, ws + OFF_PQF, ws + OFF_PQL, kv LAYVAL(l));
    STEP();
    k_attn_b<<<256, 576, 0, stream>>>(ws + OFF_PQF, ws + OFF_PQL, kv, ws + OFF_WO, xin, ws + OFF_XF1, ws + OFF_ST1 LAYVAL(l));
    STEP();
    k_gu<<<256, 512, 0, stream>>>(ws + OFF_XF1, ws + OFF_ST1, ws + OFF_WGU, ws + OFF_HF LAYVAL(l));
    STEP();
    if (l == 0) k_down<false><<<256, 256, 0, stream>>>(ws + OFF_HF, ws + OFF_WD, ws + OFF_XF1, ws + OFF_XF2, ws + OFF_ST2 LAYVAL(l));
    else k_down<true><<<256, 256, 0, stream>>>(ws + OFF_HF, ws + OFF_WD, ws + OFF_XF1, (char*)d_out, nullptr LAYVAL(l));
  }
}
```

```cpp
#include <hip/hip_runtime.h>
#include <hip/hip_bf16.h>
#include <stdint.h>

typedef __attribute__((ext_vector_type(8))) __bf16 bf16x8;
typedef __attribute__((ext_vector_type(2))) __bf16 bf16x2;
typedef __attribute__((ext_vector_type(2))) float f32x2;
typedef __attribute__((ext_vector_type(4))) float f32x4;
typedef __attribute__((ext_vector_type(16))) float f32x16;
typedef __attribute__((ext_vector_type(4))) unsigned u32x4;
typedef __attribute__((ext_vector_type(2))) unsigned u32x2;
#define DI __device__ __forceinline__
#define LDS_AS __attribute__((address_space(3)))

#define LAYARG
#define LAYVAL(l)
#define STAMP(kid, i) do { } while (0)
constexpr int S = 2048, D = 576, NH = 32, DH = 18, F = 4608;

constexpr size_t SZ_XF = (size_t)S * D * 2;
constexpr size_t OFF_XF0 = 0;
constexpr size_t OFF_XF1 = OFF_XF0 + SZ_XF;
constexpr size_t OFF_XF2 = OFF_XF1 + SZ_XF;
constexpr size_t OFF_ST0 = OFF_XF2 + SZ_XF;
constexpr size_t OFF_ST1 = OFF_ST0 + (size_t)S * 64;
constexpr size_t OFF_ST2 = OFF_ST1 + (size_t)S * 32;
constexpr size_t OFF_PQF = OFF_ST2 + (size_t)S * 64;
constexpr size_t OFF_PQL = OFF_PQF + (size_t)64 * 32 * 1024;
constexpr size_t KV_DBL = (size_t)NH * 576;
constexpr size_t OFF_KV = OFF_PQL + (size_t)64 * 4 * 1024;
constexpr size_t SZ_KV = 2 * KV_DBL * 8;
constexpr int KA_NFRAG = 148, GU_NFRAG = 333;
constexpr size_t OFF_WQKV = OFF_KV + SZ_KV;
constexpr size_t OFF_WO = OFF_WQKV + (size_t)16 * KA_NFRAG * 1024;
constexpr size_t OFF_WGU = OFF_WO + (size_t)36 * 18 * 1024;
constexpr size_t OFF_WD = OFF_WGU + (size_t)32 * GU_NFRAG * 1024 + 16384;
constexpr size_t OFF_HF = OFF_WD + (size_t)36 * 144 * 1024;
constexpr size_t WS_END = OFF_HF + (size_t)S * F * 2;

DI unsigned pack2(float lo, float hi) {
  f32x2 f = {lo, hi};
  bf16x2 v = __builtin_convertvector(f, bf16x2);
  return __builtin_bit_cast(unsigned, v);
}
DI float asf(unsigned u) { return __builtin_bit_cast(float, u); }
DI float bflo(unsigned u) { return __builtin_bit_cast(float, u << 16); }
DI float bfhi(unsigned u) { return __builtin_bit_cast(float, u & 0xffff0000u); }
DI float rbf(float x) { return bflo(pack2(x, 0.f) & 0xffffu); }
DI bf16x8 asbf(u32x4 v) { return __builtin_bit_cast(bf16x8, v); }
DI f32x16 mfma32(u32x4 a, u32x4 b, f32x16 c) { return __builtin_amdgcn_mfma_f32_32x32x16_bf16(asbf(a), asbf(b), c, 0, 0, 0); }
DI f32x4 mfma16(u32x4 a, u32x4 b, f32x4 c) { return __builtin_amdgcn_mfma_f32_16x16x32_bf16(asbf(a), asbf(b), c, 0, 0, 0); }
DI unsigned lds_addr(const void* p) { return (unsigned)(size_t)(LDS_AS const char*)p; }

DI void glds16(const void* sbase, unsigned voff, unsigned dst) {
  unsigned keep;
  asm volatile("s_mov_b32 %0, m0\n\ts_mov_b32 m0, %3\n\ts_nop 0\n\tglobal_load_lds_dwordx4 %1, %2\n\ts_mov_b32 m0, %0"
               : "=&s"(keep) : "v"(voff), "s"(sbase), "s"(dst) : "memory");
}
template <int IMM> DI void gload16(u32x4& dst, const void* sbase, unsigned voff) {
  asm volatile("global_load_dwordx4 %0, %1, %2 offset:%3" : "=v"(dst) : "v"(voff), "s"(sbase), "n"(IMM));
}
template <int G> DI void gload16_g(u32x4& dst, const void* sbase, unsigned voff, int grp) {
  asm volatile("s_cmp_lg_u32 %3, %4\n\ts_cbranch_scc1 .Lsk%=\n\tglobal_load_dwordx4 %0, %1, %2\n.Lsk%=:"
               : "+v"(dst) : "v"(voff), "s"(sbase), "s"(grp), "n"(G) : "scc");
}
template <int G> DI void glds16_g(const void* sbase, unsigned voff, unsigned dst, int grp) {
  unsigned keep;
  asm volatile("s_cmp_lg_u32 %4, %5\n\ts_cbranch_scc1 .Lsk%=\n\ts_mov_b32 %0, m0\n\ts_mov_b32 m0, %3\n\ts_nop 0\n\tglobal_load_lds_dwordx4 %1, %2\n\ts_mov_b32 m0, %0\n.Lsk%=:"
               : "=&s"(keep) : "v"(voff), "s"(sbase), "s"(dst), "s"(grp), "n"(G) : "memory", "scc");
}
template <int IMM> DI void gload8(u32x2& dst, const void* sbase, unsigned voff) {
  asm volatile("global_load_dwordx2 %0, %1, %2 offset:%3" : "=v"(dst) : "v"(voff), "s"(sbase), "n"(IMM));
}
DI float sum_x16(float x) {
  auto r = __builtin_amdgcn_permlane16_swap(__builtin_bit_cast(unsigned, x), __builtin_bit_cast(unsigned, x), false, false);
  return __builtin_bit_cast(float, (unsigned)r[0]) + __builtin_bit_cast(float, (unsigned)r[1]);
}
DI float sum_x32(float x) {
  auto r = __builtin_amdgcn_permlane32_swap(__builtin_bit_cast(unsigned, x), __builtin_bit_cast(unsigned, x), false, false);
  return __builtin_bit_cast(float, (unsigned)r[0]) + __builtin_bit_cast(float, (unsigned)r[1]);
}
template <int N> DI void waitv() { asm volatile("s_waitcnt vmcnt(%0)" ::"n"(N) : "memory"); }
template <int N> DI void waitv_pin(u32x4& x) { asm volatile("s_waitcnt vmcnt(%1)" : "+v"(x) : "n"(N)); }
DI void pin(u32x4& x) { asm volatile("" : "+v"(x)); }
DI u32x4 lds_rd(unsigned addr) { return *(const LDS_AS u32x4*)(size_t)addr; }
DI void bar() { asm volatile("" ::: "memory"); __builtin_amdgcn_s_barrier(); asm volatile("" ::: "memory"); }

__host__ __device__ constexpr int dg_start(int g) { return g < 4 ? 5 * g : 20 + 4 * (g - 4); }
__host__ __device__ constexpr int dg_count(int g) { return g < 4 ? 5 : 4; }

#define PLD4(p) __builtin_nontemporal_load(p)
#define PLD2(p) __builtin_nontemporal_load(p)
DI void st16_wt(void* p, u32x4 v) { asm volatile("global_store_dwordx4 %0, %1, off sc1\n\ts_nop 1" ::"v"(p), "v"(v) : "memory"); }
DI void st16_nt(void* p, u32x4 v) { __builtin_nontemporal_store(v, (u32x4*)p); }
#define RST4(p, v) PST4(p, v)
#define PST4(p, v) (*(u32x4*)(p) = (v))
typedef __attribute__((ext_vector_type(4))) short s16x4;
DI u32x2 lds_tr(unsigned addr) {
  return __builtin_bit_cast(u32x2, __builtin_amdgcn_ds_read_tr16_b64_v4i16((LDS_AS s16x4*)(size_t)addr));
}
DI float dot_ones(unsigned u, float acc) {
  return __builtin_amdgcn_fdot2_f32_bf16(__builtin_bit_cast(bf16x2, u), __builtin_bit_cast(bf16x2, 0x3f803f80u), acc, false);
}
DI void put_colsum(char* chunk0, int kq, float cs) {
  unsigned p = pack2(-cs, 0.f) & 0xffffu;
  float hi = bflo(p);
  unsigned lo = pack2(-cs - hi, 0.f) & 0xffffu;
#pragma unroll
  for (int t = 0; t < 3; ++t) {
    int sl = 3 * kq + t;
    *(unsigned short*)(chunk0 + (sl >> 3) * 512 + (sl & 7) * 2) = (unsigned short)(t == 1 ? lo : p);
  }
  if (kq == 3) *(u32x2*)(chunk0 + 512 + 8) = u32x2{0u, 0u};
}
__host__ __device__ constexpr int gu_frag_index(int blk, int ks) { return blk < 3 ? 3 * ks + blk : 111 + 37 * (blk - 3) + ks; }

constexpr int P_RS = 144;
struct RtRegs { u32x4 v[5]; };
DI void rt_load(int u, int tid, const unsigned short* wg, const unsigned short* wu, const unsigned short* wd, RtRegs& R) {
  if (u < 576) {
    const int m = u & 1, kq = (u >> 1) & 3, fq = u >> 3;
    const unsigned short* wsrc = m ? wu : wg;
#pragma unroll
    for (int i = 0; i < 5; ++i) {
      int ch = tid + 256 * i; if (ch > 1151) ch = 1151;
      R.v[i] = PLD4((const u32x4*)(wsrc + (size_t)(144 * kq + (ch >> 3)) * F + 64 * fq + 8 * (ch & 7)));
    }
  } else {
    const int ub = u - 576, cgp = ub % 9, sg = ub / 9;
#pragma unroll
    for (int i = 0; i < 4; ++i) {
      int ch = tid + 256 * i;
      R.v[i] = PLD4((const u32x4*)(wd + (size_t)(128 * sg + (ch >> 3)) * D + 64 * cgp + 8 * (ch & 7)));
    }
    R.v[4] = R.v[3];
  }
}
DI void rt_stage(int u, int tid, char* sm, const RtRegs& R) {
  const int nch = u < 576 ? 1152 : 1024;
#pragma unroll
  for (int i = 0; i < 5; ++i) {
    int ch = tid + 256 * i;
    if (ch < nch) *(u32x4*)(sm + (ch >> 3) * P_RS + (ch & 7) * 16) = R.v[i];
  }
}
DI void rt_emit(int u, int tid, char* sm, char* ws) {
  const int lane = tid & 63, w = tid >> 6;
  const unsigned sb = lds_addr(sm);
  if (u < 576) {
    const int m = u & 1, kq = (u >> 1) & 3, fq = u >> 3;
    const int fb = 4 * fq + w, fs = fb / 9, blk = fb - 9 * fs;
    const int g = lane >> 4, h = g & 1, kpar = g >> 1, qq = (lane >> 2) & 3, pp = lane & 3, i16 = lane & 15;
    const int pq = (pp >> 1) | ((pp & 1) << 1);
    const unsigned rd = sb + (16 * kpar + 8 * h + qq) * P_RS + w * 32 + pq * 8;
    char* dst = ws + OFF_WGU + (size_t)fs * GU_NFRAG * 1024 + (h * 32 + m * 16 + i16) * 16;
    float cs = 0.f;
#pragma unroll
    for (int k2 = 0; k2 < 5; ++k2) {
      const int kl = 2 * k2 + kpar, back = kl < 9 ? 0 : 16;
      u32x2 lo = lds_tr(rd + (2 * k2 * 16 - back) * P_RS), hi = lds_tr(rd + (2 * k2 * 16 + 4 - back) * P_RS);
      u32x4 o = {lo[0], lo[1], hi[0], hi[1]};
      if (kl < 9) {
        cs = dot_ones(o[0], cs); cs = dot_ones(o[1], cs); cs = dot_ones(o[2], cs); cs = dot_ones(o[3], cs);
        RST4(dst + (size_t)gu_frag_index(blk, 9 * kq + kl) * 1024, o);
      }
    }
    cs += __shfl_xor(cs, 16); cs += __shfl_xor(cs, 32);
    if (lane < 16) put_colsum(ws + OFF_WGU + ((size_t)fs * GU_NFRAG + gu_frag_index(blk, 36)) * 1024 + (m * 16 + lane) * 16, kq, cs);
  } else {
    const int ub = u - 576, cgp = ub % 9, sg = ub / 9;
    const int c = 4 * cgp + w;
    const int g = c < 20 ? c / 5 : 4 + (c - 20) / 4;
    const int st = dg_start(g), n = dg_count(g);
    const int q = lane >> 4, qq = (lane >> 2) & 3, pp = lane & 3;
    const unsigned rd = sb + (8 * q + qq) * P_RS + w * 32 + pp * 8;
#pragma unroll
    for (int sl = 0; sl < 4; ++sl) {
      u32x2 lo = lds_tr(rd + sl * 32 * P_RS), hi = lds_tr(rd + (sl * 32 + 4) * P_RS);
      size_t frag = (size_t)st * 144 + (size_t)(4 * sg + sl) * n + (c - st);
      RST4(ws + OFF_WD + frag * 1024 + lane * 16, (u32x4{lo[0], lo[1], hi[0], hi[1]}));
    }
  }
}

constexpr int PB_WQKV = 64, PB_RT = 450, PB_X = 128, PB_WO = 18, PB_Z = 9;
constexpr int PB_TOTAL = PB_WQKV + PB_RT + PB_X + PB_WO + PB_Z;

__global__ __launch_bounds__(512) void k_prep(const unsigned short* __restrict__ x, const unsigned short* __restrict__ wqkv,
                                              const unsigned short* __restrict__ wo, const unsigned short* __restrict__ wg,
                                              const unsigned short* __restrict__ wu, const unsigned short* __restrict__ wd,
                                              char* __restrict__ ws) {
  __shared__ __attribute__((aligned(16))) char smem[2 * 576 * 40];
  int b = blockIdx.x;
  const int tid = threadIdx.x, lane = tid & 63, w8 = tid >> 6;
  if (b < PB_WQKV) {
    constexpr int RS = 224;
    const int p = b >> 2, kq = b & 3;
    u32x2 v[8];
#pragma unroll
    for (int i = 0; i < 8; ++i) {
      int ch = tid + 512 * i; if (ch > 3887) ch = 3887;
      int row = ch / 27, rem = ch - 27 * row, m = rem / 9, c4 = rem - 9 * m;
      v[i] = PLD2((const u32x2*)(wqkv + (size_t)(144 * kq + row) * (3 * D) + 576 * m + 36 * p + 4 * c4));
    }
    const int blk = w8 & 3, kh = w8 >> 2, h = lane >> 5, rho = lane & 31;
    int col = 108;
    if (blk == 0) col = 36 + rho;
    else if (blk == 1) {
      if (rho < 18) col = 72 + rho;
      else if (rho >= 19 && rho <= 22) col = 36 + 32 + (rho - 19);
      else if (rho >= 23 && rho <= 26) col = 18 * ((rho - 23) >> 1) + 16 + ((rho - 23) & 1);
    } else if (blk == 2) { if (rho < 18) col = 72 + 18 + rho; }
    else col = 18 * (rho >> 4) + 8 * ((rho >> 2) & 1) + 4 * ((rho >> 3) & 1) + (rho & 3);
#pragma unroll
    for (int i = 0; i < 8; ++i) {
      int ch = tid + 512 * i;
      if (ch < 3888) { int row = ch / 27, rem = ch - 27 * row; *(u32x2*)(smem + row * RS + rem * 8) = v[i]; }
    }
    if (tid < 144) *(u32x2*)(smem + tid * RS + 216) = u32x2{0u, 0u};
    __syncthreads();
    const unsigned short* src = (const unsigned short*)(smem + (80 * kh + 8 * h) * RS) + col;
    char* dst = ws + OFF_WQKV + (size_t)p * KA_NFRAG * 1024 + blk * 1024 + lane * 16;
    float cs = 0.f;
#pragma unroll
    for (int kl = 0; kl < 5; ++kl) {
      if (5 * kh + kl < 9) {
        u32x4 o;
#pragma unroll
        for (int i = 0; i < 4; ++i)
          o[i] = (unsigned)src[(kl * 16 + 2 * i) * (RS / 2)] | ((unsigned)src[(kl * 16 + 2 * i + 1) * (RS / 2)] << 16);
        cs = dot_ones(o[0], cs); cs = dot_ones(o[1], cs); cs = dot_ones(o[2], cs); cs = dot_ones(o[3], cs);
        PST4(dst + (size_t)(9 * kq + 5 * kh + kl) * 4096, o);
      }
    }
    cs += __shfl_xor(cs, 32);
    float* cx = (float*)(smem + 144 * RS);
    if (kh == 1 && lane < 32) cx[blk * 32 + lane] = cs;
    __syncthreads();
    if (kh == 0 && lane < 32) put_colsum(ws + OFF_WQKV + ((size_t)p * KA_NFRAG + 144 + blk) * 1024 + lane * 16, kq, cs + cx[blk * 32 + lane]);
    return;
  }
  b -= PB_WQKV;
  if (b < PB_RT) {
    const int half = tid >> 8, t = tid & 255, u = 2 * b + half;
    char* sm = smem + half * (144 * P_RS);
    RtRegs R;
    rt_load(u, t, wg, wu, wd, R);
    rt_stage(u, t, sm, R);
    __syncthreads();
    rt_emit(u, t, sm, ws);
    return;
  }
  b -= PB_RT;
  if (b < PB_X) {
    const int ks = lane < 36 ? lane : 35;
    u32x4 v0[2], v1[2];
#pragma unroll
    for (int i = 0; i < 2; ++i) {
      const u32x4* src = (const u32x4*)(x + (size_t)(b * 16 + w8 * 2 + i) * D + 16 * ks);
      v0[i] = PLD4(src); v1[i] = PLD4(src + 1);
    }
#pragma unroll
    for (int i = 0; i < 2; ++i) {
      const int token = b * 16 + w8 * 2 + i, tb = token >> 5, r = token & 31;
      if (lane < 36) {
        char* dst = ws + OFF_XF0 + ((size_t)(tb * 36 + ks) * 64 + r) * 16;
        PST4(dst, v0[i]); PST4(dst + 512, v1[i]);
      }
      float s = 0.f;
#pragma unroll
      for (int j = 0; j < 4; ++j) s += bflo(v0[i][j]) + bfhi(v0[i][j]) + bflo(v1[i][j]) + bfhi(v1[i][j]);
      float m16 = s * (1.f / 16.f), q = 0.f;
#pragma unroll
      for (int j = 0; j < 4; ++j) {
        float a = bflo(v0[i][j]) - m16, c = bfhi(v0[i][j]) - m16, d = bflo(v1[i][j]) - m16, e = bfhi(v1[i][j]) - m16;
        q += a * a + c * c + d * d + e * e;
      }
      int g = lane & 7, st = dg_start(g), n = dg_count(g);
      float ms = 0.f, mm[5], qq = 0.f;
#pragma unroll
      for (int j = 0; j < 5; ++j) {
        int sl = st + (j < n ? j : 0);
        mm[j] = __shfl(m16, sl); float qi = __shfl(q, sl);
        if (j < n) { ms += mm[j]; qq += qi; }
      }
      float mean = ms / n;
#pragma unroll
      for (int j = 0; j < 5; ++j) if (j < n) { float d = mm[j] - mean; qq += 16.f * d * d; }
      if (lane < 8) *(f32x2*)(ws + OFF_ST0 + ((size_t)token * 8 + lane) * 8) = f32x2{mean, qq};
    }
    return;
  }
  b -= PB_X;
  if (b < PB_WO) {
    constexpr int RS = 40;
    const int half = tid >> 8, t = tid & 255, w = w8 & 3, c = 2 * b + half;
    char* sm = smem + half * (576 * RS);
    u32x4 v[5];
#pragma unroll
    for (int i = 0; i < 5; ++i) {
      int ch = t + 256 * i; if (ch > 1151) ch = 1151;
      v[i] = PLD4((const u32x4*)(wo + (size_t)(ch >> 1) * D + 16 * c + 8 * (ch & 1)));
    }
#pragma unroll
    for (int i = 0; i < 5; ++i) {
      int ch = t + 256 * i;
      if (ch < 1152) { char* d = sm + (ch >> 1) * RS + (ch & 1) * 16; *(u32x2*)d = u32x2{v[i][0], v[i][1]}; *(u32x2*)(d + 8) = u32x2{v[i][2], v[i][3]}; }
    }
    __syncthreads();
    const unsigned sb = lds_addr(sm);
    const int q = lane >> 4, qq = (lane >> 2) & 3, pp = lane & 3;
    for (int s = w; s < 18; s += 4) {
      int r0, r1;
      if (s < 16) { r0 = (2 * s + (q >> 1)) * 18 + 8 * (q & 1) + qq; r1 = r0 + 4; }
      else { int w0 = 4 * (s - 16) + (q >> 1); r0 = (4 * w0 + qq) * 18 + 16 + (q & 1); r1 = (4 * (w0 + 2) + qq) * 18 + 16 + (q & 1); }
      u32x2 lo = lds_tr(sb + r0 * RS + pp * 8), hi = lds_tr(sb + r1 * RS + pp * 8);
      PST4(ws + OFF_WO + ((size_t)c * 18 + s) * 1024 + lane * 16, (u32x4{lo[0], lo[1], hi[0], hi[1]}));
    }
    return;
  }
  b -= PB_WO;
#pragma unroll
  for (int i = 0; i < 4; ++i) {
    size_t o = (((size_t)b * 4 + i) * 512 + tid) * 16;
    if (o < SZ_KV) PST4(ws + OFF_KV + o, (u32x4{0, 0, 0, 0}));
  }
}

DI u32x4 mean_frag(float mean, int h) {
  unsigned mh = pack2(mean, 0.f) & 0xffffu;
  unsigned ml = pack2(mean - bflo(mh), 0.f) & 0xffffu;
  unsigned hh = mh | (mh << 16), lh = ml | (mh << 16), hl = mh | (ml << 16);
  return h == 0 ? u32x4{hh, lh, hl, hh} : u32x4{lh, hl, 0u, 0u};
}

DI float phi2(float a, float rs2) {
  const float t = a * rs2;
  return __builtin_fmaf(fmaxf(t, 0.f), 0.6931471805599453f, __builtin_amdgcn_exp2f(fminf(t, 0.f)) + 1e-6f);
}
constexpr int KA_PRO = 12;
constexpr int KA_RING = 37;
constexpr int KA_PD = 8;
constexpr int ka_kv_row(int e) { return e < 16 ? (e & 3) + 4 * (e >> 3) + 8 * ((e >> 2) & 1) : (e == 16 ? 16 : (e == 17 ? 20 : 17)); }
template <int KS>
DI void ka_steps(u32x4 (&X)[37], u32x4 (&A)[KA_PD], f32x16 (&acc)[4], const char* xs, unsigned l16, unsigned rd0, unsigned rd1, float mean, int h) {
  if constexpr (KS == 36) X[36] = mean_frag(mean, h);
  if constexpr (KS % 4 == 0 && KS > 0 && KS <= 32) bar();
  if constexpr (KS < 36) {
    constexpr int issued = KS + KA_PRO < 36 ? KS + KA_PRO : 36;
    waitv_pin<issued - KS - 1>(X[KS]);
    if constexpr (KS + KA_PRO < 36) gload16<0>(X[KS + KA_PRO], xs + (KS + KA_PRO) * 1024, l16);
  }
#pragma unroll
  for (int blk = 0; blk < 4; ++blk) {
    const int i = 4 * KS + blk;
    u32x4 a = A[i % KA_PD];
    if (blk < 3) acc[blk] = mfma32(X[KS], a, acc[blk]);
    else acc[3] = mfma32(a, X[KS], acc[3]);
    if (i + KA_PD < KA_NFRAG) { const int f = (i + KA_PD) % (4 * KA_RING); A[i % KA_PD] = lds_rd((f < 64 ? rd0 : (f < 128 ? rd1 : rd1 + 65536)) + (f & 63) * 1024); }
    __builtin_amdgcn_sched_barrier(0);
  }
  if constexpr (KS + 1 < 37) ka_steps<KS + 1>(X, A, acc, xs, l16, rd0, rd1, mean, h);
}
constexpr int ka_to(int G) { int t = G == 0 ? 9 : 4 * G + 16; if (G > 0 && t > 4 * (G - 1) + KA_RING) t = 4 * (G - 1) + KA_RING; return t < 37 ? t : 37; }
struct KaFoTable { short v[256][8]; };
constexpr KaFoTable ka_make_fo() {
  KaFoTable T{};
  for (int t = 0; t < 256; ++t)
    for (int i = 0; i < 8; ++i) {
      const int it = t + 256 * i;
      int val = -1;
      if (i < 5 && it < 1152) {
        const int hs = it >= 576, sl = it - 576 * hs;
        int row = 0, d = 0;
        if (sl < 512) { row = (sl >> 1) & 31; d = 8 * ((sl >> 6) & 1) + 2 * (sl >> 7) + (sl & 1); }
        else { row = sl & 31; d = 16 + ((sl >> 5) & 1); }
        const int e = row < 16 ? (row & 3) + 4 * (row >> 3) + 8 * ((row >> 2) & 1) : (row == 16 ? 16 : (row == 20 ? 17 : (row == 17 ? 18 : -1)));
        if (e >= 0) val = hs == 0 ? d * 33 + e : (d < 14 ? (18 + d) * 33 + e : (32 + d - 14) * 33 + e);
      }
      T.v[t][i] = (short)val;
    }
  return T;
}
__device__ __attribute__((aligned(16))) const KaFoTable KA_FO = ka_make_fo();
template <int G, int GEND> DI void ka_loader(const char* wsrc, unsigned l16, unsigned dst) {
  constexpr int need = 4 * G + 7 < 36 ? 4 * G + 7 : 36;
  constexpr int from = G == 0 ? 0 : ka_to(G - 1);
  constexpr int to = ka_to(G);
  static_assert(to > need, "loader schedule");
#pragma unroll
  for (int ks = from; ks < to; ++ks) glds16(wsrc + ks * 4096, l16, dst + (4 * (ks % KA_RING)) * 1024);
  waitv<to - 1 - need>();
  bar();
  if constexpr (G < GEND) ka_loader<G + 1, GEND>(wsrc, l16, dst);
}

__global__ __launch_bounds__(512, 2) void k_attn_a(const char* xf, const char* st, const char* wqkvf, char* pqf, char* pql, double* kv LAYARG) {
  __shared__ __attribute__((aligned(16))) char smem[KA_RING * 4096];
  asm volatile("" : "+s"(xf), "+s"(st), "+s"(wqkvf), "+s"(pqf), "+s"(pql), "+s"(kv));
  const int tid = threadIdx.x, lane = tid & 63;
  const int w8 = __builtin_amdgcn_readfirstlane(tid >> 6);
  const int w = w8 & 3;
  const int b = blockIdx.x, xcd = b & 7, idx = b >> 3;
  const int tt = 2 * xcd + (idx & 1), p = idx >> 1;
  const int tb = 4 * tt + w;
  const unsigned sb = lds_addr(smem);
  const unsigned l16 = lane * 16;
  const int h = lane >> 5, r = lane & 31;
  STAMP(1, 0);
  if (w8 >= 4) {
    const char* wsrc = wqkvf + (size_t)p * KA_NFRAG * 1024 + w * 1024;
    u32x4 fot;
    gload16<0>(fot, &KA_FO, (tid - 256) * 16);
    ka_loader<0, 8>(wsrc, l16, sb + w * 1024);
    asm volatile("" : "+v"(fot));
    int fo[5] = {(short)(fot[0] & 0xffff), (short)(fot[0] >> 16), (short)(fot[1] & 0xffff), (short)(fot[1] >> 16), (short)(fot[2] & 0xffff)};
    bar();
    bar();
    float sum[5];
#pragma unroll
    for (int i = 0; i < 5; ++i) {
      const float* rp = (const float*)smem + (fo[i] >= 0 ? fo[i] : 0);
      sum[i] = (rp[0] + rp[1280]) + (rp[2560] + rp[3840]);
    }
#pragma unroll
    for (int i = 0; i < 5; ++i)
      if (fo[i] >= 0) unsafeAtomicAdd(kv + (size_t)(2 * p) * 576 + (tid - 256) + 256 * i, (double)sum[i]);
  } else {
    u32x4 sv[4], X[37];
    const char* xs = xf + (size_t)tb * 36 * 1024;
    gload16<0>(X[0], xs, l16); gload16<0>(X[1], xs + 1024, l16);
    {
      const char* sp = st + (size_t)(tb * 32) * 64;
      unsigned so = r * 64;
      gload16<0>(sv[0], sp, so); gload16<16>(sv[1], sp, so); gload16<32>(sv[2], sp, so); gload16<48>(sv[3], sp, so);
    }
#pragma unroll
    for (int k = 2; k < KA_PRO; ++k) gload16<0>(X[k], xs + k * 1024, l16);
    STAMP(1, 1);
    float rstd, mean;
    {
      asm volatile("s_waitcnt vmcnt(%4)" : "+v"(sv[0]), "+v"(sv[1]), "+v"(sv[2]), "+v"(sv[3]) : "n"(KA_PRO - 2));
      float m[8], q[8];
#pragma unroll
      for (int i = 0; i < 4; ++i) {
        m[2 * i] = asf(sv[i][0]); q[2 * i] = asf(sv[i][1]);
        m[2 * i + 1] = asf(sv[i][2]); q[2 * i + 1] = asf(sv[i][3]);
      }
      mean = (80.f * (m[0] + m[1] + m[2] + m[3]) + 64.f * (m[4] + m[5] + m[6] + m[7])) * (1.f / 576.f);
      float M2 = 0.f;
#pragma unroll
      for (int i = 0; i < 8; ++i) { float d = m[i] - mean; M2 += q[i] + (i < 4 ? 80.f : 64.f) * d * d; }
      rstd = rsqrtf(M2 * (1.f / 576.f) + 1e-5f);
    }
    f32x16 acc[4];
#pragma unroll
    for (int i = 0; i < 4; ++i)
#pragma unroll
      for (int j = 0; j < 16; ++j) acc[i][j] = 0.f;
    unsigned rd0 = sb + l16, rd1 = rd0 + 65536;
    asm volatile("" : "+v"(rd0), "+v"(rd1));
    bar();
    STAMP(1, 2);
    u32x4 A[KA_PD];
#pragma unroll
    for (int i = 0; i < KA_PD; ++i) A[i] = lds_rd(rd0 + i * 1024);
    __builtin_amdgcn_sched_barrier(0);
    ka_steps<0>(X, A, acc, xs, l16, rd0, rd1, mean, h);
    STAMP(1, 3);
    bar();
    STAMP(1, 6);

    const int c = r;
    float* rs = (float*)(smem + 32768) + w * 160;
    float* ql = rs + 32;
    if (h == 0) rs[r] = rstd;
    f32x4 rr[4];
#pragma unroll
    for (int g = 0; g < 4; ++g) rr[g] = *(const f32x4*)(rs + 8 * g + 4 * h);
    u32x4 k1[2], v1[2], v2[2];
    f32x4 pv[4];
    const bool ph = c >= 19 && c <= 26, one = c == 18;
#pragma unroll
    for (int s = 0; s < 2; ++s)
#pragma unroll
      for (int t = 0; t < 4; ++t) {
        const int r0 = 8 * s + 2 * t;
        const float s0 = rr[r0 >> 2][r0 & 3], s1 = rr[r0 >> 2][(r0 & 3) + 1];
        const float e0 = s0 * 1.4426950408889634f, e1 = s1 * 1.4426950408889634f;
        k1[s][t] = pack2(phi2(acc[0][r0], e0), phi2(acc[0][r0 + 1], e1));
        float a0 = acc[1][r0] * s0, a1 = acc[1][r0 + 1] * s1;
        float p0 = phi2(acc[1][r0], e0), p1 = phi2(acc[1][r0 + 1], e1);
        a0 = ph ? p0 : (one ? 1.f : a0); a1 = ph ? p1 : (one ? 1.f : a1);
        pv[r0 >> 2][r0 & 3] = p0; pv[r0 >> 2][(r0 & 3) + 1] = p1;
        v1[s][t] = pack2(a0, a1);
        v2[s][t] = one ? 0x3f803f80u : pack2(acc[2][r0] * s0, acc[2][r0 + 1] * s1);
      }
    if (c >= 23 && c <= 26) {
#pragma unroll
      for (int g = 0; g < 4; ++g) *(f32x4*)(ql + (c - 23) * 32 + 8 * g + 4 * h) = pv[g];
    }
    STAMP(1, 7);
    f32x16 R1, R2, R3;
#pragma unroll
    for (int j = 0; j < 16; ++j) { R1[j] = 0.f; R2[j] = 0.f; R3[j] = 0.f; }
#pragma unroll
    for (int s = 0; s < 2; ++s) {
      R1 = mfma32(k1[s], v1[s], R1);
      R2 = mfma32(k1[s], v2[s], R2);
      R3 = mfma32(v1[s], v2[s], R3);
    }
    float* red = (float*)smem + w * 1280;
#pragma unroll
    for (int j = 0; j < 16; ++j) {
      int row = (j & 3) + 8 * (j >> 2) + 4 * h;
      red[row * 33 + c] = row < 18 ? R1[j] : R2[j];
    }
    if (h == 0) red[32 * 33 + c] = R3[11];
    else { red[33 * 33 + c] = R3[8]; red[34 * 33 + c] = R3[9]; red[35 * 33 + c] = R3[10]; }
    STAMP(1, 4);
    asm volatile("s_waitcnt lgkmcnt(0)" ::: "memory");
    bar();
    {
      u32x4 qa, qb;
      const float rse = rstd * 1.4426950408889634f;
#pragma unroll
      for (int t = 0; t < 4; ++t) {
        qa[t] = pack2(phi2(acc[3][2 * t], rse), phi2(acc[3][2 * t + 1], rse));
        qb[t] = pack2(phi2(acc[3][8 + 2 * t], rse), phi2(acc[3][8 + 2 * t + 1], rse));
      }
      char* dst = pqf + ((size_t)tb * 32 + 2 * p) * 1024 + l16;
      *(u32x4*)dst = qa; *(u32x4*)(dst + 1024) = qb;
      float la = ql[h * 32 + r], lb = ql[(2 + h) * 32 + r];
      *(unsigned*)(pql + ((size_t)tb * 4 + (p >> 2)) * 1024 + l16 + 4 * (p & 3)) = pack2(la, lb);
    }
  }
}

__global__ __launch_bounds__(576) void k_attn_b(const char* pqf, const char* pql, const double* kv, const char* wof, const char* xin, char* xout,
                                                char* stout LAYARG) {
  __shared__ __attribute__((aligned(16))) char smem[36 * 1024 + 9 * 32 * 8];
  asm volatile("" : "+s"(pqf), "+s"(pql), "+s"(kv), "+s"(wof), "+s"(xin), "+s"(xout), "+s"(stout));
  const int tid = threadIdx.x, lane = tid & 63;
  const int w = __builtin_amdgcn_readfirstlane(tid >> 6);
  const int b = blockIdx.x, xcd = b & 7, idx = b >> 3;
  const int tb = 8 * xcd + (idx & 7), cg = idx >> 3;
  const int ct = 9 * cg + w;
  const int h = lane >> 5, r = lane & 31, q = lane >> 4, t = lane & 15;

  STAMP(2, 0);
  u32x4 A[18];
  const char* wp = wof + (size_t)ct * 18 * 1024;
  const unsigned l16 = lane * 16;
  u32x2 res[2];
  const unsigned xo = ((tb * 36 + ct) * 64 + (q >> 1) * 32 + t) * 16 + (q & 1) * 8;
#define KB_LDA(s_) gload16<((s_) & 3) * 1024>(A[s_], wp + ((s_) >> 2) * 4096, l16)

  if (w < 8) {
    const bool valid = r < 18 || r == 20 || r == 21;
    const int rr = valid ? (r == 21 ? 17 : r) : 0;
    const unsigned kvo = (h * 32 + rr) * 16, klo = (h * 32 + rr) * 8;
    u32x4 kd[4][4];
    u32x2 kl[4];
    u32x4 bq[4], bl;
    __builtin_amdgcn_s_setprio(3);
    gload16<0>(bl, pql + ((size_t)tb * 4 + (w >> 1)) * 1024, l16);
#pragma unroll
    for (int i = 0; i < 4; ++i) {
      const char* src = (const char*)(kv + (size_t)(4 * w + i) * 576);
      gload16<0>(kd[i][0], src, kvo); gload16<1024>(kd[i][1], src, kvo); gload16<2048>(kd[i][2], src, kvo); gload16<3072>(kd[i][3], src, kvo);
      gload8<0>(kl[i], src + 4096, klo);
      gload16<0>(bq[i], pqf + ((size_t)tb * 32 + 4 * w + i) * 1024, l16);
    }
    __builtin_amdgcn_s_setprio(0);
    STAMP(2, 1);
    float lo[4];
#pragma unroll
    for (int i = 0; i < 4; ++i) {
      if (i == 0) asm volatile("s_waitcnt vmcnt(18)" : "+v"(bl), "+v"(kd[0][0]), "+v"(kd[0][1]), "+v"(kd[0][2]), "+v"(kd[0][3]), "+v"(kl[0]), "+v"(bq[0]));
      if (i == 1) asm volatile("s_waitcnt vmcnt(17)" : "+v"(kd[1][0]), "+v"(kd[1][1]), "+v"(kd[1][2]), "+v"(kd[1][3]), "+v"(kl[1]), "+v"(bq[1]));
      if (i == 2) asm volatile("s_waitcnt vmcnt(16)" : "+v"(kd[2][0]), "+v"(kd[2][1]), "+v"(kd[2][2]), "+v"(kd[2][3]), "+v"(kl[2]), "+v"(bq[2]));
      if (i == 3) asm volatile("s_waitcnt vmcnt(14)" : "+v"(kd[3][0]), "+v"(kd[3][1]), "+v"(kd[3][2]), "+v"(kd[3][3]), "+v"(kl[3]), "+v"(bq[3]));
      u32x4 a1, a2;
#pragma unroll
      for (int k = 0; k < 4; ++k) {
        double d0 = __builtin_bit_cast(double, u32x2{kd[i][k][0], kd[i][k][1]});
        double d1 = __builtin_bit_cast(double, u32x2{kd[i][k][2], kd[i][k][3]});
        a1[k] = valid ? pack2((float)d0, (float)d1) : 0u;
      }
      const float klf = (float)__builtin_bit_cast(double, kl[i]);
      const unsigned lv = valid ? ((i & 1) ? pack2(0.f, klf) : pack2(klf, 0.f)) : 0u;
      __builtin_amdgcn_sched_barrier(0);
      if (i == 0) { KB_LDA(0); KB_LDA(1); KB_LDA(2); KB_LDA(3); KB_LDA(4); }
      if (i == 1) { KB_LDA(5); KB_LDA(6); KB_LDA(7); KB_LDA(8); KB_LDA(9); }
      if (i == 2) { KB_LDA(10); KB_LDA(11); KB_LDA(12); KB_LDA(13); }
      if (i == 3) { KB_LDA(14); KB_LDA(15); KB_LDA(16); KB_LDA(17); gload8<0>(res[0], xin, xo); gload8<256>(res[1], xin, xo); }
      const int dw = 2 * (w & 1) + (i >> 1);
#pragma unroll
      for (int k = 0; k < 4; ++k) a2[k] = (k == dw) ? lv : 0u;
      f32x16 n;
#pragma unroll
      for (int j = 0; j < 16; ++j) n[j] = 0.f;
      n = mfma32(a1, bq[i], n);
      n = mfma32(a2, bl, n);
      float rd = __builtin_amdgcn_rcpf(n[9] + 1e-6f);
      u32x4 o;
#pragma unroll
      for (int k = 0; k < 4; ++k) o[k] = pack2(n[2 * k] * rd, n[2 * k + 1] * rd);
      lo[i] = n[8] * rd;
      const int head = 4 * w + i, s = head >> 1, qq = 2 * (head & 1) + h;
      *(u32x4*)(smem + (((r >> 4) * 18 + s) * 64 + qq * 16 + (r & 15)) * 16) = o;
    }
    {
      const int s = 16 + (w >> 2), qq = 2 * (w & 1) + h, half = (w >> 1) & 1;
      *(u32x2*)(smem + (((r >> 4) * 18 + s) * 64 + qq * 16 + (r & 15)) * 16 + half * 8) = u32x2{pack2(lo[0], lo[1]), pack2(lo[2], lo[3])};
    }
  } else {
    KB_LDA(0); KB_LDA(1); KB_LDA(2); KB_LDA(3); KB_LDA(4); KB_LDA(5); KB_LDA(6); KB_LDA(7); KB_LDA(8);
    KB_LDA(9); KB_LDA(10); KB_LDA(11); KB_LDA(12); KB_LDA(13); KB_LDA(14); KB_LDA(15); KB_LDA(16); KB_LDA(17);
    gload8<0>(res[0], xin, xo); gload8<256>(res[1], xin, xo);
  }
  STAMP(2, 2);
  asm volatile("s_waitcnt lgkmcnt(0)" ::: "memory");
  bar();
  STAMP(2, 3);
  f32x4 acc[2] = {{0.f, 0.f, 0.f, 0.f}, {0.f, 0.f, 0.f, 0.f}};
#pragma unroll
  for (int s = 0; s < 18; ++s) {
    if (s == 0) asm volatile("s_waitcnt vmcnt(15)" : "+v"(A[0]), "+v"(A[1]), "+v"(A[2]), "+v"(A[3]), "+v"(A[4]));
    if (s == 5) asm volatile("s_waitcnt vmcnt(10)" : "+v"(A[5]), "+v"(A[6]), "+v"(A[7]), "+v"(A[8]), "+v"(A[9]));
    if (s == 10) asm volatile("s_waitcnt vmcnt(6)" : "+v"(A[10]), "+v"(A[11]), "+v"(A[12]), "+v"(A[13]));
    if (s == 14) asm volatile("s_waitcnt vmcnt(2)" : "+v"(A[14]), "+v"(A[15]), "+v"(A[16]), "+v"(A[17]));
    u32x4 b0 = *(const u32x4*)(smem + (s * 64 + lane) * 16);
    u32x4 b1 = *(const u32x4*)(smem + ((18 + s) * 64 + lane) * 16);
    acc[0] = mfma16(A[s], b0, acc[0]);
    acc[1] = mfma16(A[s], b1, acc[1]);
  }
  STAMP(2, 4);
  asm volatile("s_waitcnt vmcnt(0)" : "+v"(res[0]), "+v"(res[1]));
  f32x2* sred = (f32x2*)(smem + 36 * 1024);
#pragma unroll
  for (int tbb = 0; tbb < 2; ++tbb) {
    float v0 = bflo(res[tbb][0]) + acc[tbb][0], v1 = bfhi(res[tbb][0]) + acc[tbb][1];
    float v2 = bflo(res[tbb][1]) + acc[tbb][2], v3 = bfhi(res[tbb][1]) + acc[tbb][3];
    u32x2 o = {pack2(v0, v1), pack2(v2, v3)};
    *(u32x2*)(xout + xo + tbb * 256) = o;
    v0 = bflo(o[0]); v1 = bfhi(o[0]); v2 = bflo(o[1]); v3 = bfhi(o[1]);
    float s = sum_x32(sum_x16((v0 + v1) + (v2 + v3)));
    float m = s * (1.f / 16.f);
    float d0 = v0 - m, d1 = v1 - m, d2 = v2 - m, d3 = v3 - m;
    float qq = sum_x32(sum_x16(d0 * d0 + d1 * d1 + d2 * d2 + d3 * d3));
    if (q == 0) sred[w * 32 + tbb * 16 + t] = f32x2{m, qq};
  }
  asm volatile("s_waitcnt lgkmcnt(0)" ::: "memory");
  bar();
  if (tid < 32) {
    float mm[9], ms = 0.f, qq = 0.f;
#pragma unroll
    for (int i = 0; i < 9; ++i) { f32x2 v = sred[i * 32 + tid]; mm[i] = v[0]; ms += v[0]; qq += v[1]; }
    float mean = ms * (1.f / 9.f);
#pragma unroll
    for (int i = 0; i < 9; ++i) { float d = mm[i] - mean; qq += 16.f * d * d; }
    *(f32x2*)(stout + ((size_t)(tb * 32 + tid) * 4 + cg) * 8) = f32x2{mean, qq};
  }
  STAMP(2, 5);
}

#define GU_STAGE_ 32
#define GU_RING_ 128
constexpr int GU_STAGE = GU_STAGE_, GU_RING = GU_RING_, GU_P1 = 111;
constexpr int GU_PD = 5;
constexpr int GU_NSLOT = GU_RING / GU_STAGE, GU_NPIECE = (GU_NFRAG + 7) / 8;
constexpr int GU_NST = (GU_NFRAG + GU_STAGE - 1) / GU_STAGE;
struct GuSched { int n; int kind[96]; int arg[96]; int step[96]; };
#define GU_PRO_X_ 10
#define GU_PRO_P_ (GU_STAGE_ / 8 + 2)
#define GU_PACE 5
#define GU_LAG_ 1
constexpr int GU_PRO_X = GU_PRO_X_;
constexpr int GU_PRO_P = GU_PRO_P_;
constexpr int gu_pstage(int p) { return 8 * p / GU_STAGE; }
constexpr int gu_pdead(int p) { int st = gu_pstage(p); return st == 0 ? 0 : st * GU_STAGE - GU_PD; }
constexpr int gu_pearly(int p) { int st = gu_pstage(p); return st < GU_NSLOT ? 0 : (st - GU_NSLOT + 2) * GU_STAGE - GU_PD + 2; }
constexpr GuSched gu_make_sched() {
  GuSched s{};
  int n = 0;
  auto push = [&](int k, int a, int st) { s.kind[n] = k; s.arg[n] = a; s.step[n] = st; ++n; };
  push(2, 0, -1); push(2, 1, -1);
  int kx = 0, pc = 0;
  while (kx < GU_PRO_X || pc < GU_PRO_P) {
    bool takep = pc < GU_PRO_P && (kx >= GU_PRO_X || (pc < GU_STAGE / 8 ? 8 * pc <= 12 * kx : 8 * pc <= 3 * kx));
    if (takep) push(1, pc++, -1); else push(0, kx++, -1);
  }
  int m = 0, last = 0;
  while (kx < 36 || pc < GU_NPIECE) {
    int dx = kx < 36 ? 3 * kx : 100000, ds = pc < GU_NPIECE ? gu_pdead(pc) : 100000;
    bool takep = ds <= dx;
    int st = kx < 36 ? (m * GU_PACE) / 2 : last + 5;
    if (takep && st < gu_pearly(pc)) { if (kx < 36) { takep = false; } else st = gu_pearly(pc); }
    if (st < last) st = last;
    if (takep) push(1, pc++, st); else push(0, kx++, st);
    last = st; ++m;
  }
  s.n = n;
  return s;
}
constexpr GuSched GUS = gu_make_sched();
constexpr int GU_LAG = GU_LAG_;
constexpr int gu_cnt_raw(int step) { int c = 0; for (int i = 0; i < GUS.n; ++i) if (GUS.step[i] < step) ++c; return c; }
constexpr int gu_cnt_before(int step) { return gu_cnt_raw(step - GU_LAG < 0 ? 0 : step - GU_LAG); }
constexpr int gu_idx(int kind, int arg) { for (int i = 0; i < GUS.n; ++i) if (GUS.kind[i] == kind && GUS.arg[i] == arg) return i; return -1; }
constexpr int gu_clamp(int n) { return n > 63 ? 63 : (n < 0 ? 0 : n); }
constexpr int gu_wait_for(int idx, int step) { return gu_clamp(gu_cnt_before(step) - idx - 1); }
constexpr int gu_last_piece(int st) { int p = (st + 1) * GU_STAGE / 8 - 1; return gu_idx(1, p < GU_NPIECE ? p : GU_NPIECE - 1); }
constexpr bool gu_sched_ok() {
  for (int i = 0; i < GUS.n; ++i) {
    if (GUS.kind[i] == 1) { if (GUS.step[i] + GU_LAG >= gu_pdead(GUS.arg[i]) && gu_pdead(GUS.arg[i]) > 0) return false;
                            if (gu_pdead(GUS.arg[i]) == 0 && GUS.step[i] >= 0) return false;
                            if (gu_pearly(GUS.arg[i]) > 0 && GUS.step[i] < gu_pearly(GUS.arg[i]) - 2) return false; }
    if (GUS.kind[i] == 0 && GUS.step[i] >= 0 && GUS.step[i] + GU_LAG >= 3 * GUS.arg[i]) return false;
    if (i > 0 && GUS.step[i] < GUS.step[i - 1]) return false;
  }
  return true;
}
static_assert(gu_sched_ok(), "GU schedule");
static_assert(GU_PRO_P >= GU_STAGE / 8, "stage 0 must be requested in the prologue");

DI void gstore16_wt(char* sbase, unsigned voff, u32x4 v) {
  asm volatile("global_store_dwordx4 %0, %1, %2 sc1\n\ts_nop 1" ::"v"(voff), "v"(v), "s"(sbase) : "memory");
}

struct GuCtx {
  const char* wsrc; const char* xs; const char* sp; unsigned l16, sb, so; int w, grp, h; unsigned rd0, rd1, rd2; float c1, c3, cinv; char* hbase; unsigned hvoff; int fb0;
};
template <int N, int G> DI void gu_issue(const GuCtx& c, u32x4 (&X)[37], u32x4 (&sv)[2]) {
  constexpr int kind = GUS.kind[N], arg = GUS.arg[N];
  if constexpr (kind == 0) {
    if constexpr (G < 0) gload16<0>(X[arg], c.xs + arg * 1024, c.l16);
    else gload16_g<G>(X[arg], c.xs + arg * 1024, c.l16, c.grp);
  }
  if constexpr (kind == 1) {
    constexpr int piece = arg * 8;
    if constexpr (G < 0) glds16(c.wsrc + piece * 1024, c.l16, c.sb + (piece % GU_RING) * 1024 + c.w * 1024);
    else glds16_g<G>(c.wsrc + piece * 1024, c.l16, c.sb + (piece % GU_RING) * 1024 + c.w * 1024, c.grp);
  }
  if constexpr (kind == 2) gload16<16 * arg>(sv[arg], c.sp, c.so);
}
template <int N, int STEP, int G> DI void gu_issue_step(const GuCtx& c, u32x4 (&X)[37], u32x4 (&sv)[2]) {
  if constexpr (N < GUS.n) {
    if constexpr (GUS.step[N] == STEP) gu_issue<N, G>(c, X, sv);
    if constexpr (GUS.step[N] <= STEP) gu_issue_step<N + 1, STEP, G>(c, X, sv);
  }
}
constexpr int gu_epi_start(int j) { int a = GU_P1 + 2 + 24 * j, e = (j < 3 ? GU_P1 : GU_P1 + 37 * (j - 2)) + 2; return a > e ? a : e; }
constexpr int gu_epi_block(int I) { for (int j = 0; j < 8; ++j) if (I >= gu_epi_start(j) && I < gu_epi_start(j) + 24) return j; return -1; }
struct GuEpi { float x, ab; unsigned pk[4]; float olo; };
template <int E> DI void gu_epi_0(const f32x16& a, const GuCtx& c, GuEpi& e) {
  float g = a[E];
  e.x = g * __builtin_fmaf(g * g, c.c3, c.c1);
}
template <int E> DI void gu_epi_1(const f32x16& a, const GuCtx& c, GuEpi& e) {
  float ex = __builtin_amdgcn_exp2f(e.x);
  e.ab = a[E] * a[E + 8];
  e.x = __builtin_fmaf(ex, c.cinv, c.cinv);
}
template <int E> DI void gu_epi_2(const GuCtx& c, GuEpi& e, int fb) {
  float o = e.ab * __builtin_amdgcn_rcpf(e.x);
  if constexpr ((E & 1) == 0) e.olo = o; else e.pk[E >> 1] = pack2(e.olo, o);
  if constexpr (E == 7) gstore16_wt(c.hbase + (size_t)(fb >> 1) * 4096 + (fb & 1) * 512, c.hvoff, u32x4{e.pk[0], e.pk[1], e.pk[2], e.pk[3]});
}
template <int I>
DI void gu_steps(GuCtx& c, u32x4 (&A)[GU_PD], u32x4 (&X)[37], u32x4 (&sv)[2], f32x16 (&acc)[4], GuEpi& ep) {
  if constexpr ((I + GU_PD) % GU_STAGE == 0 && (I + GU_PD) / GU_STAGE < GU_NST) {
    constexpr int s = (I + GU_PD) / GU_STAGE;
    waitv<gu_wait_for(gu_last_piece(s), I)>();
    bar();
  }
  constexpr int blk = I < GU_P1 ? I % 3 : 3 + (I - GU_P1) / 37;
  constexpr int ks = I < GU_P1 ? I / 3 : (I - GU_P1) % 37;
  constexpr int slot = blk & 3;
  if constexpr (I < 108 && I % 3 == 0 && ks >= 2) waitv_pin<gu_wait_for(gu_idx(0, ks), I)>(X[ks]);
  gu_issue_step<gu_cnt_raw(I), I, 0>(c, X, sv);
  if constexpr (I >= GU_LAG) gu_issue_step<gu_cnt_raw(I - GU_LAG), I - GU_LAG, 1>(c, X, sv);
  if constexpr (I >= GU_P1 && ks == 0) {
    f32x16 z;
#pragma unroll
    for (int j = 0; j < 16; ++j) z[j] = 0.f;
    acc[slot] = mfma32(A[I % GU_PD], X[ks], z);
  } else {
    acc[slot] = mfma32(A[I % GU_PD], X[ks], acc[slot]);
  }
  if constexpr (I + GU_PD < GU_NFRAG) {
    constexpr int fr = (I + GU_PD) % GU_RING;
    A[I % GU_PD] = lds_rd((fr < 64 ? c.rd0 : (fr < 128 ? c.rd1 : c.rd2)) + (fr & 63) * 1024);
  }
  __builtin_amdgcn_sched_barrier(0);
  {
    constexpr int j = gu_epi_block(I);
    if constexpr (j >= 0) {
      constexpr int d = I - gu_epi_start(j);
      if constexpr (d % 3 == 0) gu_epi_0<d / 3>(acc[j & 3], c, ep);
      if constexpr (d % 3 == 1) gu_epi_1<d / 3>(acc[j & 3], c, ep);
      if constexpr (d % 3 == 2) gu_epi_2<d / 3>(c, ep, c.fb0 + j);
    }
  }
  __builtin_amdgcn_sched_barrier(0);
  if constexpr (I + 1 < GU_NFRAG) gu_steps<I + 1>(c, A, X, sv, acc, ep);
}

__global__ __launch_bounds__(512, 2) void k_gu(const char* __restrict__ xf, const char* __restrict__ st, const char* __restrict__ wguf,
                                               char* __restrict__ hf LAYARG) {
  __shared__ __attribute__((aligned(16))) char smem[GU_RING * 1024];
  const int tid = threadIdx.x, lane = tid & 63;
  const int w = __builtin_amdgcn_readfirstlane(tid >> 6);
  const int b = blockIdx.x, xcd = b & 7, idx = b >> 3;
  const int tt = idx & 7, fs = 4 * xcd + (idx >> 3);
  const int tb32 = 8 * tt + w;
  const unsigned sb = lds_addr(smem), l16 = lane * 16;
  const int h = lane >> 5, r = lane & 31;

  STAMP(3, 0);
  GuCtx c;
  c.wsrc = wguf + (size_t)fs * GU_NFRAG * 1024 + w * 1024;
  c.xs = xf + (size_t)tb32 * 36 * 1024;
  c.sp = st + (size_t)(tb32 * 32) * 32; c.so = r * 32;
  c.l16 = l16; c.sb = sb; c.w = w; c.grp = w >> 2; c.h = h; c.fb0 = 9 * fs;
  c.c1 = 0.f; c.c3 = 0.f; c.cinv = 0.f;
  u32x4 sv[2], X[37];
  gu_issue_step<0, -1, -1>(c, X, sv);
#pragma unroll
  for (int k = GU_PRO_X; k < 36; ++k) asm volatile("" : "=v"(X[k]));
  STAMP(3, 1);
  f32x16 acc[4];
#pragma unroll
  for (int i = 0; i < 3; ++i)
#pragma unroll
    for (int j = 0; j < 16; ++j) acc[i][j] = 0.f;
  {
    asm volatile("s_waitcnt vmcnt(%2)" : "+v"(sv[0]), "+v"(sv[1]) : "n"(gu_wait_for(gu_idx(2, 1), 0)));
    float m0 = asf(sv[0][0]), q0 = asf(sv[0][1]), m1 = asf(sv[0][2]), q1 = asf(sv[0][3]);
    float m2 = asf(sv[1][0]), q2 = asf(sv[1][1]), m3 = asf(sv[1][2]), q3 = asf(sv[1][3]);
    float mean = 0.25f * ((m0 + m1) + (m2 + m3));
    float d0 = m0 - mean, d1 = m1 - mean, d2 = m2 - mean, d3 = m3 - mean;
    float M2 = (q0 + q1) + (q2 + q3) + 144.f * (d0 * d0 + d1 * d1 + d2 * d2 + d3 * d3);
    c.cinv = M2 * (1.f / 576.f) + 1e-5f;
    float rstd = rsqrtf(c.cinv);
    c.c1 = -2.3022081985f * rstd;
    c.c3 = -0.1029432397f * rstd * rstd * rstd;
    X[36] = mean_frag(mean, h);
  }
  waitv<gu_wait_for(gu_last_piece(0) > gu_idx(0, 1) ? gu_last_piece(0) : gu_idx(0, 1), 0)>();
  pin(X[0]); pin(X[1]);
  bar();
  STAMP(3, 2);
  c.rd0 = sb + l16; c.rd1 = sb + l16 + 65536; c.rd2 = sb + l16 + 131072;
  asm volatile("" : "+v"(c.rd0), "+v"(c.rd1));
  if (GU_RING > 128) asm volatile("" : "+v"(c.rd2));
  c.hbase = hf + (size_t)(tb32 >> 1) * 144 * 4096;
  c.hvoff = (2 * (tb32 & 1) + (r >> 4)) * 1024 + h * 256 + (r & 15) * 16;
  u32x4 A[GU_PD];
#pragma unroll
  for (int i = 0; i < GU_PD; ++i) A[i] = lds_rd(c.rd0 + i * 1024);
  GuEpi ep;
  __builtin_amdgcn_sched_barrier(0);
  gu_steps<0>(c, A, X, sv, acc, ep);
  STAMP(3, 3);
  {
    float o[8];
#pragma unroll
    for (int e = 0; e < 8; ++e) {
      float g = acc[0][e];
      float ex = __builtin_amdgcn_exp2f(g * __builtin_fmaf(g * g, c.c3, c.c1));
      o[e] = g * acc[0][e + 8] * __builtin_amdgcn_rcpf(__builtin_fmaf(ex, c.cinv, c.cinv));
    }
    const int fb = c.fb0 + 8;
    gstore16_wt(c.hbase + (size_t)(fb >> 1) * 4096 + (fb & 1) * 512, c.hvoff,
                u32x4{pack2(o[0], o[1]), pack2(o[2], o[3]), pack2(o[4], o[5]), pack2(o[6], o[7])});
  }
  STAMP(3, 4);
}

constexpr int D_DEPTH = 6;
template <int NC, int Sx>
DI void d_load(u32x4 (&Bf)[D_DEPTH][4], u32x4 (&Af)[D_DEPTH][5], const char* hs, const char* ws_, unsigned l16) {
  constexpr int sl = Sx % D_DEPTH;
  const char* hp = hs + Sx * 4096;
  gload16<0>(Bf[sl][0], hp, l16); gload16<1024>(Bf[sl][1], hp, l16); gload16<2048>(Bf[sl][2], hp, l16); gload16<3072>(Bf[sl][3], hp, l16);
  const char* wp = ws_ + Sx * NC * 1024;
  gload16<0>(Af[sl][0], wp, l16); gload16<1024>(Af[sl][1], wp, l16); gload16<2048>(Af[sl][2], wp, l16); gload16<3072>(Af[sl][3], wp, l16);
  if constexpr (NC == 5) gload16<0>(Af[sl][4], wp + 4096, l16);
}
struct DRes { const char* x1; unsigned off[2]; u32x4 v[2][2]; };
template <int NC, int Sx>
DI void d_steps(u32x4 (&Bf)[D_DEPTH][4], u32x4 (&Af)[D_DEPTH][5], f32x4 (&acc)[4][5], const char* hs, const char* ws_, unsigned l16, DRes& rs) {
  constexpr int sl = Sx % D_DEPTH;
  constexpr int ahead = (36 - 1 - Sx) < (D_DEPTH - 1) ? (36 - 1 - Sx) : (D_DEPTH - 1);
  constexpr int n = ahead * (4 + NC);
  if constexpr (NC == 5)
    asm volatile("s_waitcnt vmcnt(%9)" : "+v"(Bf[sl][0]), "+v"(Bf[sl][1]), "+v"(Bf[sl][2]), "+v"(Bf[sl][3]), "+v"(Af[sl][0]), "+v"(Af[sl][1]),
                 "+v"(Af[sl][2]), "+v"(Af[sl][3]), "+v"(Af[sl][4]) : "n"(n));
  else
    asm volatile("s_waitcnt vmcnt(%8)" : "+v"(Bf[sl][0]), "+v"(Bf[sl][1]), "+v"(Bf[sl][2]), "+v"(Bf[sl][3]), "+v"(Af[sl][0]), "+v"(Af[sl][1]),
                 "+v"(Af[sl][2]), "+v"(Af[sl][3]) : "n"(n));
#pragma unroll
  for (int c = 0; c < NC; ++c)
#pragma unroll
    for (int t = 0; t < 4; ++t) acc[t][c] = mfma16(Af[sl][c], Bf[sl][t], acc[t][c]);
  if constexpr (Sx + D_DEPTH < 36) {
    __builtin_amdgcn_sched_barrier(0);
    d_load<NC, Sx + D_DEPTH>(Bf, Af, hs, ws_, l16);
  }
  if constexpr (Sx == 36 - D_DEPTH + 1) {
    __builtin_amdgcn_sched_barrier(0);
    gload16<0>(rs.v[0][0], rs.x1, rs.off[0]); gload16<512>(rs.v[0][1], rs.x1, rs.off[0]);
    gload16<0>(rs.v[1][0], rs.x1, rs.off[1]); gload16<512>(rs.v[1][1], rs.x1, rs.off[1]);
  }
  if constexpr (Sx + 1 < 36) d_steps<NC, Sx + 1>(Bf, Af, acc, hs, ws_, l16, rs);
}
constexpr int D_STRIDE = 84;
template <int NC, bool LAST>
DI void d_body(char* smem, const char* hf, const char* wdf, const char* x1, char* xout, char* stout, int tt, int g, int tid, int lane, int kq LAYARG) {
  const unsigned l16 = lane * 16;
  const int st = dg_start(g);
  const char* hs = hf + ((size_t)(tt * 144 + 36 * kq) * 4) * 1024;
  const char* ws_ = wdf + ((size_t)st * 144 + (size_t)36 * kq * NC) * 1024;
  u32x4 Bf[D_DEPTH][4], Af[D_DEPTH][5];
  f32x4 acc[4][5];
#pragma unroll
  for (int t = 0; t < 4; ++t)
#pragma unroll
    for (int c = 0; c < 5; ++c) acc[t][c] = f32x4{0.f, 0.f, 0.f, 0.f};
  DRes rs;
  rs.x1 = x1;
#pragma unroll
  for (int i = 0; i < 2; ++i) {
    int it = tid + 256 * i; if (it >= 64 * NC) it = tid;
    int token, kl;
    if (LAST) { token = it / NC; kl = it - token * NC; } else { token = it & 63; kl = it >> 6; }
    const int gt = tt * 64 + token, ks = st + kl;
    rs.off[i] = (((gt >> 5) * 36 + ks) * 64 + (gt & 31)) * 16;
  }
  STAMP(4, 0);
  d_load<NC, 0>(Bf, Af, hs, ws_, l16); d_load<NC, 1>(Bf, Af, hs, ws_, l16); d_load<NC, 2>(Bf, Af, hs, ws_, l16);
  d_load<NC, 3>(Bf, Af, hs, ws_, l16); d_load<NC, 4>(Bf, Af, hs, ws_, l16); d_load<NC, 5>(Bf, Af, hs, ws_, l16);
  STAMP(4, 1);
  d_steps<NC, 0>(Bf, Af, acc, hs, ws_, l16, rs);
  STAMP(4, 2);
  float* red = (float*)smem;
  {
    const int q = lane >> 4, t = lane & 15;
    float* rp = red + (size_t)kq * 64 * D_STRIDE + t * D_STRIDE + 4 * q;
#pragma unroll
    for (int tb = 0; tb < 4; ++tb)
#pragma unroll
      for (int c = 0; c < NC; ++c) *(f32x4*)(rp + tb * 16 * D_STRIDE + c * 16) = acc[tb][c];
  }
  __syncthreads();
  STAMP(4, 3);
  f32x2* sred = (f32x2*)(smem + 4 * 64 * D_STRIDE * 4);
  asm volatile("" : "+v"(rs.v[0][0]), "+v"(rs.v[0][1]), "+v"(rs.v[1][0]), "+v"(rs.v[1][1]));
#pragma unroll
  for (int ii = 0; ii < 2; ++ii) {
    const int it = tid + 256 * ii;
    if (it >= 64 * NC) break;
    int token, kl;
    if (LAST) { token = it / NC; kl = it - token * NC; } else { token = it & 63; kl = it >> 6; }
    const float* rp = red + token * D_STRIDE + kl * 16;
    float v[16];
#pragma unroll
    for (int i = 0; i < 4; ++i) {
      f32x4 a = *(const f32x4*)(rp + 4 * i), b = *(const f32x4*)(rp + 64 * D_STRIDE + 4 * i);
      f32x4 c = *(const f32x4*)(rp + 2 * 64 * D_STRIDE + 4 * i), d = *(const f32x4*)(rp + 3 * 64 * D_STRIDE + 4 * i);
#pragma unroll
      for (int j = 0; j < 4; ++j) v[4 * i + j] = (a[j] + b[j]) + (c[j] + d[j]);
    }
    const int gt = tt * 64 + token, ks = st + kl;
    const size_t xo = rs.off[ii];
    const u32x4 r0 = rs.v[ii][0], r1 = rs.v[ii][1];
    u32x4 o0, o1;
#pragma unroll
    for (int i = 0; i < 4; ++i) {
      o0[i] = pack2(bflo(r0[i]) + v[2 * i], bfhi(r0[i]) + v[2 * i + 1]);
      o1[i] = pack2(bflo(r1[i]) + v[8 + 2 * i], bfhi(r1[i]) + v[8 + 2 * i + 1]);
    }
    if (LAST) {
      char* dst = xout + ((size_t)gt * D + 16 * ks) * 2;
      *(u32x4*)dst = o0; *(u32x4*)(dst + 16) = o1;
    } else {
      *(u32x4*)(xout + xo) = o0; *(u32x4*)(xout + xo + 512) = o1;
      float s = 0.f;
#pragma unroll
      for (int i = 0; i < 4; ++i) s += bflo(o0[i]) + bfhi(o0[i]) + bflo(o1[i]) + bfhi(o1[i]);
      float m = s * (1.f / 16.f), qq = 0.f;
#pragma unroll
      for (int i = 0; i < 4; ++i) {
        float a = bflo(o0[i]) - m, b = bfhi(o0[i]) - m, c = bflo(o1[i]) - m, d = bfhi(o1[i]) - m;
        qq += a * a + b * b + c * c + d * d;
      }
      sred[kl * 64 + token] = f32x2{m, qq};
    }
  }
  STAMP(4, 4);
  if (!LAST) {
    __syncthreads();
    if (tid < 64) {
      float mm[NC], ms = 0.f, qq = 0.f;
#pragma unroll
      for (int i = 0; i < NC; ++i) { f32x2 v = sred[i * 64 + tid]; mm[i] = v[0]; ms += v[0]; qq += v[1]; }
      float mean = ms * (1.f / NC);
#pragma unroll
      for (int i = 0; i < NC; ++i) { float d = mm[i] - mean; qq += 16.f * d * d; }
      *(f32x2*)(stout + ((size_t)(tt * 64 + tid) * 8 + g) * 8) = f32x2{mean, qq};
    }
  }
}
template <bool LAST>
__global__ __launch_bounds__(256, 1) void k_down(const char* __restrict__ hf, const char* __restrict__ wdf, const char* __restrict__ x1,
                                                 char* __restrict__ xout, char* __restrict__ stout LAYARG) {
  __shared__ __attribute__((aligned(16))) char smem[4 * 64 * D_STRIDE * 4 + 5 * 64 * 8];
  const int tid = threadIdx.x, lane = tid & 63;
  const int kq = __builtin_amdgcn_readfirstlane(tid >> 6);
  const int b = blockIdx.x, xcd = b & 7, idx = b >> 3;
  const int tt = 4 * xcd + (idx & 3), g = idx >> 2;
  if (g < 4) d_body<5, LAST>(smem, hf, wdf, x1, xout, stout, tt, g, tid, lane, kq LAYVAL(lay_));
  else d_body<4, LAST>(smem, hf, wdf, x1, xout, stout, tt, g, tid, lane, kq LAYVAL(lay_));
}

#define STEP() do { } while (0)
extern "C" void kernel_launch(void* const* d_in, const int* in_sizes, int n_in, void* d_out, int out_size, void* d_ws, size_t ws_size,
                              hipStream_t stream) {
  const unsigned short* x = (const unsigned short*)d_in[0];
  const unsigned short* wqkv = (const unsigned short*)d_in[1];
  const unsigned short* wo = (const unsigned short*)d_in[2];
  const unsigned short* wg = (const unsigned short*)d_in[4];
  const unsigned short* wu = (const unsigned short*)d_in[5];
  const unsigned short* wd = (const unsigned short*)d_in[6];
  char* ws = (char*)d_ws;
  STEP();
  k_prep<<<PB_TOTAL, 512, 0, stream>>>(x, wqkv, wo, wg, wu, wd, ws);
  for (int l = 0; l < 2; ++l) {
    const char* xin = ws + (l == 0 ? OFF_XF0 : OFF_XF2);
    const char* stin = ws + (l == 0 ? OFF_ST0 : OFF_ST2);
    double* kv = (double*)(ws + OFF_KV) + (size_t)l * KV_DBL;
    STEP();
    k_attn_a<<<256, 512, 0, stream>>>(xin, stin, ws + OFF_WQKV, ws + OFF_PQF, ws + OFF_PQL, kv LAYVAL(l));
    STEP();
    k_attn_b<<<256, 576, 0, stream>>>(ws + OFF_PQF, ws + OFF_PQL, kv, ws + OFF_WO, xin, ws + OFF_XF1, ws + OFF_ST1 LAYVAL(l));
    STEP();
    k_gu<<<256, 512, 0, stream>>>(ws + OFF_XF1, ws + OFF_ST1, ws + OFF_WGU, ws + OFF_HF LAYVAL(l));
    STEP();
    if (l == 0) k_down<false><<<256, 256, 0, stream>>>(ws + OFF_HF, ws + OFF_WD, ws + OFF_XF1, ws + OFF_XF2, ws + OFF_ST2 LAYVAL(l));
    else k_down<true><<<256, 256, 0, stream>>>(ws + OFF_HF, ws + OFF_WD, ws + OFF_XF1, (char*)d_out, nullptr LAYVAL(l));
  }
}
```

```cpp
#include <hip/hip_runtime.h>
#include <hip/hip_bf16.h>
#include <stdint.h>

typedef __attribute__((ext_vector_type(8))) __bf16 bf16x8;
typedef __attribute__((ext_vector_type(2))) __bf16 bf16x2;
typedef __attribute__((ext_vector_type(2))) float f32x2;
typedef __attribute__((ext_vector_type(4))) float f32x4;
typedef __attribute__((ext_vector_type(16))) float f32x16;
typedef __attribute__((ext_vector_type(4))) unsigned u32x4;
typedef __attribute__((ext_vector_type(2))) unsigned u32x2;
#define DI __device__ __forceinline__
#define LDS_AS __attribute__((address_space(3)))

#define STAMP(kid, i) do { } while (0)
constexpr int S = 2048, D = 576, NH = 32, DH = 18, F = 4608;

constexpr size_t SZ_XF = (size_t)S * D * 2;
constexpr size_t OFF_XF0 = 0;
constexpr size_t OFF_XF2 = OFF_XF0 + SZ_XF;
constexpr size_t OFF_XF1 = OFF_XF2 + SZ_XF;
constexpr size_t OFF_ST0 = OFF_XF1 + 2 * SZ_XF;
constexpr size_t OFF_ST2 = OFF_ST0 + (size_t)S * 64;
constexpr size_t SZ_ST1 = (size_t)S * 32;
constexpr size_t OFF_ST1 = OFF_ST2 + (size_t)S * 64;
constexpr size_t SZ_PQF = (size_t)64 * 32 * 1024, SZ_PQL = (size_t)64 * 4 * 1024;
constexpr size_t OFF_PQF = OFF_ST1 + 2 * SZ_ST1;
constexpr size_t OFF_PQL = OFF_PQF + 2 * SZ_PQF;
constexpr size_t KV_DBL = (size_t)NH * 576;
constexpr size_t OFF_KV = OFF_PQL + 2 * SZ_PQL;
constexpr size_t SZ_KV = 2 * KV_DBL * 8;
constexpr size_t OFF_SYNC = OFF_KV + SZ_KV;
constexpr size_t SZ_ZERO = SZ_KV + 72 * 256;
constexpr int KA_NFRAG = 148, GU_NFRAG = 333;
constexpr size_t OFF_WQKV = OFF_KV + SZ_ZERO;
constexpr size_t OFF_WO = OFF_WQKV + (size_t)16 * KA_NFRAG * 1024;
constexpr size_t OFF_WGU = OFF_WO + (size_t)36 * 18 * 1024;
constexpr size_t OFF_WD = OFF_WGU + (size_t)32 * GU_NFRAG * 1024 + 16384;
constexpr size_t SZ_HF = (size_t)S * F * 2;
constexpr size_t OFF_HF = OFF_WD + (size_t)36 * 144 * 1024;
constexpr size_t WS_END = OFF_HF + 2 * SZ_HF;
static_assert(OFF_WQKV % 1024 == 0 && WS_END < (size_t)200 << 20, "ws map");

DI unsigned pack2(float lo, float hi) {
  f32x2 f = {lo, hi};
  bf16x2 v = __builtin_convertvector(f, bf16x2);
  return __builtin_bit_cast(unsigned, v);
}
DI float asf(unsigned u) { return __builtin_bit_cast(float, u); }
DI float bflo(unsigned u) { return __builtin_bit_cast(float, u << 16); }
DI float bfhi(unsigned u) { return __builtin_bit_cast(float, u & 0xffff0000u); }
DI float rbf(float x) { return bflo(pack2(x, 0.f) & 0xffffu); }
DI bf16x8 asbf(u32x4 v) { return __builtin_bit_cast(bf16x8, v); }
DI f32x16 mfma32(u32x4 a, u32x4 b, f32x16 c) { return __builtin_amdgcn_mfma_f32_32x32x16_bf16(asbf(a), asbf(b), c, 0, 0, 0); }
DI f32x4 mfma16(u32x4 a, u32x4 b, f32x4 c) { return __builtin_amdgcn_mfma_f32_16x16x32_bf16(asbf(a), asbf(b), c, 0, 0, 0); }
DI unsigned lds_addr(const void* p) { return (unsigned)(size_t)(LDS_AS const char*)p; }

DI void glds16(const void* sbase, unsigned voff, unsigned dst) {
  unsigned keep;
  asm volatile("s_mov_b32 %0, m0\n\ts_mov_b32 m0, %3\n\ts_nop 0\n\tglobal_load_lds_dwordx4 %1, %2\n\ts_mov_b32 m0, %0"
               : "=&s"(keep) : "v"(voff), "s"(sbase), "s"(dst) : "memory");
}
template <int IMM> DI void gload16(u32x4& dst, const void* sbase, unsigned voff) {
  asm volatile("global_load_dwordx4 %0, %1, %2 offset:%3" : "=v"(dst) : "v"(voff), "s"(sbase), "n"(IMM));
}
template <int IMM> DI void hload16(u32x4& dst, const void* sbase, unsigned voff) {
  asm volatile("global_load_dwordx4 %0, %1, %2 offset:%3 sc1" : "=v"(dst) : "v"(voff), "s"(sbase), "n"(IMM));
}
template <int IMM> DI void hload8(u32x2& dst, const void* sbase, unsigned voff) {
  asm volatile("global_load_dwordx2 %0, %1, %2 offset:%3 sc1" : "=v"(dst) : "v"(voff), "s"(sbase), "n"(IMM));
}
DI void hstore16(char* sbase, unsigned voff, u32x4 v) {
  asm volatile("s_nop 4\n\tglobal_store_dwordx4 %0, %1, %2 sc1\n\ts_nop 1" ::"v"(voff), "v"(v), "s"(sbase) : "memory");
}
DI void hstore8(char* sbase, unsigned voff, u32x2 v) {
  asm volatile("s_nop 4\n\tglobal_store_dwordx2 %0, %1, %2 sc1" ::"v"(voff), "v"(v), "s"(sbase) : "memory");
}
DI void hstore4(char* sbase, unsigned voff, unsigned v) {
  asm volatile("s_nop 4\n\tglobal_store_dword %0, %1, %2 sc1" ::"v"(voff), "v"(v), "s"(sbase) : "memory");
}
template <int G> DI void hload16_g(u32x4& dst, const void* sbase, unsigned voff, int grp) {
  asm volatile("s_cmp_lg_u32 %3, %4\n\ts_cbranch_scc1 .Lsk%=\n\tglobal_load_dwordx4 %0, %1, %2 sc1\n.Lsk%=:"
               : "+v"(dst) : "v"(voff), "s"(sbase), "s"(grp), "n"(G) : "scc");
}
template <int G> DI void glds16_g(const void* sbase, unsigned voff, unsigned dst, int grp) {
  unsigned keep;
  asm volatile("s_cmp_lg_u32 %4, %5\n\ts_cbranch_scc1 .Lsk%=\n\ts_mov_b32 %0, m0\n\ts_mov_b32 m0, %3\n\ts_nop 0\n\tglobal_load_lds_dwordx4 %1, %2\n\ts_mov_b32 m0, %0\n.Lsk%=:"
               : "=&s"(keep) : "v"(voff), "s"(sbase), "s"(dst), "s"(grp), "n"(G) : "memory", "scc");
}
template <int IMM> DI void gload8(u32x2& dst, const void* sbase, unsigned voff) {
  asm volatile("global_load_dwordx2 %0, %1, %2 offset:%3" : "=v"(dst) : "v"(voff), "s"(sbase), "n"(IMM));
}
DI float sum_x16(float x) {
  auto r = __builtin_amdgcn_permlane16_swap(__builtin_bit_cast(unsigned, x), __builtin_bit_cast(unsigned, x), false, false);
  return __builtin_bit_cast(float, (unsigned)r[0]) + __builtin_bit_cast(float, (unsigned)r[1]);
}
DI float sum_x32(float x) {
  auto r = __builtin_amdgcn_permlane32_swap(__builtin_bit_cast(unsigned, x), __builtin_bit_cast(unsigned, x), false, false);
  return __builtin_bit_cast(float, (unsigned)r[0]) + __builtin_bit_cast(float, (unsigned)r[1]);
}
template <int N> DI void waitv() { asm volatile("s_waitcnt vmcnt(%0)" ::"n"(N) : "memory"); }
template <int N> DI void waitv_pin(u32x4& x) { asm volatile("s_waitcnt vmcnt(%1)" : "+v"(x) : "n"(N)); }
DI void pin(u32x4& x) { asm volatile("" : "+v"(x)); }
DI u32x4 lds_rd(unsigned addr) { return *(const LDS_AS u32x4*)(size_t)addr; }
DI void bar() { asm volatile("" ::: "memory"); __builtin_amdgcn_s_barrier(); asm volatile("" ::: "memory"); }

__host__ __device__ constexpr int dg_start(int g) { return g < 4 ? 5 * g : 20 + 4 * (g - 4); }
__host__ __device__ constexpr int dg_count(int g) { return g < 4 ? 5 : 4; }

DI int tid_opaque() { int t = threadIdx.x; asm volatile("" : "+v"(t)); return t; }

typedef __attribute__((address_space(1))) unsigned gu32;
constexpr int SY_KA = 0, SY_KB = 16, SY_GU = 32, SY_D = 48, SY_TMO = 64;
DI gu32* sync_word(char* ws, int i) { return (gu32*)(ws + OFF_SYNC + (size_t)i * 256); }
DI void sig_done(char* ws, int i) {
  asm volatile("s_waitcnt vmcnt(0) lgkmcnt(0)" ::: "memory");
  bar();
  if (threadIdx.x == 0) __hip_atomic_fetch_add(sync_word(ws, i), 1u, __ATOMIC_RELAXED, __HIP_MEMORY_SCOPE_AGENT);
}
DI void dep_wait(char* ws, int i, int n, unsigned target) {
  if (threadIdx.x < 64) {
    const int lane = threadIdx.x;
    gu32* wd = sync_word(ws, i + (lane < n ? lane : 0));
    for (unsigned spins = 0;; ++spins) {
      const unsigned v = __hip_atomic_load(wd, __ATOMIC_RELAXED, __HIP_MEMORY_SCOPE_AGENT);
      if (__all(v >= target)) break;
      __builtin_amdgcn_s_sleep(1);
      if (spins > (1u << 20)) { if (lane == 0) __hip_atomic_store(sync_word(ws, SY_TMO), 1u, __ATOMIC_RELAXED, __HIP_MEMORY_SCOPE_AGENT); break; }
    }
  }
  bar();
}

#define PLD4(p) __builtin_nontemporal_load(p)
#define PLD2(p) __builtin_nontemporal_load(p)
DI void st16_wt(void* p, u32x4 v) { asm volatile("global_store_dwordx4 %0, %1, off sc1\n\ts_nop 1" ::"v"(p), "v"(v) : "memory"); }
DI void st16_nt(void* p, u32x4 v) { __builtin_nontemporal_store(v, (u32x4*)p); }
#define RST4(p, v) PST4(p, v)
#define PST4(p, v) (*(u32x4*)(p) = (v))
typedef __attribute__((ext_vector_type(4))) short s16x4;
DI u32x2 lds_tr(unsigned addr) {
  return __builtin_bit_cast(u32x2, __builtin_amdgcn_ds_read_tr16_b64_v4i16((LDS_AS s16x4*)(size_t)addr));
}
DI float dot_ones(unsigned u, float acc) {
  return __builtin_amdgcn_fdot2_f32_bf16(__builtin_bit_cast(bf16x2, u), __builtin_bit_cast(bf16x2, 0x3f803f80u), acc, false);
}
DI void put_colsum(char* chunk0, int kq, float cs) {
  unsigned p = pack2(-cs, 0.f) & 0xffffu;
  float hi = bflo(p);
  unsigned lo = pack2(-cs - hi, 0.f) & 0xffffu;
#pragma unroll
  for (int t = 0; t < 3; ++t) {
    int sl = 3 * kq + t;
    *(unsigned short*)(chunk0 + (sl >> 3) * 512 + (sl & 7) * 2) = (unsigned short)(t == 1 ? lo : p);
  }
  if (kq == 3) *(u32x2*)(chunk0 + 512 + 8) = u32x2{0u, 0u};
}
__host__ __device__ constexpr int gu_frag_index(int blk, int ks) { return blk < 3 ? 3 * ks + blk : 111 + 37 * (blk - 3) + ks; }

constexpr int P_RS = 144;
struct RtRegs { u32x4 v[5]; };
DI void rt_load(int u, int tid, const unsigned short* wg, const unsigned short* wu, const unsigned short* wd, RtRegs& R) {
  if (u < 576) {
    const int m = u & 1, kq = (u >> 1) & 3, fq = u >> 3;
    const unsigned short* wsrc = m ? wu : wg;
#pragma unroll
    for (int i = 0; i < 5; ++i) {
      int ch = tid + 256 * i; if (ch > 1151) ch = 1151;
      R.v[i] = PLD4((const u32x4*)(wsrc + (size_t)(144 * kq + (ch >> 3)) * F + 64 * fq + 8 * (ch & 7)));
    }
  } else {
    const int ub = u - 576, cgp = ub % 9, sg = ub / 9;
#pragma unroll
    for (int i = 0; i < 4; ++i) {
      int ch = tid + 256 * i;
      R.v[i] = PLD4((const u32x4*)(wd + (size_t)(128 * sg + (ch >> 3)) * D + 64 * cgp + 8 * (ch & 7)));
    }
    R.v[4] = R.v[3];
  }
}
DI void rt_stage(int u, int tid, char* sm, const RtRegs& R) {
  const int nch = u < 576 ? 1152 : 1024;
#pragma unroll
  for (int i = 0; i < 5; ++i) {
    int ch = tid + 256 * i;
    if (ch < nch) *(u32x4*)(sm + (ch >> 3) * P_RS + (ch & 7) * 16) = R.v[i];
  }
}
DI void rt_emit(int u, int tid, char* sm, char* ws) {
  const int lane = tid & 63, w = tid >> 6;
  const unsigned sb = lds_addr(sm);
  if (u < 576) {
    const int m = u & 1, kq = (u >> 1) & 3, fq = u >> 3;
    const int fb = 4 * fq + w, fs = fb / 9, blk = fb - 9 * fs;
    const int g = lane >> 4, h = g & 1, kpar = g >> 1, qq = (lane >> 2) & 3, pp = lane & 3, i16 = lane & 15;
    const int pq = (pp >> 1) | ((pp & 1) << 1);
    const unsigned rd = sb + (16 * kpar + 8 * h + qq) * P_RS + w * 32 + pq * 8;
    char* dst = ws + OFF_WGU + (size_t)fs * GU_NFRAG * 1024 + (h * 32 + m * 16 + i16) * 16;
    float cs = 0.f;
#pragma unroll
    for (int k2 = 0; k2 < 5; ++k2) {
      const int kl = 2 * k2 + kpar, back = kl < 9 ? 0 : 16;
      u32x2 lo = lds_tr(rd + (2 * k2 * 16 - back) * P_RS), hi = lds_tr(rd + (2 * k2 * 16 + 4 - back) * P_RS);
      u32x4 o = {lo[0], lo[1], hi[0], hi[1]};
      if (kl < 9) {
        cs = dot_ones(o[0], cs); cs = dot_ones(o[1], cs); cs = dot_ones(o[2], cs); cs = dot_ones(o[3], cs);
        RST4(dst + (size_t)gu_frag_index(blk, 9 * kq + kl) * 1024, o);
      }
    }
    cs += __shfl_xor(cs, 16); cs += __shfl_xor(cs, 32);
    if (lane < 16) put_colsum(ws + OFF_WGU + ((size_t)fs * GU_NFRAG + gu_frag_index(blk, 36)) * 1024 + (m * 16 + lane) * 16, kq, cs);
  } else {
    const int ub = u - 576, cgp = ub % 9, sg = ub / 9;
    const int c = 4 * cgp + w;
    const int g = c < 20 ? c / 5 : 4 + (c - 20) / 4;
    const int st = dg_start(g), n = dg_count(g);
    const int q = lane >> 4, qq = (lane >> 2) & 3, pp = lane & 3;
    const unsigned rd = sb + (8 * q + qq) * P_RS + w * 32 + pp * 8;
#pragma unroll
    for (int sl = 0; sl < 4; ++sl) {
      u32x2 lo = lds_tr(rd + sl * 32 * P_RS), hi = lds_tr(rd + (sl * 32 + 4) * P_RS);
      size_t frag = (size_t)st * 144 + (size_t)(4 * sg + sl) * n + (c - st);
      RST4(ws + OFF_WD + frag * 1024 + lane * 16, (u32x4{lo[0], lo[1], hi[0], hi[1]}));
    }
  }
}

constexpr int PB_WQKV = 64, PB_RT = 450, PB_X = 128, PB_WO = 18, PB_Z = 10;
constexpr int PB_TOTAL = PB_WQKV + PB_X + PB_WO + PB_Z + PB_RT;

__global__ __launch_bounds__(512) void k_prep(const unsigned short* __restrict__ x, const unsigned short* __restrict__ wqkv,
                                              const unsigned short* __restrict__ wo, const unsigned short* __restrict__ wg,
                                              const unsigned short* __restrict__ wu, const unsigned short* __restrict__ wd,
                                              char* __restrict__ ws) {
  __shared__ __attribute__((aligned(16))) char smem[2 * 576 * 40];
  int b = blockIdx.x;
  const int tid = threadIdx.x, lane = tid & 63, w8 = tid >> 6;
  if (b < PB_WQKV) {
    constexpr int RS = 224;
    const int p = b >> 2, kq = b & 3;
    u32x2 v[8];
#pragma unroll
    for (int i = 0; i < 8; ++i) {
      int ch = tid + 512 * i; if (ch > 3887) ch = 3887;
      int row = ch / 27, rem = ch - 27 * row, m = rem / 9, c4 = rem - 9 * m;
      v[i] = PLD2((const u32x2*)(wqkv + (size_t)(144 * kq + row) * (3 * D) + 576 * m + 36 * p + 4 * c4));
    }
    const int blk = w8 & 3, kh = w8 >> 2, h = lane >> 5, rho = lane & 31;
    int col = 108;
    if (blk == 0) col = 36 + rho;
    else if (blk == 1) {
      if (rho < 18) col = 72 + rho;
      else if (rho >= 19 && rho <= 22) col = 36 + 32 + (rho - 19);
      else if (rho >= 23 && rho <= 26) col = 18 * ((rho - 23) >> 1) + 16 + ((rho - 23) & 1);
    } else if (blk == 2) { if (rho < 18) col = 72 + 18 + rho; }
    else col = 18 * (rho >> 4) + 8 * ((rho >> 2) & 1) + 4 * ((rho >> 3) & 1) + (rho & 3);
#pragma unroll
    for (int i = 0; i < 8; ++i) {
      int ch = tid + 512 * i;
      if (ch < 3888) { int row = ch / 27, rem = ch - 27 * row; *(u32x2*)(smem + row * RS + rem * 8) = v[i]; }
    }
    if (tid < 144) *(u32x2*)(smem + tid * RS + 216) = u32x2{0u, 0u};
    __syncthreads();
    const unsigned short* src = (const unsigned short*)(smem + (80 * kh + 8 * h) * RS) + col;
    char* dst = ws + OFF_WQKV + (size_t)p * KA_NFRAG * 1024 + blk * 1024 + lane * 16;
    float cs = 0.f;
#pragma unroll
    for (int kl = 0; kl < 5; ++kl) {
      if (5 * kh + kl < 9) {
        u32x4 o;
#pragma unroll
        for (int i = 0; i < 4; ++i)
          o[i] = (unsigned)src[(kl * 16 + 2 * i) * (RS / 2)] | ((unsigned)src[(kl * 16 + 2 * i + 1) * (RS / 2)] << 16);
        cs = dot_ones(o[0], cs); cs = dot_ones(o[1], cs); cs = dot_ones(o[2], cs); cs = dot_ones(o[3], cs);
        PST4(dst + (size_t)(9 * kq + 5 * kh + kl) * 4096, o);
      }
    }
    cs += __shfl_xor(cs, 32);
    float* cx = (float*)(smem + 144 * RS);
    if (kh == 1 && lane < 32) cx[blk * 32 + lane] = cs;
    __syncthreads();
    if (kh == 0 && lane < 32) put_colsum(ws + OFF_WQKV + ((size_t)p * KA_NFRAG + 144 + blk) * 1024 + lane * 16, kq, cs + cx[blk * 32 + lane]);
    return;
  }
  b -= PB_WQKV;
  if (b < PB_X) {
    const int ks = lane < 36 ? lane : 35;
    u32x4 v0[2], v1[2];
#pragma unroll
    for (int i = 0; i < 2; ++i) {
      const u32x4* src = (const u32x4*)(x + (size_t)(b * 16 + w8 * 2 + i) * D + 16 * ks);
      v0[i] = PLD4(src); v1[i] = PLD4(src + 1);
    }
#pragma unroll
    for (int i = 0; i < 2; ++i) {
      const int token = b * 16 + w8 * 2 + i, tb = token >> 5, r = token & 31;
      if (lane < 36) {
        char* dst = ws + OFF_XF0 + ((size_t)(tb * 36 + ks) * 64 + r) * 16;
        PST4(dst, v0[i]); PST4(dst + 512, v1[i]);
      }
      float s = 0.f;
#pragma unroll
      for (int j = 0; j < 4; ++j) s += bflo(v0[i][j]) + bfhi(v0[i][j]) + bflo(v1[i][j]) + bfhi(v1[i][j]);
      float m16 = s * (1.f / 16.f), q = 0.f;
#pragma unroll
      for (int j = 0; j < 4; ++j) {
        float a = bflo(v0[i][j]) - m16, c = bfhi(v0[i][j]) - m16, d = bflo(v1[i][j]) - m16, e = bfhi(v1[i][j]) - m16;
        q += a * a + c * c + d * d + e * e;
      }
      int g = lane & 7, st = dg_start(g), n = dg_count(g);
      float ms = 0.f, mm[5], qq = 0.f;
#pragma unroll
      for (int j = 0; j < 5; ++j) {
        int sl = st + (j < n ? j : 0);
        mm[j] = __shfl(m16, sl); float qi = __shfl(q, sl);
        if (j < n) { ms += mm[j]; qq += qi; }
      }
      float mean = ms / n;
#pragma unroll
      for (int j = 0; j < 5; ++j) if (j < n) { float d = mm[j] - mean; qq += 16.f * d * d; }
      if (lane < 8) *(f32x2*)(ws + OFF_ST0 + ((size_t)token * 8 + lane) * 8) = f32x2{mean, qq};
    }
    return;
  }
  b -= PB_X;
  if (b < PB_WO) {
    constexpr int RS = 40;
    const int half = tid >> 8, t = tid & 255, w = w8 & 3, c = 2 * b + half;
    char* sm = smem + half * (576 * RS);
    u32x4 v[5];
#pragma unroll
    for (int i = 0; i < 5; ++i) {
      int ch = t + 256 * i; if (ch > 1151) ch = 1151;
      v[i] = PLD4((const u32x4*)(wo + (size_t)(ch >> 1) * D + 16 * c + 8 * (ch & 1)));
    }
#pragma unroll
    for (int i = 0; i < 5; ++i) {
      int ch = t + 256 * i;
      if (ch < 1152) { char* d = sm + (ch >> 1) * RS + (ch & 1) * 16; *(u32x2*)d = u32x2{v[i][0], v[i][1]}; *(u32x2*)(d + 8) = u32x2{v[i][2], v[i][3]}; }
    }
    __syncthreads();
    const unsigned sb = lds_addr(sm);
    const int q = lane >> 4, qq = (lane >> 2) & 3, pp = lane & 3;
    for (int s = w; s < 18; s += 4) {
      int r0, r1;
      if (s < 16) { r0 = (2 * s + (q >> 1)) * 18 + 8 * (q & 1) + qq; r1 = r0 + 4; }
      else { int w0 = 4 * (s - 16) + (q >> 1); r0 = (4 * w0 + qq) * 18 + 16 + (q & 1); r1 = (4 * (w0 + 2) + qq) * 18 + 16 + (q & 1); }
      u32x2 lo = lds_tr(sb + r0 * RS + pp * 8), hi = lds_tr(sb + r1 * RS + pp * 8);
      PST4(ws + OFF_WO + ((size_t)c * 18 + s) * 1024 + lane * 16, (u32x4{lo[0], lo[1], hi[0], hi[1]}));
    }
    return;
  }
  b -= PB_WO;
  if (b < PB_Z) {
#pragma unroll
    for (int i = 0; i < 4; ++i) {
      size_t o = (((size_t)b * 4 + i) * 512 + tid) * 16;
      if (o < SZ_ZERO) PST4(ws + OFF_KV + o, (u32x4{0, 0, 0, 0}));
    }
    return;
  }
  b -= PB_Z;
  {
    const int half = tid >> 8, t = tid & 255, u = 2 * b + half;
    char* sm = smem + half * (144 * P_RS);
    RtRegs R;
    rt_load(u, t, wg, wu, wd, R);
    rt_stage(u, t, sm, R);
    __syncthreads();
    rt_emit(u, t, sm, ws);
  }
}

DI u32x4 mean_frag(float mean, int h) {
  unsigned mh = pack2(mean, 0.f) & 0xffffu;
  unsigned ml = pack2(mean - bflo(mh), 0.f) & 0xffffu;
  unsigned hh = mh | (mh << 16), lh = ml | (mh << 16), hl = mh | (ml << 16);
  return h == 0 ? u32x4{hh, lh, hl, hh} : u32x4{lh, hl, 0u, 0u};
}

DI float phi2(float a, float rs2) {
  const float t = a * rs2;
  return __builtin_fmaf(fmaxf(t, 0.f), 0.6931471805599453f, __builtin_amdgcn_exp2f(fminf(t, 0.f)) + 1e-6f);
}
constexpr int KA_PRO = 12;
constexpr int KA_RING = 37;
constexpr int KA_PD = 8;
constexpr int ka_kv_row(int e) { return e < 16 ? (e & 3) + 4 * (e >> 3) + 8 * ((e >> 2) & 1) : (e == 16 ? 16 : (e == 17 ? 20 : 17)); }
template <int KS>
DI void ka_steps(u32x4 (&X)[37], u32x4 (&A)[KA_PD], f32x16 (&acc)[4], const char* xs, unsigned l16, unsigned rd0, unsigned rd1, float mean, int h) {
  if constexpr (KS == 36) X[36] = mean_frag(mean, h);
  if constexpr (KS % 4 == 0 && KS > 0 && KS <= 32) bar();
  if constexpr (KS < 36) {
    constexpr int issued = KS + KA_PRO < 36 ? KS + KA_PRO : 36;
    waitv_pin<issued - KS - 1>(X[KS]);
    if constexpr (KS + KA_PRO < 36) hload16<0>(X[KS + KA_PRO], xs + (KS + KA_PRO) * 1024, l16);
  }
#pragma unroll
  for (int blk = 0; blk < 4; ++blk) {
    const int i = 4 * KS + blk;
    u32x4 a = A[i % KA_PD];
    if (blk < 3) acc[blk] = mfma32(X[KS], a, acc[blk]);
    else acc[3] = mfma32(a, X[KS], acc[3]);
    if (i + KA_PD < KA_NFRAG) { const int f = (i + KA_PD) % (4 * KA_RING); A[i % KA_PD] = lds_rd((f < 64 ? rd0 : (f < 128 ? rd1 : rd1 + 65536)) + (f & 63) * 1024); }
    __builtin_amdgcn_sched_barrier(0);
  }
  if constexpr (KS + 1 < 37) ka_steps<KS + 1>(X, A, acc, xs, l16, rd0, rd1, mean, h);
}
constexpr int KA_PRE = 12;
constexpr int ka_to(int G) { int t = G == 0 ? KA_PRE : 4 * G + 16; if (G > 0 && t > 4 * (G - 1) + KA_RING) t = 4 * (G - 1) + KA_RING; return t < 37 ? t : 37; }
struct KaFoTable { short v[256][8]; };
constexpr KaFoTable ka_make_fo() {
  KaFoTable T{};
  for (int t = 0; t < 256; ++t)
    for (int i = 0; i < 8; ++i) {
      const int it = t + 256 * i;
      int val = -1;
      if (i < 5 && it < 1152) {
        const int hs = it >= 576, sl = it - 576 * hs;
        int row = 0, d = 0;
        if (sl < 512) { row = (sl >> 1) & 31; d = 8 * ((sl >> 6) & 1) + 2 * (sl >> 7) + (sl & 1); }
        else { row = sl & 31; d = 16 + ((sl >> 5) & 1); }
        const int e = row < 16 ? (row & 3) + 4 * (row >> 3) + 8 * ((row >> 2) & 1) : (row == 16 ? 16 : (row == 20 ? 17 : (row == 17 ? 18 : -1)));
        if (e >= 0) val = hs == 0 ? d * 33 + e : (d < 14 ? (18 + d) * 33 + e : (32 + d - 14) * 33 + e);
      }
      T.v[t][i] = (short)val;
    }
  return T;
}
__device__ __attribute__((aligned(16))) const KaFoTable KA_FO = ka_make_fo();
template <int G, int GEND> DI void ka_loader(const char* wsrc, unsigned l16, unsigned dst) {
  constexpr int need = 4 * G + 7 < 36 ? 4 * G + 7 : 36;
  constexpr int from = G == 0 ? KA_PRE : ka_to(G - 1);
  constexpr int to = ka_to(G);
  static_assert(to > need && to >= from, "loader schedule");
#pragma unroll
  for (int ks = from; ks < to; ++ks) glds16(wsrc + ks * 4096, l16, dst + (4 * (ks % KA_RING)) * 1024);
  waitv<to - 1 - need>();
  bar();
  if constexpr (G < GEND) ka_loader<G + 1, GEND>(wsrc, l16, dst);
}

DI void ka_phase(char* smem, char* ws, const int lay_) {
  const char* xf = ws + (lay_ ? OFF_XF2 : OFF_XF0);
  const char* st = ws + (lay_ ? OFF_ST2 : OFF_ST0);
  const char* wqkvf = ws + OFF_WQKV;
  char* pqf = ws + OFF_PQF + lay_ * SZ_PQF;
  char* pql = ws + OFF_PQL + lay_ * SZ_PQL;
  double* kv = (double*)(ws + OFF_KV) + (size_t)lay_ * KV_DBL;
  const int tid = tid_opaque(), lane = tid & 63;
  const int w8 = __builtin_amdgcn_readfirstlane(tid >> 6);
  const int w = w8 & 3;
  const int b = blockIdx.x, xcd = b & 7, idx = b >> 3;
  const int tt = 2 * xcd + (idx & 1), p = idx >> 1;
  const int tb = 4 * tt + w;
  const unsigned sb = lds_addr(smem);
  const unsigned l16 = lane * 16;
  const int h = lane >> 5, r = lane & 31;
  STAMP(1, 0);
  if (w8 >= 4) {
    const char* wsrc = wqkvf + (size_t)p * KA_NFRAG * 1024 + w * 1024;
    u32x4 fot;
    gload16<0>(fot, &KA_FO, (tid - 256) * 16);
#pragma unroll
    for (int ks = 0; ks < KA_PRE; ++ks) glds16(wsrc + ks * 4096, l16, sb + w * 1024 + (4 * ks) * 1024);
    if (lay_) dep_wait(ws, SY_D + tt, 1, 16);
    ka_loader<0, 8>(wsrc, l16, sb + w * 1024);
    asm volatile("" : "+v"(fot));
    int fo[5] = {(short)(fot[0] & 0xffff), (short)(fot[0] >> 16), (short)(fot[1] & 0xffff), (short)(fot[1] >> 16), (short)(fot[2] & 0xffff)};
    bar();
    bar();
    float sum[5];
#pragma unroll
    for (int i = 0; i < 5; ++i) {
      const float* rp = (const float*)smem + (fo[i] >= 0 ? fo[i] : 0);
      sum[i] = (rp[0] + rp[1280]) + (rp[2560] + rp[3840]);
    }
#pragma unroll
    for (int i = 0; i < 5; ++i)
      if (fo[i] >= 0) unsafeAtomicAdd(kv + (size_t)(2 * p) * 576 + (tid - 256) + 256 * i, (double)sum[i]);
  } else {
    u32x4 sv[4], X[37];
    const char* xs = xf + (size_t)tb * 36 * 1024;
    if (lay_) dep_wait(ws, SY_D + tt, 1, 16);
    hload16<0>(X[0], xs, l16); hload16<0>(X[1], xs + 1024, l16);
    {
      const char* sp = st + (size_t)(tb * 32) * 64;
      unsigned so = r * 64;
      hload16<0>(sv[0], sp, so); hload16<16>(sv[1], sp, so); hload16<32>(sv[2], sp, so); hload16<48>(sv[3], sp, so);
    }
#pragma unroll
    for (int k = 2; k < KA_PRO; ++k) hload16<0>(X[k], xs + k * 1024, l16);
    STAMP(1, 1);
    float rstd, mean;
    {
      asm volatile("s_waitcnt vmcnt(%4)" : "+v"(sv[0]), "+v"(sv[1]), "+v"(sv[2]), "+v"(sv[3]) : "n"(KA_PRO - 2));
      float m[8], q[8];
#pragma unroll
      for (int i = 0; i < 4; ++i) {
        m[2 * i] = asf(sv[i][0]); q[2 * i] = asf(sv[i][1]);
        m[2 * i + 1] = asf(sv[i][2]); q[2 * i + 1] = asf(sv[i][3]);
      }
      mean = (80.f * (m[0] + m[1] + m[2] + m[3]) + 64.f * (m[4] + m[5] + m[6] + m[7])) * (1.f / 576.f);
      float M2 = 0.f;
#pragma unroll
      for (int i = 0; i < 8; ++i) { float d = m[i] - mean; M2 += q[i] + (i < 4 ? 80.f : 64.f) * d * d; }
      rstd = rsqrtf(M2 * (1.f / 576.f) + 1e-5f);
    }
    f32x16 acc[4];
#pragma unroll
    for (int i = 0; i < 4; ++i)
#pragma unroll
      for (int j = 0; j < 16; ++j) acc[i][j] = 0.f;
    unsigned rd0 = sb + l16, rd1 = rd0 + 65536;
    asm volatile("" : "+v"(rd0), "+v"(rd1));
    bar();
    STAMP(1, 2);
    u32x4 A[KA_PD];
#pragma unroll
    for (int i = 0; i < KA_PD; ++i) A[i] = lds_rd(rd0 + i * 1024);
    __builtin_amdgcn_sched_barrier(0);
    ka_steps<0>(X, A, acc, xs, l16, rd0, rd1, mean, h);
    STAMP(1, 3);
    bar();
    STAMP(1, 6);

    const int c = r;
    float* rs = (float*)(smem + 32768) + w * 160;
    float* ql = rs + 32;
    if (h == 0) rs[r] = rstd;
    f32x4 rr[4];
#pragma unroll
    for (int g = 0; g < 4; ++g) rr[g] = *(const f32x4*)(rs + 8 * g + 4 * h);
    u32x4 k1[2], v1[2], v2[2];
    f32x4 pv[4];
    const bool ph = c >= 19 && c <= 26, one = c == 18;
#pragma unroll
    for (int s = 0; s < 2; ++s)
#pragma unroll
      for (int t = 0; t < 4; ++t) {
        const int r0 = 8 * s + 2 * t;
        const float s0 = rr[r0 >> 2][r0 & 3], s1 = rr[r0 >> 2][(r0 & 3) + 1];
        const float e0 = s0 * 1.4426950408889634f, e1 = s1 * 1.4426950408889634f;
        k1[s][t] = pack2(phi2(acc[0][r0], e0), phi2(acc[0][r0 + 1], e1));
        float a0 = acc[1][r0] * s0, a1 = acc[1][r0 + 1] * s1;
        float p0 = phi2(acc[1][r0], e0), p1 = phi2(acc[1][r0 + 1], e1);
        a0 = ph ? p0 : (one ? 1.f : a0); a1 = ph ? p1 : (one ? 1.f : a1);
        pv[r0 >> 2][r0 & 3] = p0; pv[r0 >> 2][(r0 & 3) + 1] = p1;
        v1[s][t] = pack2(a0, a1);
        v2[s][t] = one ? 0x3f803f80u : pack2(acc[2][r0] * s0, acc[2][r0 + 1] * s1);
      }
    if (c >= 23 && c <= 26) {
#pragma unroll
      for (int g = 0; g < 4; ++g) *(f32x4*)(ql + (c - 23) * 32 + 8 * g + 4 * h) = pv[g];
    }
    STAMP(1, 7);
    f32x16 R1, R2, R3;
#pragma unroll
    for (int j = 0; j < 16; ++j) { R1[j] = 0.f; R2[j] = 0.f; R3[j] = 0.f; }
#pragma unroll
    for (int s = 0; s < 2; ++s) {
      R1 = mfma32(k1[s], v1[s], R1);
      R2 = mfma32(k1[s], v2[s], R2);
      R3 = mfma32(v1[s], v2[s], R3);
    }
    float* red = (float*)smem + w * 1280;
#pragma unroll
    for (int j = 0; j < 16; ++j) {
      int row = (j & 3) + 8 * (j >> 2) + 4 * h;
      red[row * 33 + c] = row < 18 ? R1[j] : R2[j];
    }
    if (h == 0) red[32 * 33 + c] = R3[11];
    else { red[33 * 33 + c] = R3[8]; red[34 * 33 + c] = R3[9]; red[35 * 33 + c] = R3[10]; }
    STAMP(1, 4);
    asm volatile("s_waitcnt lgkmcnt(0)" ::: "memory");
    bar();
    {
      u32x4 qa, qb;
      const float rse = rstd * 1.4426950408889634f;
#pragma unroll
      for (int t = 0; t < 4; ++t) {
        qa[t] = pack2(phi2(acc[3][2 * t], rse), phi2(acc[3][2 * t + 1], rse));
        qb[t] = pack2(phi2(acc[3][8 + 2 * t], rse), phi2(acc[3][8 + 2 * t + 1], rse));
      }
      char* dst = pqf + ((size_t)tb * 32 + 2 * p) * 1024;
      hstore16(dst, l16, qa); hstore16(dst + 1024, l16, qb);
      float la = ql[h * 32 + r], lb = ql[(2 + h) * 32 + r];
      hstore4(pql + ((size_t)tb * 4 + (p >> 2)) * 1024 + 4 * (p & 3), l16, pack2(la, lb));
    }
  }
  sig_done(ws, SY_KA + 8 * lay_ + xcd);
}

constexpr int KB_LDS = 36 * 1024 + 16 * 1024 + 9 * 32 * 8;
DI void kb_phase(char* smem, char* ws, const int lay_) {
  const char* pqf = ws + OFF_PQF + lay_ * SZ_PQF;
  const char* pql = ws + OFF_PQL + lay_ * SZ_PQL;
  const double* kv = (const double*)(ws + OFF_KV) + (size_t)lay_ * KV_DBL;
  const char* wof = ws + OFF_WO;
  const char* xin = ws + (lay_ ? OFF_XF2 : OFF_XF0);
  char* xout = ws + OFF_XF1 + lay_ * SZ_XF;
  char* stout = ws + OFF_ST1 + lay_ * SZ_ST1;
  const int tid = tid_opaque(), lane = tid & 63;
  const int w = __builtin_amdgcn_readfirstlane(tid >> 6);
  const int b = blockIdx.x, xcd = b & 7, idx = b >> 3;
  const int tb = 8 * xcd + (idx & 7), cg = idx >> 3;
  const int ct = 9 * cg + w, ct8 = 9 * cg + 8;
  const int h = lane >> 5, r = lane & 31, q = lane >> 4, t = lane & 15;

  STAMP(2, 0);
  u32x4 A[18], A8[3];
  const char* wp = wof + (size_t)ct * 18 * 1024;
  const char* wp8 = wof + ((size_t)ct8 * 18 + w) * 1024;
  const unsigned l16 = lane * 16;
  u32x2 res[2], res8;
  const unsigned xq = ((q >> 1) * 32 + t) * 16 + (q & 1) * 8;
  const unsigned xo = (tb * 36 + ct) * 1024 + xq;
  const unsigned xo8 = (tb * 36 + ct8) * 1024 + xq + (w & 1) * 256;
#define KB_LDA(s_) gload16<((s_) & 3) * 1024>(A[s_], wp + ((s_) >> 2) * 4096, l16)
  gload16<0>(A8[0], wp8, l16); gload16<0>(A8[1], wp8 + 8192, l16); gload16<0>(A8[2], wp8 + (w < 2 ? 16384 : 0), l16);
  KB_LDA(0); KB_LDA(1); KB_LDA(2); KB_LDA(3); KB_LDA(4); KB_LDA(5); KB_LDA(6); KB_LDA(7); KB_LDA(8);
  KB_LDA(9); KB_LDA(10); KB_LDA(11); KB_LDA(12); KB_LDA(13); KB_LDA(14); KB_LDA(15); KB_LDA(16); KB_LDA(17);
  dep_wait(ws, SY_KA + 8 * lay_, 8, 32);

  const bool valid = r < 18 || r == 20 || r == 21;
  const int rr = valid ? (r == 21 ? 17 : r) : 0;
  const unsigned kvo = (h * 32 + rr) * 16, klo = (h * 32 + rr) * 8;
  u32x4 kd[4][4];
  u32x2 kl[4];
  u32x4 bq[4], bl;
  hload16<0>(bl, pql + ((size_t)tb * 4 + (w >> 1)) * 1024, l16);
#pragma unroll
  for (int i = 0; i < 4; ++i) {
    const char* src = (const char*)(kv + (size_t)(4 * w + i) * 576);
    hload16<0>(kd[i][0], src, kvo); hload16<1024>(kd[i][1], src, kvo); hload16<2048>(kd[i][2], src, kvo); hload16<3072>(kd[i][3], src, kvo);
    hload8<0>(kl[i], src + 4096, klo);
    hload16<0>(bq[i], pqf + ((size_t)tb * 32 + 4 * w + i) * 1024, l16);
  }
  hload8<0>(res[0], xin, xo); hload8<256>(res[1], xin, xo); hload8<0>(res8, xin, xo8);
  STAMP(2, 1);
  float lo[4];
#pragma unroll
  for (int i = 0; i < 4; ++i) {
    if (i == 0) asm volatile("s_waitcnt vmcnt(21)" : "+v"(bl), "+v"(kd[0][0]), "+v"(kd[0][1]), "+v"(kd[0][2]), "+v"(kd[0][3]), "+v"(kl[0]), "+v"(bq[0]));
    if (i == 1) asm volatile("s_waitcnt vmcnt(15)" : "+v"(kd[1][0]), "+v"(kd[1][1]), "+v"(kd[1][2]), "+v"(kd[1][3]), "+v"(kl[1]), "+v"(bq[1]));
    if (i == 2) asm volatile("s_waitcnt vmcnt(9)" : "+v"(kd[2][0]), "+v"(kd[2][1]), "+v"(kd[2][2]), "+v"(kd[2][3]), "+v"(kl[2]), "+v"(bq[2]));
    if (i == 3) asm volatile("s_waitcnt vmcnt(3)" : "+v"(kd[3][0]), "+v"(kd[3][1]), "+v"(kd[3][2]), "+v"(kd[3][3]), "+v"(kl[3]), "+v"(bq[3]));
    u32x4 a1, a2;
#pragma unroll
    for (int k = 0; k < 4; ++k) {
      double d0 = __builtin_bit_cast(double, u32x2{kd[i][k][0], kd[i][k][1]});
      double d1 = __builtin_bit_cast(double, u32x2{kd[i][k][2], kd[i][k][3]});
      a1[k] = valid ? pack2((float)d0, (float)d1) : 0u;
    }
    const float klf = (float)__builtin_bit_cast(double, kl[i]);
    const unsigned lv = valid ? ((i & 1) ? pack2(0.f, klf) : pack2(klf, 0.f)) : 0u;
    const int dw = 2 * (w & 1) + (i >> 1);
#pragma unroll
    for (int k = 0; k < 4; ++k) a2[k] = (k == dw) ? lv : 0u;
    f32x16 n;
#pragma unroll
    for (int j = 0; j < 16; ++j) n[j] = 0.f;
    n = mfma32(a1, bq[i], n);
    n = mfma32(a2, bl, n);
    float rd = __builtin_amdgcn_rcpf(n[9] + 1e-6f);
    u32x4 o;
#pragma unroll
    for (int k = 0; k < 4; ++k) o[k] = pack2(n[2 * k] * rd, n[2 * k + 1] * rd);
    lo[i] = n[8] * rd;
    const int head = 4 * w + i, s = head >> 1, qq = 2 * (head & 1) + h;
    *(u32x4*)(smem + (((r >> 4) * 18 + s) * 64 + qq * 16 + (r & 15)) * 16) = o;
  }
  {
    const int s = 16 + (w >> 2), qq = 2 * (w & 1) + h, half = (w >> 1) & 1;
    *(u32x2*)(smem + (((r >> 4) * 18 + s) * 64 + qq * 16 + (r & 15)) * 16 + half * 8) = u32x2{pack2(lo[0], lo[1]), pack2(lo[2], lo[3])};
  }
  STAMP(2, 2);
  asm volatile("s_waitcnt lgkmcnt(0)" ::: "memory");
  bar();
  STAMP(2, 3);
  {
    asm volatile("" : "+v"(A8[0]), "+v"(A8[1]), "+v"(A8[2]));
    f32x4 p8[2] = {{0.f, 0.f, 0.f, 0.f}, {0.f, 0.f, 0.f, 0.f}};
#pragma unroll
    for (int j = 0; j < 3; ++j) {
      const int s = w + 8 * j;
      if (j < 2 || w < 2) {
        u32x4 b0 = *(const u32x4*)(smem + (s * 64 + lane) * 16);
        u32x4 b1 = *(const u32x4*)(smem + ((18 + s) * 64 + lane) * 16);
        p8[0] = mfma16(A8[j], b0, p8[0]);
        p8[1] = mfma16(A8[j], b1, p8[1]);
      }
    }
    *(f32x4*)(smem + 36 * 1024 + ((w * 2 + 0) * 64 + lane) * 16) = p8[0];
    *(f32x4*)(smem + 36 * 1024 + ((w * 2 + 1) * 64 + lane) * 16) = p8[1];
  }
  f32x4 acc[2] = {{0.f, 0.f, 0.f, 0.f}, {0.f, 0.f, 0.f, 0.f}};
#pragma unroll
  for (int s = 0; s < 18; ++s) {
    if (s == 0) asm volatile("" : "+v"(A[0]), "+v"(A[1]), "+v"(A[2]));
    if (s == 3) asm volatile("" : "+v"(A[3]), "+v"(A[4]), "+v"(A[5]), "+v"(A[6]), "+v"(A[7]));
    if (s == 8) asm volatile("" : "+v"(A[8]), "+v"(A[9]), "+v"(A[10]), "+v"(A[11]), "+v"(A[12]));
    if (s == 13) asm volatile("" : "+v"(A[13]), "+v"(A[14]), "+v"(A[15]), "+v"(A[16]), "+v"(A[17]));
    u32x4 b0 = *(const u32x4*)(smem + (s * 64 + lane) * 16);
    u32x4 b1 = *(const u32x4*)(smem + ((18 + s) * 64 + lane) * 16);
    acc[0] = mfma16(A[s], b0, acc[0]);
    acc[1] = mfma16(A[s], b1, acc[1]);
  }
  STAMP(2, 4);
  asm volatile("s_waitcnt vmcnt(0) lgkmcnt(0)" : "+v"(res[0]), "+v"(res[1]), "+v"(res8) :: "memory");
  bar();
  f32x2* sred = (f32x2*)(smem + 52 * 1024);
  auto finish = [&](const f32x4& a, u32x2 rs, unsigned off, int slot, int tok) {
    float v0 = bflo(rs[0]) + a[0], v1 = bfhi(rs[0]) + a[1], v2 = bflo(rs[1]) + a[2], v3 = bfhi(rs[1]) + a[3];
    u32x2 o = {pack2(v0, v1), pack2(v2, v3)};
    hstore8(xout, off, o);
    v0 = bflo(o[0]); v1 = bfhi(o[0]); v2 = bflo(o[1]); v3 = bfhi(o[1]);
    float s = sum_x32(sum_x16((v0 + v1) + (v2 + v3)));
    float m = s * (1.f / 16.f);
    float d0 = v0 - m, d1 = v1 - m, d2 = v2 - m, d3 = v3 - m;
    float qq = sum_x32(sum_x16(d0 * d0 + d1 * d1 + d2 * d2 + d3 * d3));
    if (q == 0) sred[slot * 32 + tok] = f32x2{m, qq};
  };
  finish(acc[0], res[0], xo, w, t);
  finish(acc[1], res[1], xo + 256, w, 16 + t);
  if (w < 2) {
    f32x4 a = {0.f, 0.f, 0.f, 0.f};
#pragma unroll
    for (int i = 0; i < 8; ++i) {
      f32x4 pv = *(const f32x4*)(smem + 36 * 1024 + ((i * 2 + (w & 1)) * 64 + lane) * 16);
      a[0] += pv[0]; a[1] += pv[1]; a[2] += pv[2]; a[3] += pv[3];
    }
    finish(a, res8, xo8, 8, 16 * (w & 1) + t);
  }
  asm volatile("s_waitcnt lgkmcnt(0)" ::: "memory");
  bar();
  if (tid < 32) {
    float mm[9], ms = 0.f, qq = 0.f;
#pragma unroll
    for (int i = 0; i < 9; ++i) { f32x2 v = sred[i * 32 + tid]; mm[i] = v[0]; ms += v[0]; qq += v[1]; }
    float mean = ms * (1.f / 9.f);
#pragma unroll
    for (int i = 0; i < 9; ++i) { float d = mm[i] - mean; qq += 16.f * d * d; }
    hstore8(stout, ((tb * 32 + tid) * 4 + cg) * 8, u32x2{__builtin_bit_cast(unsigned, mean), __builtin_bit_cast(unsigned, qq)});
  }
  STAMP(2, 5);
  sig_done(ws, SY_KB + 8 * lay_ + xcd);
}

#define GU_STAGE_ 32
#define GU_RING_ 128
constexpr int GU_STAGE = GU_STAGE_, GU_RING = GU_RING_, GU_P1 = 111;
constexpr int GU_PD = 5;
constexpr int GU_NSLOT = GU_RING / GU_STAGE, GU_NPIECE = (GU_NFRAG + 7) / 8;
constexpr int GU_NST = (GU_NFRAG + GU_STAGE - 1) / GU_STAGE;
struct GuSched { int n; int kind[96]; int arg[96]; int step[96]; };
#define GU_PRO_X_ 10
#define GU_PRO_P_ 8
#define GU_PACE 5
#define GU_LAG_ 1
constexpr int GU_PRO_X = GU_PRO_X_;
constexpr int GU_PRO_P = GU_PRO_P_;
constexpr int gu_pstage(int p) { return 8 * p / GU_STAGE; }
constexpr int gu_pdead(int p) { int st = gu_pstage(p); return st == 0 ? 0 : st * GU_STAGE - GU_PD; }
constexpr int gu_pearly(int p) { int st = gu_pstage(p); return st < GU_NSLOT ? 0 : (st - GU_NSLOT + 2) * GU_STAGE - GU_PD + 2; }
constexpr GuSched gu_make_sched() {
  GuSched s{};
  int n = 0;
  auto push = [&](int k, int a, int st) { s.kind[n] = k; s.arg[n] = a; s.step[n] = st; ++n; };
  int kx = 0, pc = 0;
  while (pc < GU_PRO_P) push(1, pc++, -2);
  push(2, 0, -1); push(2, 1, -1);
  while (kx < GU_PRO_X) push(0, kx++, -1);
  int m = 0, last = 0;
  while (kx < 36 || pc < GU_NPIECE) {
    int dx = kx < 36 ? 3 * kx : 100000, ds = pc < GU_NPIECE ? gu_pdead(pc) : 100000;
    bool takep = ds <= dx;
    int st = kx < 36 ? (m * GU_PACE) / 2 : last + 5;
    if (takep && st < gu_pearly(pc)) { if (kx < 36) { takep = false; } else st = gu_pearly(pc); }
    if (st < last) st = last;
    if (takep) push(1, pc++, st); else push(0, kx++, st);
    last = st; ++m;
  }
  s.n = n;
  return s;
}
constexpr GuSched GUS = gu_make_sched();
constexpr int GU_LAG = GU_LAG_;
constexpr int gu_cnt_raw(int step) { int c = 0; for (int i = 0; i < GUS.n; ++i) if (GUS.step[i] < step) ++c; return c; }
constexpr int gu_cnt_before(int step) { return gu_cnt_raw(step - GU_LAG < 0 ? 0 : step - GU_LAG); }
constexpr int gu_idx(int kind, int arg) { for (int i = 0; i < GUS.n; ++i) if (GUS.kind[i] == kind && GUS.arg[i] == arg) return i; return -1; }
constexpr int gu_clamp(int n) { return n > 63 ? 63 : (n < 0 ? 0 : n); }
constexpr int gu_wait_for(int idx, int step) { return gu_clamp(gu_cnt_before(step) - idx - 1); }
constexpr int gu_last_piece(int st) { int p = (st + 1) * GU_STAGE / 8 - 1; return gu_idx(1, p < GU_NPIECE ? p : GU_NPIECE - 1); }
constexpr bool gu_sched_ok() {
  for (int i = 0; i < GUS.n; ++i) {
    if (GUS.kind[i] == 1) { if (GUS.step[i] + GU_LAG >= gu_pdead(GUS.arg[i]) && gu_pdead(GUS.arg[i]) > 0) return false;
                            if (gu_pdead(GUS.arg[i]) == 0 && GUS.step[i] >= 0) return false;
                            if (gu_pearly(GUS.arg[i]) > 0 && GUS.step[i] < gu_pearly(GUS.arg[i]) - 2) return false; }
    if (GUS.kind[i] == 0 && GUS.step[i] >= 0 && GUS.step[i] + GU_LAG >= 3 * GUS.arg[i]) return false;
    if (i > 0 && GUS.step[i] < GUS.step[i - 1]) return false;
  }
  return true;
}
static_assert(gu_sched_ok(), "GU schedule");
static_assert(GU_PRO_P >= GU_STAGE / 8, "stage 0 must be requested in the prologue");

struct GuCtx {
  const char* wsrc; const char* xs; const char* sp; unsigned l16, sb, so; int w, grp, h; unsigned rd0, rd1, rd2; float c1, c3, cinv; char* hbase; unsigned hvoff; int fb0;
};
template <int N, int G> DI void gu_issue(const GuCtx& c, u32x4 (&X)[37], u32x4 (&sv)[2]) {
  constexpr int kind = GUS.kind[N], arg = GUS.arg[N];
  if constexpr (kind == 0) {
    if constexpr (G < 0) hload16<0>(X[arg], c.xs + arg * 1024, c.l16);
    else hload16_g<G>(X[arg], c.xs + arg * 1024, c.l16, c.grp);
  }
  if constexpr (kind == 1) {
    constexpr int piece = arg * 8;
    if constexpr (G < 0) glds16(c.wsrc + piece * 1024, c.l16, c.sb + (piece % GU_RING) * 1024 + c.w * 1024);
    else glds16_g<G>(c.wsrc + piece * 1024, c.l16, c.sb + (piece % GU_RING) * 1024 + c.w * 1024, c.grp);
  }
  if constexpr (kind == 2) hload16<16 * arg>(sv[arg], c.sp, c.so);
}
template <int N, int STEP, int G> DI void gu_issue_step(const GuCtx& c, u32x4 (&X)[37], u32x4 (&sv)[2]) {
  if constexpr (N < GUS.n) {
    if constexpr (GUS.step[N] == STEP) gu_issue<N, G>(c, X, sv);
    if constexpr (GUS.step[N] <= STEP) gu_issue_step<N + 1, STEP, G>(c, X, sv);
  }
}
constexpr int gu_epi_start(int j) { int a = GU_P1 + 2 + 24 * j, e = (j < 3 ? GU_P1 : GU_P1 + 37 * (j - 2)) + 2; return a > e ? a : e; }
constexpr int gu_epi_block(int I) { for (int j = 0; j < 8; ++j) if (I >= gu_epi_start(j) && I < gu_epi_start(j) + 24) return j; return -1; }
struct GuEpi { float x, ab; unsigned pk[4]; float olo; };
template <int E> DI void gu_epi_0(const f32x16& a, const GuCtx& c, GuEpi& e) {
  float g = a[E];
  e.x = g * __builtin_fmaf(g * g, c.c3, c.c1);
}
template <int E> DI void gu_epi_1(const f32x16& a, const GuCtx& c, GuEpi& e) {
  float ex = __builtin_amdgcn_exp2f(e.x);
  e.ab = a[E] * a[E + 8];
  e.x = __builtin_fmaf(ex, c.cinv, c.cinv);
}
template <int E> DI void gu_epi_2(const GuCtx& c, GuEpi& e, int fb) {
  float o = e.ab * __builtin_amdgcn_rcpf(e.x);
  if constexpr ((E & 1) == 0) e.olo = o; else e.pk[E >> 1] = pack2(e.olo, o);
  if constexpr (E == 7) hstore16(c.hbase + (size_t)(fb >> 1) * 4096 + (fb & 1) * 512, c.hvoff, u32x4{e.pk[0], e.pk[1], e.pk[2], e.pk[3]});
}
template <int I>
DI void gu_steps(GuCtx& c, u32x4 (&A)[GU_PD], u32x4 (&X)[37], u32x4 (&sv)[2], f32x16 (&acc)[4], GuEpi& ep) {
  if constexpr ((I + GU_PD) % GU_STAGE == 0 && (I + GU_PD) / GU_STAGE < GU_NST) {
    constexpr int s = (I + GU_PD) / GU_STAGE;
    waitv<gu_wait_for(gu_last_piece(s), I)>();
    bar();
  }
  constexpr int blk = I < GU_P1 ? I % 3 : 3 + (I - GU_P1) / 37;
  constexpr int ks = I < GU_P1 ? I / 3 : (I - GU_P1) % 37;
  constexpr int slot = blk & 3;
  if constexpr (I < 108 && I % 3 == 0 && ks >= 2) waitv_pin<gu_wait_for(gu_idx(0, ks), I)>(X[ks]);
  gu_issue_step<gu_cnt_raw(I), I, 0>(c, X, sv);
  if constexpr (I >= GU_LAG) gu_issue_step<gu_cnt_raw(I - GU_LAG), I - GU_LAG, 1>(c, X, sv);
  if constexpr (I >= GU_P1 && ks == 0) {
    f32x16 z;
#pragma unroll
    for (int j = 0; j < 16; ++j) z[j] = 0.f;
    acc[slot] = mfma32(A[I % GU_PD], X[ks], z);
  } else {
    acc[slot] = mfma32(A[I % GU_PD], X[ks], acc[slot]);
  }
  if constexpr (I + GU_PD < GU_NFRAG) {
    constexpr int fr = (I + GU_PD) % GU_RING;
    A[I % GU_PD] = lds_rd((fr < 64 ? c.rd0 : (fr < 128 ? c.rd1 : c.rd2)) + (fr & 63) * 1024);
  }
  __builtin_amdgcn_sched_barrier(0);
  {
    constexpr int j = gu_epi_block(I);
    if constexpr (j >= 0) {
      constexpr int d = I - gu_epi_start(j);
      if constexpr (d % 3 == 0) gu_epi_0<d / 3>(acc[j & 3], c, ep);
      if constexpr (d % 3 == 1) gu_epi_1<d / 3>(acc[j & 3], c, ep);
      if constexpr (d % 3 == 2) gu_epi_2<d / 3>(c, ep, c.fb0 + j);
    }
  }
  __builtin_amdgcn_sched_barrier(0);
  if constexpr (I + 1 < GU_NFRAG) gu_steps<I + 1>(c, A, X, sv, acc, ep);
}

DI void gu_phase(char* smem, char* ws, const int lay_) {
  const char* xf = ws + OFF_XF1 + lay_ * SZ_XF;
  const char* st = ws + OFF_ST1 + lay_ * SZ_ST1;
  const char* wguf = ws + OFF_WGU;
  char* hf = ws + OFF_HF + lay_ * SZ_HF;
  const int tid = tid_opaque(), lane = tid & 63;
  const int w = __builtin_amdgcn_readfirstlane(tid >> 6);
  const int b = blockIdx.x, xcd = b & 7, idx = b >> 3;
  const int tt = idx & 7, fs = 4 * xcd + (idx >> 3);
  const int tb32 = 8 * tt + w;
  const unsigned sb = lds_addr(smem), l16 = lane * 16;
  const int h = lane >> 5, r = lane & 31;

  STAMP(3, 0);
  GuCtx c;
  c.wsrc = wguf + (size_t)fs * GU_NFRAG * 1024 + w * 1024;
  c.xs = xf + (size_t)tb32 * 36 * 1024;
  c.sp = st + (size_t)(tb32 * 32) * 32; c.so = r * 32;
  c.l16 = l16; c.sb = sb; c.w = w; c.grp = w >> 2; c.h = h; c.fb0 = 9 * fs;
  c.c1 = 0.f; c.c3 = 0.f; c.cinv = 0.f;
  u32x4 sv[2], X[37];
  gu_issue_step<0, -2, -1>(c, X, sv);
  dep_wait(ws, SY_KB + 8 * lay_ + tt, 1, 32);
  gu_issue_step<gu_cnt_raw(-1), -1, -1>(c, X, sv);
#pragma unroll
  for (int k = GU_PRO_X; k < 36; ++k) asm volatile("" : "=v"(X[k]));
  STAMP(3, 1);
  f32x16 acc[4];
#pragma unroll
  for (int i = 0; i < 3; ++i)
#pragma unroll
    for (int j = 0; j < 16; ++j) acc[i][j] = 0.f;
  {
    asm volatile("s_waitcnt vmcnt(%2)" : "+v"(sv[0]), "+v"(sv[1]) : "n"(gu_wait_for(gu_idx(2, 1), 0)));
    float m0 = asf(sv[0][0]), q0 = asf(sv[0][1]), m1 = asf(sv[0][2]), q1 = asf(sv[0][3]);
    float m2 = asf(sv[1][0]), q2 = asf(sv[1][1]), m3 = asf(sv[1][2]), q3 = asf(sv[1][3]);
    float mean = 0.25f * ((m0 + m1) + (m2 + m3));
    float d0 = m0 - mean, d1 = m1 - mean, d2 = m2 - mean, d3 = m3 - mean;
    float M2 = (q0 + q1) + (q2 + q3) + 144.f * (d0 * d0 + d1 * d1 + d2 * d2 + d3 * d3);
    c.cinv = M2 * (1.f / 576.f) + 1e-5f;
    float rstd = rsqrtf(c.cinv);
    c.c1 = -2.3022081985f * rstd;
    c.c3 = -0.1029432397f * rstd * rstd * rstd;
    X[36] = mean_frag(mean, h);
  }
  waitv<gu_wait_for(gu_last_piece(0) > gu_idx(0, 1) ? gu_last_piece(0) : gu_idx(0, 1), 0)>();
  pin(X[0]); pin(X[1]);
  bar();
  STAMP(3, 2);
  c.rd0 = sb + l16; c.rd1 = sb + l16 + 65536; c.rd2 = sb + l16 + 131072;
  asm volatile("" : "+v"(c.rd0), "+v"(c.rd1));
  if (GU_RING > 128) asm volatile("" : "+v"(c.rd2));
  c.hbase = hf + (size_t)(tb32 >> 1) * 144 * 4096;
  c.hvoff = (2 * (tb32 & 1) + (r >> 4)) * 1024 + h * 256 + (r & 15) * 16;
  u32x4 A[GU_PD];
#pragma unroll
  for (int i = 0; i < GU_PD; ++i) A[i] = lds_rd(c.rd0 + i * 1024);
  GuEpi ep;
  __builtin_amdgcn_sched_barrier(0);
  gu_steps<0>(c, A, X, sv, acc, ep);
  STAMP(3, 3);
  {
    float o[8];
#pragma unroll
    for (int e = 0; e < 8; ++e) {
      float g = acc[0][e];
      float ex = __builtin_amdgcn_exp2f(g * __builtin_fmaf(g * g, c.c3, c.c1));
      o[e] = g * acc[0][e + 8] * __builtin_amdgcn_rcpf(__builtin_fmaf(ex, c.cinv, c.cinv));
    }
    const int fb = c.fb0 + 8;
    hstore16(c.hbase + (size_t)(fb >> 1) * 4096 + (fb & 1) * 512, c.hvoff,
                u32x4{pack2(o[0], o[1]), pack2(o[2], o[3]), pack2(o[4], o[5]), pack2(o[6], o[7])});
  }
  STAMP(3, 4);
  sig_done(ws, SY_GU + 8 * lay_ + tt);
}

constexpr int D_DEPTH = 4;
template <int NC, int Sx> DI void d_load_b(u32x4 (&Bf)[D_DEPTH][4], const char* hs, unsigned l16) {
  constexpr int sl = Sx % D_DEPTH;
  const char* hp = hs + Sx * 4096;
  hload16<0>(Bf[sl][0], hp, l16); hload16<1024>(Bf[sl][1], hp, l16); hload16<2048>(Bf[sl][2], hp, l16); hload16<3072>(Bf[sl][3], hp, l16);
}
template <int NC, int Sx> DI void d_load_a(u32x4 (&Af)[D_DEPTH][5], const char* ws_, unsigned l16) {
  constexpr int sl = Sx % D_DEPTH;
  const char* wp = ws_ + Sx * NC * 1024;
  gload16<0>(Af[sl][0], wp, l16); gload16<1024>(Af[sl][1], wp, l16); gload16<2048>(Af[sl][2], wp, l16); gload16<3072>(Af[sl][3], wp, l16);
  if constexpr (NC == 5) gload16<0>(Af[sl][4], wp + 4096, l16);
}
struct DRes { const char* x1; unsigned off; u32x4 v[2]; };
constexpr int d_wait_count(int NC, int s) {
  const int per = 4 + NC;
  const int later = s < 36 - D_DEPTH ? s : 36 - D_DEPTH;
  const int issued = D_DEPTH * per + later * per;
  const int last = s < D_DEPTH ? D_DEPTH * NC + 4 * (s + 1) - 1 : D_DEPTH * per + (s - D_DEPTH + 1) * per - 1;
  return issued - 1 - last;
}
template <int NC, int Sx>
DI void d_steps(u32x4 (&Bf)[D_DEPTH][4], u32x4 (&Af)[D_DEPTH][5], f32x4 (&acc)[4][5], const char* hs, const char* ws_, unsigned l16, DRes& rs) {
  constexpr int sl = Sx % D_DEPTH;
  constexpr int n = d_wait_count(NC, Sx);
  if constexpr (NC == 5)
    asm volatile("s_waitcnt vmcnt(%9)" : "+v"(Bf[sl][0]), "+v"(Bf[sl][1]), "+v"(Bf[sl][2]), "+v"(Bf[sl][3]), "+v"(Af[sl][0]), "+v"(Af[sl][1]),
                 "+v"(Af[sl][2]), "+v"(Af[sl][3]), "+v"(Af[sl][4]) : "n"(n));
  else
    asm volatile("s_waitcnt vmcnt(%8)" : "+v"(Bf[sl][0]), "+v"(Bf[sl][1]), "+v"(Bf[sl][2]), "+v"(Bf[sl][3]), "+v"(Af[sl][0]), "+v"(Af[sl][1]),
                 "+v"(Af[sl][2]), "+v"(Af[sl][3]) : "n"(n));
#pragma unroll
  for (int c = 0; c < NC; ++c)
#pragma unroll
    for (int t = 0; t < 4; ++t) acc[t][c] = mfma16(Af[sl][c], Bf[sl][t], acc[t][c]);
  if constexpr (Sx + D_DEPTH < 36) {
    __builtin_amdgcn_sched_barrier(0);
    d_load_b<NC, Sx + D_DEPTH>(Bf, hs, l16);
    d_load_a<NC, Sx + D_DEPTH>(Af, ws_, l16);
  }
  if constexpr (Sx == 36 - D_DEPTH + 1) {
    __builtin_amdgcn_sched_barrier(0);
    hload16<0>(rs.v[0], rs.x1, rs.off); hload16<512>(rs.v[1], rs.x1, rs.off);
  }
  if constexpr (Sx + 1 < 36) d_steps<NC, Sx + 1>(Bf, Af, acc, hs, ws_, l16, rs);
}
constexpr int D_STRIDE = 84;
template <int NC>
DI void d_body(char* smem, char* ws, char* dout, const int lay_, int tt, int g, int tid, int lane, int w8) {
  const char* hf = ws + OFF_HF + lay_ * SZ_HF;
  const char* wdf = ws + OFF_WD;
  char* xout = ws + OFF_XF2;
  char* stout = ws + OFF_ST2;
  const unsigned l16 = lane * 16;
  const int st = dg_start(g);
  DRes rs;
  rs.x1 = ws + OFF_XF1 + lay_ * SZ_XF;
  const bool has = tid < 64 * NC;
  const int it = has ? tid : 0;
  int token, kl;
  if (lay_) { token = it / NC; kl = it - token * NC; } else { token = it & 63; kl = it >> 6; }
  const int gt = tt * 64 + token, ks = st + kl;
  rs.off = (((gt >> 5) * 36 + ks) * 64 + (gt & 31)) * 16;
  float* red = (float*)smem;
  STAMP(4, 0);
  if (w8 < 4) {
    const char* hs = hf + ((size_t)(tt * 144 + 36 * w8) * 4) * 1024;
    const char* ws_ = wdf + ((size_t)st * 144 + (size_t)36 * w8 * NC) * 1024;
    u32x4 Bf[D_DEPTH][4], Af[D_DEPTH][5];
    f32x4 acc[4][5];
#pragma unroll
    for (int t = 0; t < 4; ++t)
#pragma unroll
      for (int c = 0; c < 5; ++c) acc[t][c] = f32x4{0.f, 0.f, 0.f, 0.f};
    d_load_a<NC, 0>(Af, ws_, l16); d_load_a<NC, 1>(Af, ws_, l16); d_load_a<NC, 2>(Af, ws_, l16); d_load_a<NC, 3>(Af, ws_, l16);
    dep_wait(ws, SY_GU + 8 * lay_ + (tt >> 2), 1, 32);
    d_load_b<NC, 0>(Bf, hs, l16); d_load_b<NC, 1>(Bf, hs, l16); d_load_b<NC, 2>(Bf, hs, l16); d_load_b<NC, 3>(Bf, hs, l16);
    STAMP(4, 1);
    d_steps<NC, 0>(Bf, Af, acc, hs, ws_, l16, rs);
    STAMP(4, 2);
    const int q = lane >> 4, t = lane & 15;
    float* rp = red + (size_t)w8 * 64 * D_STRIDE + t * D_STRIDE + 4 * q;
#pragma unroll
    for (int tb = 0; tb < 4; ++tb)
#pragma unroll
      for (int c = 0; c < NC; ++c) *(f32x4*)(rp + tb * 16 * D_STRIDE + c * 16) = acc[tb][c];
  } else {
    dep_wait(ws, SY_GU + 8 * lay_ + (tt >> 2), 1, 32);
    hload16<0>(rs.v[0], rs.x1, rs.off); hload16<512>(rs.v[1], rs.x1, rs.off);
  }
  asm volatile("s_waitcnt vmcnt(0) lgkmcnt(0)" : "+v"(rs.v[0]), "+v"(rs.v[1]) :: "memory");
  bar();
  STAMP(4, 3);
  f32x2* sred = (f32x2*)(smem + 4 * 64 * D_STRIDE * 4);
  if (has) {
    const float* rp = red + token * D_STRIDE + kl * 16;
    float v[16];
#pragma unroll
    for (int i = 0; i < 4; ++i) {
      f32x4 a = *(const f32x4*)(rp + 4 * i), b = *(const f32x4*)(rp + 64 * D_STRIDE + 4 * i);
      f32x4 c = *(const f32x4*)(rp + 2 * 64 * D_STRIDE + 4 * i), d = *(const f32x4*)(rp + 3 * 64 * D_STRIDE + 4 * i);
#pragma unroll
      for (int j = 0; j < 4; ++j) v[4 * i + j] = (a[j] + b[j]) + (c[j] + d[j]);
    }
    const u32x4 r0 = rs.v[0], r1 = rs.v[1];
    u32x4 o0, o1;
#pragma unroll
    for (int i = 0; i < 4; ++i) {
      o0[i] = pack2(bflo(r0[i]) + v[2 * i], bfhi(r0[i]) + v[2 * i + 1]);
      o1[i] = pack2(bflo(r1[i]) + v[8 + 2 * i], bfhi(r1[i]) + v[8 + 2 * i + 1]);
    }
    if (lay_) {
      char* dst = dout + ((size_t)gt * D + 16 * ks) * 2;
      *(u32x4*)dst = o0; *(u32x4*)(dst + 16) = o1;
    } else {
      hstore16(xout, rs.off, o0); hstore16(xout + 512, rs.off, o1);
      float s = 0.f;
#pragma unroll
      for (int i = 0; i < 4; ++i) s += bflo(o0[i]) + bfhi(o0[i]) + bflo(o1[i]) + bfhi(o1[i]);
      float m = s * (1.f / 16.f), qq = 0.f;
#pragma unroll
      for (int i = 0; i < 4; ++i) {
        float a = bflo(o0[i]) - m, b = bfhi(o0[i]) - m, c = bflo(o1[i]) - m, d = bfhi(o1[i]) - m;
        qq += a * a + b * b + c * c + d * d;
      }
      sred[kl * 64 + token] = f32x2{m, qq};
    }
  }
  STAMP(4, 4);
  if (!lay_) {
    asm volatile("s_waitcnt lgkmcnt(0)" ::: "memory");
    bar();
    if (tid < 64) {
      float mm[NC], ms = 0.f, qq = 0.f;
#pragma unroll
      for (int i = 0; i < NC; ++i) { f32x2 v = sred[i * 64 + tid]; mm[i] = v[0]; ms += v[0]; qq += v[1]; }
      float mean = ms * (1.f / NC);
#pragma unroll
      for (int i = 0; i < NC; ++i) { float d = mm[i] - mean; qq += 16.f * d * d; }
      hstore8(stout, ((tt * 64 + tid) * 8 + g) * 8, u32x2{__builtin_bit_cast(unsigned, mean), __builtin_bit_cast(unsigned, qq)});
    }
    STAMP(4, 5);
    sig_done(ws, SY_D + (tt >> 1));
  }
}
DI void d_phase(char* smem, char* ws, char* dout, const int lay_) {
  const int tid = tid_opaque(), lane = tid & 63;
  const int w8 = __builtin_amdgcn_readfirstlane(tid >> 6);
  const int b = blockIdx.x, xcd = b & 7, idx = b >> 3;
  const int tt = 4 * xcd + (idx & 3), g = idx >> 2;
  if (g < 4) d_body<5>(smem, ws, dout, lay_, tt, g, tid, lane, w8);
  else d_body<4>(smem, ws, dout, lay_, tt, g, tid, lane, w8);
}

__global__ __launch_bounds__(512, 2) void k_layers(char* ws, char* dout) {
  __shared__ __attribute__((aligned(16))) char smem[KA_RING * 4096];
#pragma nounroll
  for (int l = 0; l < 2; ++l) {
    ka_phase(smem, ws, l);
    kb_phase(smem, ws, l);
    gu_phase(smem, ws, l);
    d_phase(smem, ws, dout, l);
  }
}

extern "C" void kernel_launch(void* const* d_in, const int* in_sizes, int n_in, void* d_out, int out_size, void* d_ws, size_t ws_size,
                              hipStream_t stream) {
  const unsigned short* x = (const unsigned short*)d_in[0];
  const unsigned short* wqkv = (const unsigned short*)d_in[1];
  const unsigned short* wo = (const unsigned short*)d_in[2];
  const unsigned short* wg = (const unsigned short*)d_in[4];
  const unsigned short* wu = (const unsigned short*)d_in[5];
  const unsigned short* wd = (const unsigned short*)d_in[6];
  char* ws = (char*)d_ws;
  k_prep<<<PB_TOTAL, 512, 0, stream>>>(x, wqkv, wo, wg, wu, wd, ws);
  k_layers<<<256, 512, 0, stream>>>(ws, (char*)d_out);
}
```

```cpp
#include <hip/hip_runtime.h>
#include <hip/hip_bf16.h>
#include <stdint.h>

typedef __attribute__((ext_vector_type(8))) __bf16 bf16x8;
typedef __attribute__((ext_vector_type(2))) __bf16 bf16x2;
typedef __attribute__((ext_vector_type(2))) float f32x2;
typedef __attribute__((ext_vector_type(4))) float f32x4;
typedef __attribute__((ext_vector_type(16))) float f32x16;
typedef __attribute__((ext_vector_type(4))) unsigned u32x4;
typedef __attribute__((ext_vector_type(2))) unsigned u32x2;
#define DI __device__ __forceinline__
#define LDS_AS __attribute__((address_space(3)))

#define STAMP(kid, i) do { } while (0)
constexpr int S = 2048, D = 576, NH = 32, DH = 18, F = 4608;

constexpr size_t SZ_XF = (size_t)S * D * 2;
constexpr size_t OFF_XF0 = 0;
constexpr size_t OFF_XF2 = OFF_XF0 + SZ_XF;
constexpr size_t OFF_XF1 = OFF_XF2 + SZ_XF;
constexpr size_t OFF_ST0 = OFF_XF1 + 2 * SZ_XF;
constexpr size_t OFF_ST2 = OFF_ST0 + (size_t)S * 64;
constexpr size_t SZ_ST1 = (size_t)S * 32;
constexpr size_t OFF_ST1 = OFF_ST2 + (size_t)S * 64;
constexpr size_t SZ_PQF = (size_t)64 * 32 * 1024, SZ_PQL = (size_t)64 * 4 * 1024;
constexpr size_t OFF_PQF = OFF_ST1 + 2 * SZ_ST1;
constexpr size_t OFF_PQL = OFF_PQF + 2 * SZ_PQF;
constexpr size_t KV_DBL = (size_t)NH * 342 + 64;
constexpr size_t OFF_KV = OFF_PQL + 2 * SZ_PQL;
constexpr size_t SZ_KV = 2 * KV_DBL * 8;
constexpr size_t OFF_SYNC = OFF_KV + SZ_KV;
constexpr size_t SZ_ZERO = SZ_KV + 72 * 256;
constexpr int KA_NFRAG = 148, GU_NFRAG = 333;
constexpr size_t OFF_WQKV = OFF_KV + SZ_ZERO;
constexpr size_t OFF_WO = OFF_WQKV + (size_t)16 * KA_NFRAG * 1024;
constexpr size_t OFF_WGU = OFF_WO + (size_t)36 * 18 * 1024;
constexpr size_t OFF_WD = OFF_WGU + (size_t)32 * GU_NFRAG * 1024 + 16384;
constexpr size_t SZ_HF = (size_t)S * F * 2;
constexpr size_t OFF_HF = OFF_WD + (size_t)36 * 144 * 1024;
constexpr size_t WS_END = OFF_HF + 2 * SZ_HF;
static_assert(OFF_WQKV % 1024 == 0 && WS_END < (size_t)200 << 20, "ws map");

DI unsigned pack2(float lo, float hi) {
  f32x2 f = {lo, hi};
  bf16x2 v = __builtin_convertvector(f, bf16x2);
  return __builtin_bit_cast(unsigned, v);
}
DI float asf(unsigned u) { return __builtin_bit_cast(float, u); }
DI float bflo(unsigned u) { return __builtin_bit_cast(float, u << 16); }
DI float bfhi(unsigned u) { return __builtin_bit_cast(float, u & 0xffff0000u); }
DI float rbf(float x) { return bflo(pack2(x, 0.f) & 0xffffu); }
DI bf16x8 asbf(u32x4 v) { return __builtin_bit_cast(bf16x8, v); }
DI f32x16 mfma32(u32x4 a, u32x4 b, f32x16 c) { return __builtin_amdgcn_mfma_f32_32x32x16_bf16(asbf(a), asbf(b), c, 0, 0, 0); }
DI f32x4 mfma16(u32x4 a, u32x4 b, f32x4 c) { return __builtin_amdgcn_mfma_f32_16x16x32_bf16(asbf(a), asbf(b), c, 0, 0, 0); }
DI unsigned lds_addr(const void* p) { return (unsigned)(size_t)(LDS_AS const char*)p; }

DI void glds16(const void* sbase, unsigned voff, unsigned dst) {
  unsigned keep;
  asm volatile("s_mov_b32 %0, m0\n\ts_mov_b32 m0, %3\n\ts_nop 0\n\tglobal_load_lds_dwordx4 %1, %2\n\ts_mov_b32 m0, %0"
               : "=&s"(keep) : "v"(voff), "s"(sbase), "s"(dst) : "memory");
}
template <int IMM> DI void gload16(u32x4& dst, const void* sbase, unsigned voff) {
  asm volatile("global_load_dwordx4 %0, %1, %2 offset:%3" : "=v"(dst) : "v"(voff), "s"(sbase), "n"(IMM));
}
template <int IMM> DI void hload16(u32x4& dst, const void* sbase, unsigned voff) {
  asm volatile("global_load_dwordx4 %0, %1, %2 offset:%3 sc1" : "=v"(dst) : "v"(voff), "s"(sbase), "n"(IMM));
}
template <int IMM> DI void hload8(u32x2& dst, const void* sbase, unsigned voff) {
  asm volatile("global_load_dwordx2 %0, %1, %2 offset:%3 sc1" : "=v"(dst) : "v"(voff), "s"(sbase), "n"(IMM));
}
DI void hstore16(char* sbase, unsigned voff, u32x4 v) {
  asm volatile("s_nop 4\n\tglobal_store_dwordx4 %0, %1, %2 sc1\n\ts_nop 1" ::"v"(voff), "v"(v), "s"(sbase) : "memory");
}
DI void hstore8(char* sbase, unsigned voff, u32x2 v) {
  asm volatile("s_nop 4\n\tglobal_store_dwordx2 %0, %1, %2 sc1" ::"v"(voff), "v"(v), "s"(sbase) : "memory");
}
DI void hstore4(char* sbase, unsigned voff, unsigned v) {
  asm volatile("s_nop 4\n\tglobal_store_dword %0, %1, %2 sc1" ::"v"(voff), "v"(v), "s"(sbase) : "memory");
}
template <int G> DI void hload16_g(u32x4& dst, const void* sbase, unsigned voff, int grp) {
  asm volatile("s_cmp_lg_u32 %3, %4\n\ts_cbranch_scc1 .Lsk%=\n\tglobal_load_dwordx4 %0, %1, %2 sc1\n.Lsk%=:"
               : "+v"(dst) : "v"(voff), "s"(sbase), "s"(grp), "n"(G) : "scc");
}
template <int G> DI void glds16_g(const void* sbase, unsigned voff, unsigned dst, int grp) {
  unsigned keep;
  asm volatile("s_cmp_lg_u32 %4, %5\n\ts_cbranch_scc1 .Lsk%=\n\ts_mov_b32 %0, m0\n\ts_mov_b32 m0, %3\n\ts_nop 0\n\tglobal_load_lds_dwordx4 %1, %2\n\ts_mov_b32 m0, %0\n.Lsk%=:"
               : "=&s"(keep) : "v"(voff), "s"(sbase), "s"(dst), "s"(grp), "n"(G) : "memory", "scc");
}
template <int IMM> DI void gload8(u32x2& dst, const void* sbase, unsigned voff) {
  asm volatile("global_load_dwordx2 %0, %1, %2 offset:%3" : "=v"(dst) : "v"(voff), "s"(sbase), "n"(IMM));
}
DI float sum_x16(float x) {
  auto r = __builtin_amdgcn_permlane16_swap(__builtin_bit_cast(unsigned, x), __builtin_bit_cast(unsigned, x), false, false);
  return __builtin_bit_cast(float, (unsigned)r[0]) + __builtin_bit_cast(float, (unsigned)r[1]);
}
DI float sum_x32(float x) {
  auto r = __builtin_amdgcn_permlane32_swap(__builtin_bit_cast(unsigned, x), __builtin_bit_cast(unsigned, x), false, false);
  return __builtin_bit_cast(float, (unsigned)r[0]) + __builtin_bit_cast(float, (unsigned)r[1]);
}
template <int N> DI void waitv() { asm volatile("s_waitcnt vmcnt(%0)" ::"n"(N) : "memory"); }
template <int N> DI void waitv_pin(u32x4& x) { asm volatile("s_waitcnt vmcnt(%1)" : "+v"(x) : "n"(N)); }
DI void pin(u32x4& x) { asm volatile("" : "+v"(x)); }
DI u32x4 lds_rd(unsigned addr) { return *(const LDS_AS u32x4*)(size_t)addr; }
DI void bar() { asm volatile("" ::: "memory"); __builtin_amdgcn_s_barrier(); asm volatile("" ::: "memory"); }

__host__ __device__ constexpr int dg_start(int g) { return g < 4 ? 5 * g : 20 + 4 * (g - 4); }
__host__ __device__ constexpr int dg_count(int g) { return g < 4 ? 5 : 4; }

DI int tid_opaque() { int t = threadIdx.x; asm volatile("" : "+v"(t)); return t; }

typedef __attribute__((address_space(1))) unsigned gu32;
constexpr int SY_KA = 0, SY_KB = 16, SY_GU = 32, SY_D = 48, SY_TMO = 64;
DI gu32* sync_word(char* ws, int i) { return (gu32*)(ws + OFF_SYNC + (size_t)i * 256); }
DI void sig_done(char* ws, int i) {
  asm volatile("s_waitcnt vmcnt(0) lgkmcnt(0)" ::: "memory");
  bar();
  if (threadIdx.x == 0) __hip_atomic_fetch_add(sync_word(ws, i), 1u, __ATOMIC_RELAXED, __HIP_MEMORY_SCOPE_AGENT);
}
DI void dep_wait(char* ws, int i, int n, unsigned target) {
  if (threadIdx.x < 64) {
    const int lane = threadIdx.x;
    gu32* wd = sync_word(ws, i + (lane < n ? lane : 0));
    for (unsigned spins = 0;; ++spins) {
      const unsigned v = __hip_atomic_load(wd, __ATOMIC_RELAXED, __HIP_MEMORY_SCOPE_AGENT);
      if (__all(v >= target)) break;
      __builtin_amdgcn_s_sleep(1);
      if (spins > (1u << 20)) { if (lane == 0) __hip_atomic_store(sync_word(ws, SY_TMO), 1u, __ATOMIC_RELAXED, __HIP_MEMORY_SCOPE_AGENT); break; }
    }
  }
  bar();
}

#define PLD4(p) __builtin_nontemporal_load(p)
#define PLD2(p) __builtin_nontemporal_load(p)
DI void st16_wt(void* p, u32x4 v) { asm volatile("global_store_dwordx4 %0, %1, off sc1\n\ts_nop 1" ::"v"(p), "v"(v) : "memory"); }
DI void st16_nt(void* p, u32x4 v) { __builtin_nontemporal_store(v, (u32x4*)p); }
#define RST4(p, v) PST4(p, v)
#define PST4(p, v) (*(u32x4*)(p) = (v))
typedef __attribute__((ext_vector_type(4))) short s16x4;
DI u32x2 lds_tr(unsigned addr) {
  return __builtin_bit_cast(u32x2, __builtin_amdgcn_ds_read_tr16_b64_v4i16((LDS_AS s16x4*)(size_t)addr));
}
DI float dot_ones(unsigned u, float acc) {
  return __builtin_amdgcn_fdot2_f32_bf16(__builtin_bit_cast(bf16x2, u), __builtin_bit_cast(bf16x2, 0x3f803f80u), acc, false);
}
DI void put_colsum(char* chunk0, int kq, float cs) {
  unsigned p = pack2(-cs, 0.f) & 0xffffu;
  float hi = bflo(p);
  unsigned lo = pack2(-cs - hi, 0.f) & 0xffffu;
#pragma unroll
  for (int t = 0; t < 3; ++t) {
    int sl = 3 * kq + t;
    *(unsigned short*)(chunk0 + (sl >> 3) * 512 + (sl & 7) * 2) = (unsigned short)(t == 1 ? lo : p);
  }
  if (kq == 3) *(u32x2*)(chunk0 + 512 + 8) = u32x2{0u, 0u};
}
__host__ __device__ constexpr int gu_frag_index(int blk, int ks) { return blk < 3 ? 3 * ks + blk : 111 + 37 * (blk - 3) + ks; }

constexpr int P_RS = 144;
struct RtRegs { u32x4 v[5]; };
DI void rt_load(int u, int tid, const unsigned short* wg, const unsigned short* wu, const unsigned short* wd, RtRegs& R) {
  if (u < 576) {
    const int m = u & 1, kq = (u >> 1) & 3, fq = u >> 3;
    const unsigned short* wsrc = m ? wu : wg;
#pragma unroll
    for (int i = 0; i < 5; ++i) {
      int ch = tid + 256 * i; if (ch > 1151) ch = 1151;
      R.v[i] = PLD4((const u32x4*)(wsrc + (size_t)(144 * kq + (ch >> 3)) * F + 64 * fq + 8 * (ch & 7)));
    }
  } else {
    const int ub = u - 576, cgp = ub % 9, sg = ub / 9;
#pragma unroll
    for (int i = 0; i < 4; ++i) {
      int ch = tid + 256 * i;
      R.v[i] = PLD4((const u32x4*)(wd + (size_t)(128 * sg + (ch >> 3)) * D + 64 * cgp + 8 * (ch & 7)));
    }
    R.v[4] = R.v[3];
  }
}
DI void rt_stage(int u, int tid, char* sm, const RtRegs& R) {
  const int nch = u < 576 ? 1152 : 1024;
#pragma unroll
  for (int i = 0; i < 5; ++i) {
    int ch = tid + 256 * i;
    if (ch < nch) *(u32x4*)(sm + (ch >> 3) * P_RS + (ch & 7) * 16) = R.v[i];
  }
}
DI void rt_emit(int u, int tid, char* sm, char* ws) {
  const int lane = tid & 63, w = tid >> 6;
  const unsigned sb = lds_addr(sm);
  if (u < 576) {
    const int m = u & 1, kq = (u >> 1) & 3, fq = u >> 3;
    const int fb = 4 * fq + w, fs = fb / 9, blk = fb - 9 * fs;
    const int g = lane >> 4, h = g & 1, kpar = g >> 1, qq = (lane >> 2) & 3, pp = lane & 3, i16 = lane & 15;
    const int pq = (pp >> 1) | ((pp & 1) << 1);
    const unsigned rd = sb + (16 * kpar + 8 * h + qq) * P_RS + w * 32 + pq * 8;
    char* dst = ws + OFF_WGU + (size_t)fs * GU_NFRAG * 1024 + (h * 32 + m * 16 + i16) * 16;
    float cs = 0.f;
#pragma unroll
    for (int k2 = 0; k2 < 5; ++k2) {
      const int kl = 2 * k2 + kpar, back = kl < 9 ? 0 : 16;
      u32x2 lo = lds_tr(rd + (2 * k2 * 16 - back) * P_RS), hi = lds_tr(rd + (2 * k2 * 16 + 4 - back) * P_RS);
      u32x4 o = {lo[0], lo[1], hi[0], hi[1]};
      if (kl < 9) {
        cs = dot_ones(o[0], cs); cs = dot_ones(o[1], cs); cs = dot_ones(o[2], cs); cs = dot_ones(o[3], cs);
        RST4(dst + (size_t)gu_frag_index(blk, 9 * kq + kl) * 1024, o);
      }
    }
    cs += __shfl_xor(cs, 16); cs += __shfl_xor(cs, 32);
    if (lane < 16) put_colsum(ws + OFF_WGU + ((size_t)fs * GU_NFRAG + gu_frag_index(blk, 36)) * 1024 + (m * 16 + lane) * 16, kq, cs);
  } else {
    const int ub = u - 576, cgp = ub % 9, sg = ub / 9;
    const int c = 4 * cgp + w;
    const int g = c < 20 ? c / 5 : 4 + (c - 20) / 4;
    const int st = dg_start(g), n = dg_count(g);
    const int q = lane >> 4, qq = (lane >> 2) & 3, pp = lane & 3;
    const unsigned rd = sb + (8 * q + qq) * P_RS + w * 32 + pp * 8;
#pragma unroll
    for (int sl = 0; sl < 4; ++sl) {
      u32x2 lo = lds_tr(rd + sl * 32 * P_RS), hi = lds_tr(rd + (sl * 32 + 4) * P_RS);
      size_t frag = (size_t)st * 144 + (size_t)(4 * sg + sl) * n + (c - st);
      RST4(ws + OFF_WD + frag * 1024 + lane * 16, (u32x4{lo[0], lo[1], hi[0], hi[1]}));
    }
  }
}

constexpr int PB_WQKV = 64, PB_RT = 450, PB_X = 128, PB_WO = 18, PB_Z = 6;
constexpr int PB_TOTAL = PB_WQKV + PB_X + PB_WO + PB_Z + PB_RT;

__global__ __launch_bounds__(512) void k_prep(const unsigned short* __restrict__ x, const unsigned short* __restrict__ wqkv,
                                              const unsigned short* __restrict__ wo, const unsigned short* __restrict__ wg,
                                              const unsigned short* __restrict__ wu, const unsigned short* __restrict__ wd,
                                              char* __restrict__ ws) {
  __shared__ __attribute__((aligned(16))) char smem[2 * 576 * 40];
  int b = blockIdx.x;
  const int tid = threadIdx.x, lane = tid & 63, w8 = tid >> 6;
  if (b < PB_WQKV) {
    constexpr int RS = 224;
    const int p = b >> 2, kq = b & 3;
    u32x2 v[8];
#pragma unroll
    for (int i = 0; i < 8; ++i) {
      int ch = tid + 512 * i; if (ch > 3887) ch = 3887;
      int row = ch / 27, rem = ch - 27 * row, m = rem / 9, c4 = rem - 9 * m;
      v[i] = PLD2((const u32x2*)(wqkv + (size_t)(144 * kq + row) * (3 * D) + 576 * m + 36 * p + 4 * c4));
    }
    const int blk = w8 & 3, kh = w8 >> 2, h = lane >> 5, rho = lane & 31;
    int col = 108;
    if (blk == 0) col = 36 + rho;
    else if (blk == 1) {
      if (rho < 18) col = 72 + rho;
      else if (rho >= 19 && rho <= 22) col = 36 + 32 + (rho - 19);
      else if (rho >= 23 && rho <= 26) col = 18 * ((rho - 23) >> 1) + 16 + ((rho - 23) & 1);
    } else if (blk == 2) { if (rho < 18) col = 72 + 18 + rho; }
    else col = 18 * (rho >> 4) + 8 * ((rho >> 2) & 1) + 4 * ((rho >> 3) & 1) + (rho & 3);
#pragma unroll
    for (int i = 0; i < 8; ++i) {
      int ch = tid + 512 * i;
      if (ch < 3888) { int row = ch / 27, rem = ch - 27 * row; *(u32x2*)(smem + row * RS + rem * 8) = v[i]; }
    }
    if (tid < 144) *(u32x2*)(smem + tid * RS + 216) = u32x2{0u, 0u};
    __syncthreads();
    const unsigned short* src = (const unsigned short*)(smem + (80 * kh + 8 * h) * RS) + col;
    char* dst = ws + OFF_WQKV + (size_t)p * KA_NFRAG * 1024 + blk * 1024 + lane * 16;
    float cs = 0.f;
#pragma unroll
    for (int kl = 0; kl < 5; ++kl) {
      if (5 * kh + kl < 9) {
        u32x4 o;
#pragma unroll
        for (int i = 0; i < 4; ++i)
          o[i] = (unsigned)src[(kl * 16 + 2 * i) * (RS / 2)] | ((unsigned)src[(kl * 16 + 2 * i + 1) * (RS / 2)] << 16);
        cs = dot_ones(o[0], cs); cs = dot_ones(o[1], cs); cs = dot_ones(o[2], cs); cs = dot_ones(o[3], cs);
        PST4(dst + (size_t)(9 * kq + 5 * kh + kl) * 4096, o);
      }
    }
    cs += __shfl_xor(cs, 32);
    float* cx = (float*)(smem + 144 * RS);
    if (kh == 1 && lane < 32) cx[blk * 32 + lane] = cs;
    __syncthreads();
    if (kh == 0 && lane < 32) put_colsum(ws + OFF_WQKV + ((size_t)p * KA_NFRAG + 144 + blk) * 1024 + lane * 16, kq, cs + cx[blk * 32 + lane]);
    return;
  }
  b -= PB_WQKV;
  if (b < PB_X) {
    const int ks = lane < 36 ? lane : 35;
    u32x4 v0[2], v1[2];
#pragma unroll
    for (int i = 0; i < 2; ++i) {
      const u32x4* src = (const u32x4*)(x + (size_t)(b * 16 + w8 * 2 + i) * D + 16 * ks);
      v0[i] = PLD4(src); v1[i] = PLD4(src + 1);
    }
#pragma unroll
    for (int i = 0; i < 2; ++i) {
      const int token = b * 16 + w8 * 2 + i, tb = token >> 5, r = token & 31;
      if (lane < 36) {
        char* dst = ws + OFF_XF0 + ((size_t)(tb * 36 + ks) * 64 + r) * 16;
        PST4(dst, v0[i]); PST4(dst + 512, v1[i]);
      }
      float s = 0.f;
#pragma unroll
      for (int j = 0; j < 4; ++j) s += bflo(v0[i][j]) + bfhi(v0[i][j]) + bflo(v1[i][j]) + bfhi(v1[i][j]);
      float m16 = s * (1.f / 16.f), q = 0.f;
#pragma unroll
      for (int j = 0; j < 4; ++j) {
        float a = bflo(v0[i][j]) - m16, c = bfhi(v0[i][j]) - m16, d = bflo(v1[i][j]) - m16, e = bfhi(v1[i][j]) - m16;
        q += a * a + c * c + d * d + e * e;
      }
      int g = lane & 7, st = dg_start(g), n = dg_count(g);
      float ms = 0.f, mm[5], qq = 0.f;
#pragma unroll
      for (int j = 0; j < 5; ++j) {
        int sl = st + (j < n ? j : 0);
        mm[j] = __shfl(m16, sl); float qi = __shfl(q, sl);
        if (j < n) { ms += mm[j]; qq += qi; }
      }
      float mean = ms / n;
#pragma unroll
      for (int j = 0; j < 5; ++j) if (j < n) { float d = mm[j] - mean; qq += 16.f * d * d; }
      if (lane < 8) *(f32x2*)(ws + OFF_ST0 + ((size_t)token * 8 + lane) * 8) = f32x2{mean, qq};
    }
    return;
  }
  b -= PB_X;
  if (b < PB_WO) {
    constexpr int RS = 40;
    const int half = tid >> 8, t = tid & 255, w = w8 & 3, c = 2 * b + half;
    char* sm = smem + half * (576 * RS);
    u32x4 v[5];
#pragma unroll
    for (int i = 0; i < 5; ++i) {
      int ch = t + 256 * i; if (ch > 1151) ch = 1151;
      v[i] = PLD4((const u32x4*)(wo + (size_t)(ch >> 1) * D + 16 * c + 8 * (ch & 1)));
    }
#pragma unroll
    for (int i = 0; i < 5; ++i) {
      int ch = t + 256 * i;
      if (ch < 1152) { char* d = sm + (ch >> 1) * RS + (ch & 1) * 16; *(u32x2*)d = u32x2{v[i][0], v[i][1]}; *(u32x2*)(d + 8) = u32x2{v[i][2], v[i][3]}; }
    }
    __syncthreads();
    const unsigned sb = lds_addr(sm);
    const int q = lane >> 4, qq = (lane >> 2) & 3, pp = lane & 3;
    for (int s = w; s < 18; s += 4) {
      int r0, r1;
      if (s < 16) { r0 = (2 * s + (q >> 1)) * 18 + 8 * (q & 1) + qq; r1 = r0 + 4; }
      else { int w0 = 4 * (s - 16) + (q >> 1); r0 = (4 * w0 + qq) * 18 + 16 + (q & 1); r1 = (4 * (w0 + 2) + qq) * 18 + 16 + (q & 1); }
      u32x2 lo = lds_tr(sb + r0 * RS + pp * 8), hi = lds_tr(sb + r1 * RS + pp * 8);
      PST4(ws + OFF_WO + ((size_t)c * 18 + s) * 1024 + lane * 16, (u32x4{lo[0], lo[1], hi[0], hi[1]}));
    }
    return;
  }
  b -= PB_WO;
  if (b < PB_Z) {
#pragma unroll
    for (int i = 0; i < 4; ++i) {
      size_t o = (((size_t)b * 4 + i) * 512 + tid) * 16;
      if (o < SZ_ZERO) PST4(ws + OFF_KV + o, (u32x4{0, 0, 0, 0}));
    }
    return;
  }
  b -= PB_Z;
  {
    const int half = tid >> 8, t = tid & 255, u = 2 * b + half;
    char* sm = smem + half * (144 * P_RS);
    RtRegs R;
    rt_load(u, t, wg, wu, wd, R);
    rt_stage(u, t, sm, R);
    __syncthreads();
    rt_emit(u, t, sm, ws);
  }
}

DI u32x4 mean_frag(float mean, int h) {
  unsigned mh = pack2(mean, 0.f) & 0xffffu;
  unsigned ml = pack2(mean - bflo(mh), 0.f) & 0xffffu;
  unsigned hh = mh | (mh << 16), lh = ml | (mh << 16), hl = mh | (ml << 16);
  return h == 0 ? u32x4{hh, lh, hl, hh} : u32x4{lh, hl, 0u, 0u};
}

DI float phi2(float a, float rs2) {
  const float t = a * rs2;
  return __builtin_fmaf(fmaxf(t, 0.f), 0.6931471805599453f, __builtin_amdgcn_exp2f(fminf(t, 0.f)) + 1e-6f);
}
constexpr int KA_PRO = 12;
constexpr int KA_RING = 37;
constexpr int KA_PD = 8;
template <int KS>
DI void ka_steps(u32x4 (&X)[37], u32x4 (&A)[KA_PD], f32x16 (&acc)[4], const char* xs, unsigned l16, unsigned rd0, unsigned rd1, float mean, int h) {
  if constexpr (KS == 36) X[36] = mean_frag(mean, h);
  if constexpr (KS % 4 == 0 && KS > 0 && KS <= 32) bar();
  if constexpr (KS < 36) {
    constexpr int issued = KS + KA_PRO < 36 ? KS + KA_PRO : 36;
    waitv_pin<issued - KS - 1>(X[KS]);
    if constexpr (KS + KA_PRO < 36) hload16<0>(X[KS + KA_PRO], xs + (KS + KA_PRO) * 1024, l16);
  }
#pragma unroll
  for (int blk = 0; blk < 4; ++blk) {
    const int i = 4 * KS + blk;
    u32x4 a = A[i % KA_PD];
    if (blk < 3) acc[blk] = mfma32(X[KS], a, acc[blk]);
    else acc[3] = mfma32(a, X[KS], acc[3]);
    if (i + KA_PD < KA_NFRAG) { const int f = (i + KA_PD) % (4 * KA_RING); A[i % KA_PD] = lds_rd((f < 64 ? rd0 : (f < 128 ? rd1 : rd1 + 65536)) + (f & 63) * 1024); }
    __builtin_amdgcn_sched_barrier(0);
  }
  if constexpr (KS + 1 < 37) ka_steps<KS + 1>(X, A, acc, xs, l16, rd0, rd1, mean, h);
}
constexpr int KA_PRE = 12;
constexpr int ka_to(int G) { int t = G == 0 ? KA_PRE : 4 * G + 16; if (G > 0 && t > 4 * (G - 1) + KA_RING) t = 4 * (G - 1) + KA_RING; return t < 37 ? t : 37; }
struct KaFoTable { short v[256][4]; };
constexpr KaFoTable ka_make_fo() {
  KaFoTable T{};
  for (int t = 0; t < 256; ++t)
    for (int i = 0; i < 4; ++i) {
      const int it = t + 256 * i;
      int val = -1;
      if (it < 684) {
        const int hs = it >= 342, sl = it - 342 * hs, e = sl / 18, d = sl - 18 * e;
        val = hs == 0 ? d * 33 + e : (d < 14 ? (18 + d) * 33 + e : (32 + d - 14) * 33 + e);
      }
      T.v[t][i] = (short)val;
    }
  return T;
}
__device__ __attribute__((aligned(16))) const KaFoTable KA_FO = ka_make_fo();
template <int G, int GEND> DI void ka_loader(const char* wsrc, unsigned l16, unsigned dst) {
  constexpr int need = 4 * G + 7 < 36 ? 4 * G + 7 : 36;
  constexpr int from = G == 0 ? KA_PRE : ka_to(G - 1);
  constexpr int to = ka_to(G);
  static_assert(to > need && to >= from, "loader schedule");
#pragma unroll
  for (int ks = from; ks < to; ++ks) glds16(wsrc + ks * 4096, l16, dst + (4 * (ks % KA_RING)) * 1024);
  waitv<to - 1 - need>();
  bar();
  if constexpr (G < GEND) ka_loader<G + 1, GEND>(wsrc, l16, dst);
}

DI void ka_phase(char* smem, char* ws, const int lay_) {
  const char* xf = ws + (lay_ ? OFF_XF2 : OFF_XF0);
  const char* st = ws + (lay_ ? OFF_ST2 : OFF_ST0);
  const char* wqkvf = ws + OFF_WQKV;
  char* pqf = ws + OFF_PQF + lay_ * SZ_PQF;
  char* pql = ws + OFF_PQL + lay_ * SZ_PQL;
  double* kv = (double*)(ws + OFF_KV) + (size_t)lay_ * KV_DBL;
  const int tid = tid_opaque(), lane = tid & 63;
  const int w8 = __builtin_amdgcn_readfirstlane(tid >> 6);
  const int w = w8 & 3;
  const int b = blockIdx.x, xcd = b & 7, idx = b >> 3;
  const int tt = 2 * xcd + (idx & 1), p = idx >> 1;
  const int tb = 4 * tt + w;
  const unsigned sb = lds_addr(smem);
  const unsigned l16 = lane * 16;
  const int h = lane >> 5, r = lane & 31;
  STAMP(1, 0);
  if (w8 >= 4) {
    const char* wsrc = wqkvf + (size_t)p * KA_NFRAG * 1024 + w * 1024;
    u32x2 fot;
    gload8<0>(fot, &KA_FO, (tid - 256) * 8);
#pragma unroll
    for (int ks = 0; ks < KA_PRE; ++ks) glds16(wsrc + ks * 4096, l16, sb + w * 1024 + (4 * ks) * 1024);
    if (lay_) dep_wait(ws, SY_D + tt, 1, 16);
    ka_loader<0, 8>(wsrc, l16, sb + w * 1024);
    asm volatile("" : "+v"(fot));
    int fo[3] = {(short)(fot[0] & 0xffff), (short)(fot[0] >> 16), (short)(fot[1] & 0xffff)};
    bar();
    bar();
    float sum[3];
#pragma unroll
    for (int i = 0; i < 3; ++i) {
      const float* rp = (const float*)smem + (fo[i] >= 0 ? fo[i] : 0);
      sum[i] = (rp[0] + rp[1280]) + (rp[2560] + rp[3840]);
    }
#pragma unroll
    for (int i = 0; i < 3; ++i)
      if (fo[i] >= 0) unsafeAtomicAdd(kv + (size_t)(2 * p) * 342 + (tid - 256) + 256 * i, (double)sum[i]);
  } else {
    u32x4 sv[4], X[37];
    const char* xs = xf + (size_t)tb * 36 * 1024;
    if (lay_) dep_wait(ws, SY_D + tt, 1, 16);
    hload16<0>(X[0], xs, l16); hload16<0>(X[1], xs + 1024, l16);
    {
      const char* sp = st + (size_t)(tb * 32) * 64;
      unsigned so = r * 64;
      hload16<0>(sv[0], sp, so); hload16<16>(sv[1], sp, so); hload16<32>(sv[2], sp, so); hload16<48>(sv[3], sp, so);
    }
#pragma unroll
    for (int k = 2; k < KA_PRO; ++k) hload16<0>(X[k], xs + k * 1024, l16);
    STAMP(1, 1);
    float rstd, mean;
    {
      asm volatile("s_waitcnt vmcnt(%4)" : "+v"(sv[0]), "+v"(sv[1]), "+v"(sv[2]), "+v"(sv[3]) : "n"(KA_PRO - 2));
      float m[8], q[8];
#pragma unroll
      for (int i = 0; i < 4; ++i) {
        m[2 * i] = asf(sv[i][0]); q[2 * i] = asf(sv[i][1]);
        m[2 * i + 1] = asf(sv[i][2]); q[2 * i + 1] = asf(sv[i][3]);
      }
      mean = (80.f * (m[0] + m[1] + m[2] + m[3]) + 64.f * (m[4] + m[5] + m[6] + m[7])) * (1.f / 576.f);
      float M2 = 0.f;
#pragma unroll
      for (int i = 0; i < 8; ++i) { float d = m[i] - mean; M2 += q[i] + (i < 4 ? 80.f : 64.f) * d * d; }
      rstd = rsqrtf(M2 * (1.f / 576.f) + 1e-5f);
    }
    f32x16 acc[4];
#pragma unroll
    for (int i = 0; i < 4; ++i)
#pragma unroll
      for (int j = 0; j < 16; ++j) acc[i][j] = 0.f;
    unsigned rd0 = sb + l16, rd1 = rd0 + 65536;
    asm volatile("" : "+v"(rd0), "+v"(rd1));
    bar();
    STAMP(1, 2);
    u32x4 A[KA_PD];
#pragma unroll
    for (int i = 0; i < KA_PD; ++i) A[i] = lds_rd(rd0 + i * 1024);
    __builtin_amdgcn_sched_barrier(0);
    ka_steps<0>(X, A, acc, xs, l16, rd0, rd1, mean, h);
    STAMP(1, 3);
    bar();
    STAMP(1, 6);

    const int c = r;
    float* rs = (float*)(smem + 32768) + w * 160;
    float* ql = rs + 32;
    if (h == 0) rs[r] = rstd;
    f32x4 rr[4];
#pragma unroll
    for (int g = 0; g < 4; ++g) rr[g] = *(const f32x4*)(rs + 8 * g + 4 * h);
    u32x4 k1[2], v1[2], v2[2];
    f32x4 pv[4];
    const bool ph = c >= 19 && c <= 26, one = c == 18;
#pragma unroll
    for (int s = 0; s < 2; ++s)
#pragma unroll
      for (int t = 0; t < 4; ++t) {
        const int r0 = 8 * s + 2 * t;
        const float s0 = rr[r0 >> 2][r0 & 3], s1 = rr[r0 >> 2][(r0 & 3) + 1];
        const float e0 = s0 * 1.4426950408889634f, e1 = s1 * 1.4426950408889634f;
        k1[s][t] = pack2(phi2(acc[0][r0], e0), phi2(acc[0][r0 + 1], e1));
        float a0 = acc[1][r0] * s0, a1 = acc[1][r0 + 1] * s1;
        float p0 = phi2(acc[1][r0], e0), p1 = phi2(acc[1][r0 + 1], e1);
        a0 = ph ? p0 : (one ? 1.f : a0); a1 = ph ? p1 : (one ? 1.f : a1);
        pv[r0 >> 2][r0 & 3] = p0; pv[r0 >> 2][(r0 & 3) + 1] = p1;
        v1[s][t] = pack2(a0, a1);
        v2[s][t] = one ? 0x3f803f80u : pack2(acc[2][r0] * s0, acc[2][r0 + 1] * s1);
      }
    if (c >= 23 && c <= 26) {
#pragma unroll
      for (int g = 0; g < 4; ++g) *(f32x4*)(ql + (c - 23) * 32 + 8 * g + 4 * h) = pv[g];
    }
    STAMP(1, 7);
    f32x16 R1, R2, R3;
#pragma unroll
    for (int j = 0; j < 16; ++j) { R1[j] = 0.f; R2[j] = 0.f; R3[j] = 0.f; }
#pragma unroll
    for (int s = 0; s < 2; ++s) {
      R1 = mfma32(k1[s], v1[s], R1);
      R2 = mfma32(k1[s], v2[s], R2);
      R3 = mfma32(v1[s], v2[s], R3);
    }
    float* red = (float*)smem + w * 1280;
#pragma unroll
    for (int j = 0; j < 16; ++j) {
      int row = (j & 3) + 8 * (j >> 2) + 4 * h;
      red[row * 33 + c] = row < 18 ? R1[j] : R2[j];
    }
    if (h == 0) red[32 * 33 + c] = R3[11];
    else { red[33 * 33 + c] = R3[8]; red[34 * 33 + c] = R3[9]; red[35 * 33 + c] = R3[10]; }
    STAMP(1, 4);
    asm volatile("s_waitcnt lgkmcnt(0)" ::: "memory");
    bar();
    {
      u32x4 qa, qb;
      const float rse = rstd * 1.4426950408889634f;
#pragma unroll
      for (int t = 0; t < 4; ++t) {
        qa[t] = pack2(phi2(acc[3][2 * t], rse), phi2(acc[3][2 * t + 1], rse));
        qb[t] = pack2(phi2(acc[3][8 + 2 * t], rse), phi2(acc[3][8 + 2 * t + 1], rse));
      }
      char* dst = pqf + ((size_t)tb * 32 + 2 * p) * 1024;
      hstore16(dst, l16, qa); hstore16(dst + 1024, l16, qb);
      float la = ql[h * 32 + r], lb = ql[(2 + h) * 32 + r];
      hstore4(pql + ((size_t)tb * 4 + (p >> 2)) * 1024 + 4 * (p & 3), l16, pack2(la, lb));
    }
  }
  STAMP(1, 8);
  sig_done(ws, SY_KA + 8 * lay_ + xcd);
  STAMP(1, 9);
}

constexpr int KB_LDS = 36 * 1024 + 16 * 1024 + 9 * 32 * 8;
constexpr int KB_KVI = 57344;
struct KbTable { unsigned short v[64][16]; };
constexpr KbTable kb_make_table() {
  KbTable T{};
  for (int l = 0; l < 64; ++l)
    for (int k = 0; k < 16; ++k) {
      const int q = l + 64 * k;
      int val = 4000;
      if (q < 684) { const int hd = q / 171, r = q - 171 * hd, e = r / 9, d2 = r - 9 * e; val = hd * 912 + e * 48 + d2 * 4; }
      T.v[l][k] = (unsigned short)val;
    }
  return T;
}
__device__ __attribute__((aligned(16))) const KbTable KB_TAB = kb_make_table();
DI void kb_phase(char* smem, char* ws, const int lay_) {
  const char* pqf = ws + OFF_PQF + lay_ * SZ_PQF;
  const char* pql = ws + OFF_PQL + lay_ * SZ_PQL;
  const double* kv = (const double*)(ws + OFF_KV) + (size_t)lay_ * KV_DBL;
  const char* wof = ws + OFF_WO;
  const char* xin = ws + (lay_ ? OFF_XF2 : OFF_XF0);
  char* xout = ws + OFF_XF1 + lay_ * SZ_XF;
  char* stout = ws + OFF_ST1 + lay_ * SZ_ST1;
  const int tid = tid_opaque(), lane = tid & 63;
  const int w = __builtin_amdgcn_readfirstlane(tid >> 6);
  const int b = blockIdx.x, xcd = b & 7, idx = b >> 3;
  const int tb = 8 * xcd + (idx & 7), cg = idx >> 3;
  const int ct = 9 * cg + w, ct8 = 9 * cg + 8;
  const int h = lane >> 5, r = lane & 31, q = lane >> 4, t = lane & 15;

  STAMP(2, 0);
  u32x4 A[18], A8[3];
  const char* wp = wof + (size_t)ct * 18 * 1024;
  const char* wp8 = wof + ((size_t)ct8 * 18 + w) * 1024;
  const unsigned l16 = lane * 16;
  u32x2 res[2], res8;
  const unsigned xq = ((q >> 1) * 32 + t) * 16 + (q & 1) * 8;
  const unsigned xo = (tb * 36 + ct) * 1024 + xq;
  const unsigned xo8 = (tb * 36 + ct8) * 1024 + xq + (w & 1) * 256;
#define KB_LDA(s_) gload16<((s_) & 3) * 1024>(A[s_], wp + ((s_) >> 2) * 4096, l16)
  u32x4 tab[2];
  gload16<0>(tab[0], &KB_TAB, lane * 32); gload16<16>(tab[1], &KB_TAB, lane * 32);
  gload16<0>(A8[0], wp8, l16); gload16<0>(A8[1], wp8 + 8192, l16); gload16<0>(A8[2], wp8 + (w < 2 ? 16384 : 0), l16);
  KB_LDA(0); KB_LDA(1); KB_LDA(2); KB_LDA(3); KB_LDA(4); KB_LDA(5); KB_LDA(6); KB_LDA(7); KB_LDA(8);
  KB_LDA(9); KB_LDA(10); KB_LDA(11); KB_LDA(12); KB_LDA(13); KB_LDA(14); KB_LDA(15); KB_LDA(16); KB_LDA(17);
  STAMP(2, 6);
  dep_wait(ws, SY_KA + 8 * lay_, 8, 32);
  STAMP(2, 7);

  u32x4 kd[11];
  u32x4 bq[4], bl;
  {
    const char* src = (const char*)(kv + (size_t)(4 * w) * 342);
#pragma unroll
    for (int k = 0; k < 10; ++k) hload16<0>(kd[k], src + k * 1024, l16);
    hload16<0>(kd[10], src + 10240, l16);
  }
  hload16<0>(bl, pql + ((size_t)tb * 4 + (w >> 1)) * 1024, l16);
#pragma unroll
  for (int i = 0; i < 4; ++i) hload16<0>(bq[i], pqf + ((size_t)tb * 32 + 4 * w + i) * 1024, l16);
  hload8<0>(res[0], xin, xo); hload8<256>(res[1], xin, xo); hload8<0>(res8, xin, xo8);
  STAMP(2, 1);
  char* kvi = smem + KB_KVI + w * 4096;
  asm volatile("s_waitcnt vmcnt(8)" : "+v"(kd[0]), "+v"(kd[1]), "+v"(kd[2]), "+v"(kd[3]), "+v"(kd[4]), "+v"(kd[5]), "+v"(kd[6]), "+v"(kd[7]),
               "+v"(kd[8]), "+v"(kd[9]), "+v"(kd[10]), "+v"(tab[0]), "+v"(tab[1]));
#pragma unroll
  for (int k = 0; k < 11; ++k) {
    const double d0 = __builtin_bit_cast(double, u32x2{kd[k][0], kd[k][1]}), d1 = __builtin_bit_cast(double, u32x2{kd[k][2], kd[k][3]});
    const unsigned tw = tab[k >> 3][(k >> 1) & 3];
    const unsigned off = (k & 1) ? (tw >> 16) : (tw & 0xffffu);
    *(unsigned*)(kvi + off) = pack2((float)d0, (float)d1);
  }
  const bool valid = r < 18 || r == 20 || r == 21;
  const int er = r < 16 ? 8 * ((r >> 2) & 1) + 4 * (r >> 3) + (r & 3) : (r == 16 ? 16 : (r == 20 ? 17 : (valid ? 18 : 0)));
  asm volatile("s_waitcnt vmcnt(3)" : "+v"(bl), "+v"(bq[0]), "+v"(bq[1]), "+v"(bq[2]), "+v"(bq[3]));
  float lo[4];
#pragma unroll
  for (int i = 0; i < 4; ++i) {
    const char* row = kvi + i * 912 + er * 48;
    u32x4 a1 = *(const u32x4*)(row + 16 * h), a2;
    const unsigned l16v = *(const unsigned short*)(row + 32 + 2 * h);
    if (!valid) a1 = u32x4{0u, 0u, 0u, 0u};
    const unsigned lv = valid ? ((i & 1) ? (l16v << 16) : l16v) : 0u;
    const int dw = 2 * (w & 1) + (i >> 1);
#pragma unroll
    for (int k = 0; k < 4; ++k) a2[k] = (k == dw) ? lv : 0u;
    f32x16 n;
#pragma unroll
    for (int j = 0; j < 16; ++j) n[j] = 0.f;
    n = mfma32(a1, bq[i], n);
    n = mfma32(a2, bl, n);
    float rd = __builtin_amdgcn_rcpf(n[9] + 1e-6f);
    u32x4 o;
#pragma unroll
    for (int k = 0; k < 4; ++k) o[k] = pack2(n[2 * k] * rd, n[2 * k + 1] * rd);
    lo[i] = n[8] * rd;
    const int head = 4 * w + i, s = head >> 1, qq = 2 * (head & 1) + h;
    *(u32x4*)(smem + (((r >> 4) * 18 + s) * 64 + qq * 16 + (r & 15)) * 16) = o;
  }
  {
    const int s = 16 + (w >> 2), qq = 2 * (w & 1) + h, half = (w >> 1) & 1;
    *(u32x2*)(smem + (((r >> 4) * 18 + s) * 64 + qq * 16 + (r & 15)) * 16 + half * 8) = u32x2{pack2(lo[0], lo[1]), pack2(lo[2], lo[3])};
  }
  STAMP(2, 2);
  asm volatile("s_waitcnt lgkmcnt(0)" ::: "memory");
  bar();
  STAMP(2, 3);
  {
    asm volatile("" : "+v"(A8[0]), "+v"(A8[1]), "+v"(A8[2]));
    f32x4 p8[2] = {{0.f, 0.f, 0.f, 0.f}, {0.f, 0.f, 0.f, 0.f}};
#pragma unroll
    for (int j = 0; j < 3; ++j) {
      const int s = w + 8 * j;
      if (j < 2 || w < 2) {
        u32x4 b0 = *(const u32x4*)(smem + (s * 64 + lane) * 16);
        u32x4 b1 = *(const u32x4*)(smem + ((18 + s) * 64 + lane) * 16);
        p8[0] = mfma16(A8[j], b0, p8[0]);
        p8[1] = mfma16(A8[j], b1, p8[1]);
      }
    }
    *(f32x4*)(smem + 36 * 1024 + ((w * 2 + 0) * 64 + lane) * 16) = p8[0];
    *(f32x4*)(smem + 36 * 1024 + ((w * 2 + 1) * 64 + lane) * 16) = p8[1];
  }
  f32x4 acc[2][2] = {{{0.f, 0.f, 0.f, 0.f}, {0.f, 0.f, 0.f, 0.f}}, {{0.f, 0.f, 0.f, 0.f}, {0.f, 0.f, 0.f, 0.f}}};
  {
    u32x4 b0[18], b1[18];
#pragma unroll
    for (int s = 0; s < 18; ++s) {
      b0[s] = *(const u32x4*)(smem + (s * 64 + lane) * 16);
      b1[s] = *(const u32x4*)(smem + ((18 + s) * 64 + lane) * 16);
    }
    __builtin_amdgcn_sched_barrier(0);
#pragma unroll
    for (int s = 0; s < 18; ++s) pin(A[s]);
#pragma unroll
    for (int s = 0; s < 18; ++s) {
      acc[0][s & 1] = mfma16(A[s], b0[s], acc[0][s & 1]);
      acc[1][s & 1] = mfma16(A[s], b1[s], acc[1][s & 1]);
    }
#pragma unroll
    for (int j = 0; j < 4; ++j) { acc[0][0][j] += acc[0][1][j]; acc[1][0][j] += acc[1][1][j]; }
  }
  STAMP(2, 4);
  asm volatile("s_waitcnt vmcnt(0) lgkmcnt(0)" : "+v"(res[0]), "+v"(res[1]), "+v"(res8) :: "memory");
  bar();
  f32x2* sred = (f32x2*)(smem + 52 * 1024);
  auto finish = [&](const f32x4& a, u32x2 rs, unsigned off, int slot, int tok) {
    float v0 = bflo(rs[0]) + a[0], v1 = bfhi(rs[0]) + a[1], v2 = bflo(rs[1]) + a[2], v3 = bfhi(rs[1]) + a[3];
    u32x2 o = {pack2(v0, v1), pack2(v2, v3)};
    hstore8(xout, off, o);
    v0 = bflo(o[0]); v1 = bfhi(o[0]); v2 = bflo(o[1]); v3 = bfhi(o[1]);
    float s = sum_x32(sum_x16((v0 + v1) + (v2 + v3)));
    float m = s * (1.f / 16.f);
    float d0 = v0 - m, d1 = v1 - m, d2 = v2 - m, d3 = v3 - m;
    float qq = sum_x32(sum_x16(d0 * d0 + d1 * d1 + d2 * d2 + d3 * d3));
    if (q == 0) sred[slot * 32 + tok] = f32x2{m, qq};
  };
  finish(acc[0][0], res[0], xo, w, t);
  finish(acc[1][0], res[1], xo + 256, w, 16 + t);
  if (w < 2) {
    f32x4 a = {0.f, 0.f, 0.f, 0.f};
#pragma unroll
    for (int i = 0; i < 8; ++i) {
      f32x4 pv = *(const f32x4*)(smem + 36 * 1024 + ((i * 2 + (w & 1)) * 64 + lane) * 16);
      a[0] += pv[0]; a[1] += pv[1]; a[2] += pv[2]; a[3] += pv[3];
    }
    finish(a, res8, xo8, 8, 16 * (w & 1) + t);
  }
  asm volatile("s_waitcnt lgkmcnt(0)" ::: "memory");
  bar();
  if (tid < 32) {
    float mm[9], ms = 0.f, qq = 0.f;
#pragma unroll
    for (int i = 0; i < 9; ++i) { f32x2 v = sred[i * 32 + tid]; mm[i] = v[0]; ms += v[0]; qq += v[1]; }
    float mean = ms * (1.f / 9.f);
#pragma unroll
    for (int i = 0; i < 9; ++i) { float d = mm[i] - mean; qq += 16.f * d * d; }
    hstore8(stout, ((tb * 32 + tid) * 4 + cg) * 8, u32x2{__builtin_bit_cast(unsigned, mean), __builtin_bit_cast(unsigned, qq)});
  }
  STAMP(2, 5);
  sig_done(ws, SY_KB + 8 * lay_ + xcd);
  STAMP(2, 9);
}

#define GU_STAGE_ 32
#define GU_RING_ 128
constexpr int GU_STAGE = GU_STAGE_, GU_RING = GU_RING_, GU_P1 = 111;
constexpr int GU_PD = 5;
constexpr int GU_NSLOT = GU_RING / GU_STAGE, GU_NPIECE = (GU_NFRAG + 7) / 8;
constexpr int GU_NST = (GU_NFRAG + GU_STAGE - 1) / GU_STAGE;
struct GuSched { int n; int kind[96]; int arg[96]; int step[96]; };
#define GU_PRO_X_ 10
#define GU_PRO_P_ 8
#define GU_PACE 5
#define GU_LAG_ 1
constexpr int GU_PRO_X = GU_PRO_X_;
constexpr int GU_PRO_P = GU_PRO_P_;
constexpr int gu_pstage(int p) { return 8 * p / GU_STAGE; }
constexpr int gu_pdead(int p) { int st = gu_pstage(p); return st == 0 ? 0 : st * GU_STAGE - GU_PD; }
constexpr int gu_pearly(int p) { int st = gu_pstage(p); return st < GU_NSLOT ? 0 : (st - GU_NSLOT + 2) * GU_STAGE - GU_PD + 2; }
constexpr GuSched gu_make_sched() {
  GuSched s{};
  int n = 0;
  auto push = [&](int k, int a, int st) { s.kind[n] = k; s.arg[n] = a; s.step[n] = st; ++n; };
  int kx = 0, pc = 0;
  while (pc < GU_PRO_P) push(1, pc++, -2);
  push(2, 0, -1); push(2, 1, -1);
  while (kx < GU_PRO_X) push(0, kx++, -1);
  int m = 0, last = 0;
  while (kx < 36 || pc < GU_NPIECE) {
    int dx = kx < 36 ? 3 * kx : 100000, ds = pc < GU_NPIECE ? gu_pdead(pc) : 100000;
    bool takep = ds <= dx;
    int st = kx < 36 ? (m * GU_PACE) / 2 : last + 5;
    if (takep && st < gu_pearly(pc)) { if (kx < 36) { takep = false; } else st = gu_pearly(pc); }
    if (st < last) st = last;
    if (takep) push(1, pc++, st); else push(0, kx++, st);
    last = st; ++m;
  }
  s.n = n;
  return s;
}
constexpr GuSched GUS = gu_make_sched();
constexpr int GU_LAG = GU_LAG_;
constexpr int gu_cnt_raw(int step) { int c = 0; for (int i = 0; i < GUS.n; ++i) if (GUS.step[i] < step) ++c; return c; }
constexpr int gu_cnt_before(int step) { return gu_cnt_raw(step - GU_LAG < 0 ? 0 : step - GU_LAG); }
constexpr int gu_idx(int kind, int arg) { for (int i = 0; i < GUS.n; ++i) if (GUS.kind[i] == kind && GUS.arg[i] == arg) return i; return -1; }
constexpr int gu_clamp(int n) { return n > 63 ? 63 : (n < 0 ? 0 : n); }
constexpr int gu_wait_for(int idx, int step) { return gu_clamp(gu_cnt_before(step) - idx - 1); }
constexpr int gu_last_piece(int st) { int p = (st + 1) * GU_STAGE / 8 - 1; return gu_idx(1, p < GU_NPIECE ? p : GU_NPIECE - 1); }
constexpr bool gu_sched_ok() {
  for (int i = 0; i < GUS.n; ++i) {
    if (GUS.kind[i] == 1) { if (GUS.step[i] + GU_LAG >= gu_pdead(GUS.arg[i]) && gu_pdead(GUS.arg[i]) > 0) return false;
                            if (gu_pdead(GUS.arg[i]) == 0 && GUS.step[i] >= 0) return false;
                            if (gu_pearly(GUS.arg[i]) > 0 && GUS.step[i] < gu_pearly(GUS.arg[i]) - 2) return false; }
    if (GUS.kind[i] == 0 && GUS.step[i] >= 0 && GUS.step[i] + GU_LAG >= 3 * GUS.arg[i]) return false;
    if (i > 0 && GUS.step[i] < GUS.step[i - 1]) return false;
  }
  return true;
}
static_assert(gu_sched_ok(), "GU schedule");
static_assert(GU_PRO_P >= GU_STAGE / 8, "stage 0 must be requested in the prologue");

struct GuCtx {
  const char* wsrc; const char* xs; const char* sp; unsigned l16, sb, so; int w, grp, h; unsigned rd0, rd1, rd2; float c1, c3, cinv; char* hbase; unsigned hvoff; int fb0;
};
template <int N, int G> DI void gu_issue(const GuCtx& c, u32x4 (&X)[37], u32x4 (&sv)[2]) {
  constexpr int kind = GUS.kind[N], arg = GUS.arg[N];
  if constexpr (kind == 0) {
    if constexpr (G < 0) hload16<0>(X[arg], c.xs + arg * 1024, c.l16);
    else hload16_g<G>(X[arg], c.xs + arg * 1024, c.l16, c.grp);
  }
  if constexpr (kind == 1) {
    constexpr int piece = arg * 8;
    if constexpr (G < 0) glds16(c.wsrc + piece * 1024, c.l16, c.sb + (piece % GU_RING) * 1024 + c.w * 1024);
    else glds16_g<G>(c.wsrc + piece * 1024, c.l16, c.sb + (piece % GU_RING) * 1024 + c.w * 1024, c.grp);
  }
  if constexpr (kind == 2) hload16<16 * arg>(sv[arg], c.sp, c.so);
}
template <int N, int STEP, int G> DI void gu_issue_step(const GuCtx& c, u32x4 (&X)[37], u32x4 (&sv)[2]) {
  if constexpr (N < GUS.n) {
    if constexpr (GUS.step[N] == STEP) gu_issue<N, G>(c, X, sv);
    if constexpr (GUS.step[N] <= STEP) gu_issue_step<N + 1, STEP, G>(c, X, sv);
  }
}
constexpr int gu_epi_start(int j) { int a = GU_P1 + 2 + 24 * j, e = (j < 3 ? GU_P1 : GU_P1 + 37 * (j - 2)) + 2; return a > e ? a : e; }
constexpr int gu_epi_block(int I) { for (int j = 0; j < 8; ++j) if (I >= gu_epi_start(j) && I < gu_epi_start(j) + 24) return j; return -1; }
struct GuEpi { float x, ab; unsigned pk[4]; float olo; };
template <int E> DI void gu_epi_0(const f32x16& a, const GuCtx& c, GuEpi& e) {
  float g = a[E];
  e.x = g * __builtin_fmaf(g * g, c.c3, c.c1);
}
template <int E> DI void gu_epi_1(const f32x16& a, const GuCtx& c, GuEpi& e) {
  float ex = __builtin_amdgcn_exp2f(e.x);
  e.ab = a[E] * a[E + 8];
  e.x = __builtin_fmaf(ex, c.cinv, c.cinv);
}
template <int E> DI void gu_epi_2(const GuCtx& c, GuEpi& e, int fb) {
  float o = e.ab * __builtin_amdgcn_rcpf(e.x);
  if constexpr ((E & 1) == 0) e.olo = o; else e.pk[E >> 1] = pack2(e.olo, o);
  if constexpr (E == 7) hstore16(c.hbase + (size_t)(fb >> 1) * 4096 + (fb & 1) * 512, c.hvoff, u32x4{e.pk[0], e.pk[1], e.pk[2], e.pk[3]});
}
template <int I>
DI void gu_steps(GuCtx& c, u32x4 (&A)[GU_PD], u32x4 (&X)[37], u32x4 (&sv)[2], f32x16 (&acc)[4], GuEpi& ep) {
  if constexpr ((I + GU_PD) % GU_STAGE == 0 && (I + GU_PD) / GU_STAGE < GU_NST) {
    constexpr int s = (I + GU_PD) / GU_STAGE;
    waitv<gu_wait_for(gu_last_piece(s), I)>();
    bar();
  }
  constexpr int blk = I < GU_P1 ? I % 3 : 3 + (I - GU_P1) / 37;
  constexpr int ks = I < GU_P1 ? I / 3 : (I - GU_P1) % 37;
  constexpr int slot = blk & 3;
  if constexpr (I < 108 && I % 3 == 0 && ks >= 2) waitv_pin<gu_wait_for(gu_idx(0, ks), I)>(X[ks]);
  gu_issue_step<gu_cnt_raw(I), I, 0>(c, X, sv);
  if constexpr (I >= GU_LAG) gu_issue_step<gu_cnt_raw(I - GU_LAG), I - GU_LAG, 1>(c, X, sv);
  if constexpr (I >= GU_P1 && ks == 0) {
    f32x16 z;
#pragma unroll
    for (int j = 0; j < 16; ++j) z[j] = 0.f;
    acc[slot] = mfma32(A[I % GU_PD], X[ks], z);
  } else {
    acc[slot] = mfma32(A[I % GU_PD], X[ks], acc[slot]);
  }
  if constexpr (I + GU_PD < GU_NFRAG) {
    constexpr int fr = (I + GU_PD) % GU_RING;
    A[I % GU_PD] = lds_rd((fr < 64 ? c.rd0 : (fr < 128 ? c.rd1 : c.rd2)) + (fr & 63) * 1024);
  }
  __builtin_amdgcn_sched_barrier(0);
  {
    constexpr int j = gu_epi_block(I);
    if constexpr (j >= 0) {
      constexpr int d = I - gu_epi_start(j);
      if constexpr (d % 3 == 0) gu_epi_0<d / 3>(acc[j & 3], c, ep);
      if constexpr (d % 3 == 1) gu_epi_1<d / 3>(acc[j & 3], c, ep);
      if constexpr (d % 3 == 2) gu_epi_2<d / 3>(c, ep, c.fb0 + j);
    }
  }
  __builtin_amdgcn_sched_barrier(0);
  if constexpr (I + 1 < GU_NFRAG) gu_steps<I + 1>(c, A, X, sv, acc, ep);
}

DI void gu_phase(char* smem, char* ws, const int lay_) {
  const char* xf = ws + OFF_XF1 + lay_ * SZ_XF;
  const char* st = ws + OFF_ST1 + lay_ * SZ_ST1;
  const char* wguf = ws + OFF_WGU;
  char* hf = ws + OFF_HF + lay_ * SZ_HF;
  const int tid = tid_opaque(), lane = tid & 63;
  const int w = __builtin_amdgcn_readfirstlane(tid >> 6);
  const int b = blockIdx.x, xcd = b & 7, idx = b >> 3;
  const int tt = idx & 7, fs = 4 * xcd + (idx >> 3);
  const int tb32 = 8 * tt + w;
  const unsigned sb = lds_addr(smem), l16 = lane * 16;
  const int h = lane >> 5, r = lane & 31;

  STAMP(3, 0);
  GuCtx c;
  c.wsrc = wguf + (size_t)fs * GU_NFRAG * 1024 + w * 1024;
  c.xs = xf + (size_t)tb32 * 36 * 1024;
  c.sp = st + (size_t)(tb32 * 32) * 32; c.so = r * 32;
  c.l16 = l16; c.sb = sb; c.w = w; c.grp = w >> 2; c.h = h; c.fb0 = 9 * fs;
  c.c1 = 0.f; c.c3 = 0.f; c.cinv = 0.f;
  u32x4 sv[2], X[37];
  gu_issue_step<0, -2, -1>(c, X, sv);
  STAMP(3, 8);
  dep_wait(ws, SY_KB + 8 * lay_ + tt, 1, 32);
  STAMP(3, 9);
  gu_issue_step<gu_cnt_raw(-1), -1, -1>(c, X, sv);
#pragma unroll
  for (int k = GU_PRO_X; k < 36; ++k) asm volatile("" : "=v"(X[k]));
  STAMP(3, 1);
  f32x16 acc[4];
#pragma unroll
  for (int i = 0; i < 3; ++i)
#pragma unroll
    for (int j = 0; j < 16; ++j) acc[i][j] = 0.f;
  {
    asm volatile("s_waitcnt vmcnt(%2)" : "+v"(sv[0]), "+v"(sv[1]) : "n"(gu_wait_for(gu_idx(2, 1), 0)));
    float m0 = asf(sv[0][0]), q0 = asf(sv[0][1]), m1 = asf(sv[0][2]), q1 = asf(sv[0][3]);
    float m2 = asf(sv[1][0]), q2 = asf(sv[1][1]), m3 = asf(sv[1][2]), q3 = asf(sv[1][3]);
    float mean = 0.25f * ((m0 + m1) + (m2 + m3));
    float d0 = m0 - mean, d1 = m1 - mean, d2 = m2 - mean, d3 = m3 - mean;
    float M2 = (q0 + q1) + (q2 + q3) + 144.f * (d0 * d0 + d1 * d1 + d2 * d2 + d3 * d3);
    c.cinv = M2 * (1.f / 576.f) + 1e-5f;
    float rstd = rsqrtf(c.cinv);
    c.c1 = -2.3022081985f * rstd;
    c.c3 = -0.1029432397f * rstd * rstd * rstd;
    X[36] = mean_frag(mean, h);
  }
  waitv<gu_wait_for(gu_last_piece(0) > gu_idx(0, 1) ? gu_last_piece(0) : gu_idx(0, 1), 0)>();
  pin(X[0]); pin(X[1]);
  bar();
  STAMP(3, 2);
  c.rd0 = sb + l16; c.rd1 = sb + l16 + 65536; c.rd2 = sb + l16 + 131072;
  asm volatile("" : "+v"(c.rd0), "+v"(c.rd1));
  if (GU_RING > 128) asm volatile("" : "+v"(c.rd2));
  c.hbase = hf + (size_t)(tb32 >> 1) * 144 * 4096;
  c.hvoff = (2 * (tb32 & 1) + (r >> 4)) * 1024 + h * 256 + (r & 15) * 16;
  u32x4 A[GU_PD];
#pragma unroll
  for (int i = 0; i < GU_PD; ++i) A[i] = lds_rd(c.rd0 + i * 1024);
  GuEpi ep;
  __builtin_amdgcn_sched_barrier(0);
  gu_steps<0>(c, A, X, sv, acc, ep);
  STAMP(3, 3);
  {
    float o[8];
#pragma unroll
    for (int e = 0; e < 8; ++e) {
      float g = acc[0][e];
      float ex = __builtin_amdgcn_exp2f(g * __builtin_fmaf(g * g, c.c3, c.c1));
      o[e] = g * acc[0][e + 8] * __builtin_amdgcn_rcpf(__builtin_fmaf(ex, c.cinv, c.cinv));
    }
    const int fb = c.fb0 + 8;
    hstore16(c.hbase + (size_t)(fb >> 1) * 4096 + (fb & 1) * 512, c.hvoff,
                u32x4{pack2(o[0], o[1]), pack2(o[2], o[3]), pack2(o[4], o[5]), pack2(o[6], o[7])});
  }
  STAMP(3, 4);
  sig_done(ws, SY_GU + 8 * lay_ + tt);
  STAMP(3, 10);
}

constexpr int D_DEPTH = 4;
template <int NC, int Sx> DI void d_load_b(u32x4 (&Bf)[D_DEPTH][4], const char* hs, unsigned l16) {
  constexpr int sl = Sx % D_DEPTH;
  const char* hp = hs + Sx * 4096;
  hload16<0>(Bf[sl][0], hp, l16); hload16<1024>(Bf[sl][1], hp, l16); hload16<2048>(Bf[sl][2], hp, l16); hload16<3072>(Bf[sl][3], hp, l16);
}
template <int NC, int Sx> DI void d_load_a(u32x4 (&Af)[D_DEPTH][5], const char* ws_, unsigned l16) {
  constexpr int sl = Sx % D_DEPTH;
  const char* wp = ws_ + Sx * NC * 1024;
  gload16<0>(Af[sl][0], wp, l16); gload16<1024>(Af[sl][1], wp, l16); gload16<2048>(Af[sl][2], wp, l16); gload16<3072>(Af[sl][3], wp, l16);
  if constexpr (NC == 5) gload16<0>(Af[sl][4], wp + 4096, l16);
}
struct DRes { const char* x1; unsigned off; u32x4 v[2]; };
constexpr int d_wait_count(int NC, int s) {
  const int per = 4 + NC;
  const int later = s < 36 - D_DEPTH ? s : 36 - D_DEPTH;
  const int issued = D_DEPTH * per + later * per;
  const int last = s < D_DEPTH ? D_DEPTH * NC + 4 * (s + 1) - 1 : D_DEPTH * per + (s - D_DEPTH + 1) * per - 1;
  return issued - 1 - last;
}
template <int NC, int Sx>
DI void d_steps(u32x4 (&Bf)[D_DEPTH][4], u32x4 (&Af)[D_DEPTH][5], f32x4 (&acc)[4][5], const char* hs, const char* ws_, unsigned l16, DRes& rs) {
  constexpr int sl = Sx % D_DEPTH;
  constexpr int n = d_wait_count(NC, Sx);
  if constexpr (NC == 5)
    asm volatile("s_waitcnt vmcnt(%9)" : "+v"(Bf[sl][0]), "+v"(Bf[sl][1]), "+v"(Bf[sl][2]), "+v"(Bf[sl][3]), "+v"(Af[sl][0]), "+v"(Af[sl][1]),
                 "+v"(Af[sl][2]), "+v"(Af[sl][3]), "+v"(Af[sl][4]) : "n"(n));
  else
    asm volatile("s_waitcnt vmcnt(%8)" : "+v"(Bf[sl][0]), "+v"(Bf[sl][1]), "+v"(Bf[sl][2]), "+v"(Bf[sl][3]), "+v"(Af[sl][0]), "+v"(Af[sl][1]),
                 "+v"(Af[sl][2]), "+v"(Af[sl][3]) : "n"(n));
#pragma unroll
  for (int c = 0; c < NC; ++c)
#pragma unroll
    for (int t = 0; t < 4; ++t) acc[t][c] = mfma16(Af[sl][c], Bf[sl][t], acc[t][c]);
  if constexpr (Sx + D_DEPTH < 36) {
    __builtin_amdgcn_sched_barrier(0);
    d_load_b<NC, Sx + D_DEPTH>(Bf, hs, l16);
    d_load_a<NC, Sx + D_DEPTH>(Af, ws_, l16);
  }
  if constexpr (Sx == 36 - D_DEPTH + 1) {
    __builtin_amdgcn_sched_barrier(0);
    hload16<0>(rs.v[0], rs.x1, rs.off); hload16<512>(rs.v[1], rs.x1, rs.off);
  }
  if constexpr (Sx + 1 < 36) d_steps<NC, Sx + 1>(Bf, Af, acc, hs, ws_, l16, rs);
}
constexpr int D_STRIDE = 84;
template <int NC>
DI void d_body(char* smem, char* ws, char* dout, const int lay_, int tt, int g, int tid, int lane, int w8) {
  const char* hf = ws + OFF_HF + lay_ * SZ_HF;
  const char* wdf = ws + OFF_WD;
  char* xout = ws + OFF_XF2;
  char* stout = ws + OFF_ST2;
  const unsigned l16 = lane * 16;
  const int st = dg_start(g);
  DRes rs;
  rs.x1 = ws + OFF_XF1 + lay_ * SZ_XF;
  const bool has = tid < 64 * NC;
  const int it = has ? tid : 0;
  int token, kl;
  if (lay_) { token = it / NC; kl = it - token * NC; } else { token = it & 63; kl = it >> 6; }
  const int gt = tt * 64 + token, ks = st + kl;
  rs.off = (((gt >> 5) * 36 + ks) * 64 + (gt & 31)) * 16;
  float* red = (float*)smem;
  STAMP(4, 0);
  if (w8 < 4) {
    const char* hs = hf + ((size_t)(tt * 144 + 36 * w8) * 4) * 1024;
    const char* ws_ = wdf + ((size_t)st * 144 + (size_t)36 * w8 * NC) * 1024;
    u32x4 Bf[D_DEPTH][4], Af[D_DEPTH][5];
    f32x4 acc[4][5];
#pragma unroll
    for (int t = 0; t < 4; ++t)
#pragma unroll
      for (int c = 0; c < 5; ++c) acc[t][c] = f32x4{0.f, 0.f, 0.f, 0.f};
    d_load_a<NC, 0>(Af, ws_, l16); d_load_a<NC, 1>(Af, ws_, l16); d_load_a<NC, 2>(Af, ws_, l16); d_load_a<NC, 3>(Af, ws_, l16);
    STAMP(4, 6);
    dep_wait(ws, SY_GU + 8 * lay_ + (tt >> 2), 1, 32);
    STAMP(4, 7);
    d_load_b<NC, 0>(Bf, hs, l16); d_load_b<NC, 1>(Bf, hs, l16); d_load_b<NC, 2>(Bf, hs, l16); d_load_b<NC, 3>(Bf, hs, l16);
    STAMP(4, 1);
    d_steps<NC, 0>(Bf, Af, acc, hs, ws_, l16, rs);
    STAMP(4, 2);
    const int q = lane >> 4, t = lane & 15;
    float* rp = red + (size_t)w8 * 64 * D_STRIDE + t * D_STRIDE + 4 * q;
#pragma unroll
    for (int tb = 0; tb < 4; ++tb)
#pragma unroll
      for (int c = 0; c < NC; ++c) *(f32x4*)(rp + tb * 16 * D_STRIDE + c * 16) = acc[tb][c];
  } else {
    dep_wait(ws, SY_GU + 8 * lay_ + (tt >> 2), 1, 32);
    hload16<0>(rs.v[0], rs.x1, rs.off); hload16<512>(rs.v[1], rs.x1, rs.off);
  }
  asm volatile("s_waitcnt vmcnt(0) lgkmcnt(0)" : "+v"(rs.v[0]), "+v"(rs.v[1]) :: "memory");
  bar();
  STAMP(4, 3);
  f32x2* sred = (f32x2*)(smem + 4 * 64 * D_STRIDE * 4);
  if (has) {
    const float* rp = red + token * D_STRIDE + kl * 16;
    float v[16];
#pragma unroll
    for (int i = 0; i < 4; ++i) {
      f32x4 a = *(const f32x4*)(rp + 4 * i), b = *(const f32x4*)(rp + 64 * D_STRIDE + 4 * i);
      f32x4 c = *(const f32x4*)(rp + 2 * 64 * D_STRIDE + 4 * i), d = *(const f32x4*)(rp + 3 * 64 * D_STRIDE + 4 * i);
#pragma unroll
      for (int j = 0; j < 4; ++j) v[4 * i + j] = (a[j] + b[j]) + (c[j] + d[j]);
    }
    const u32x4 r0 = rs.v[0], r1 = rs.v[1];
    u32x4 o0, o1;
#pragma unroll
    for (int i = 0; i < 4; ++i) {
      o0[i] = pack2(bflo(r0[i]) + v[2 * i], bfhi(r0[i]) + v[2 * i + 1]);
      o1[i] = pack2(bflo(r1[i]) + v[8 + 2 * i], bfhi(r1[i]) + v[8 + 2 * i + 1]);
    }
    if (lay_) {
      char* dst = dout + ((size_t)gt * D + 16 * ks) * 2;
      *(u32x4*)dst = o0; *(u32x4*)(dst + 16) = o1;
    } else {
      hstore16(xout, rs.off, o0); hstore16(xout + 512, rs.off, o1);
      float s = 0.f;
#pragma unroll
      for (int i = 0; i < 4; ++i) s += bflo(o0[i]) + bfhi(o0[i]) + bflo(o1[i]) + bfhi(o1[i]);
      float m = s * (1.f / 16.f), qq = 0.f;
#pragma unroll
      for (int i = 0; i < 4; ++i) {
        float a = bflo(o0[i]) - m, b = bfhi(o0[i]) - m, c = bflo(o1[i]) - m, d = bfhi(o1[i]) - m;
        qq += a * a + b * b + c * c + d * d;
      }
      sred[kl * 64 + token] = f32x2{m, qq};
    }
  }
  STAMP(4, 4);
  if (!lay_) {
    asm volatile("s_waitcnt lgkmcnt(0)" ::: "memory");
    bar();
    if (tid < 64) {
      float mm[NC], ms = 0.f, qq = 0.f;
#pragma unroll
      for (int i = 0; i < NC; ++i) { f32x2 v = sred[i * 64 + tid]; mm[i] = v[0]; ms += v[0]; qq += v[1]; }
      float mean = ms * (1.f / NC);
#pragma unroll
      for (int i = 0; i < NC; ++i) { float d = mm[i] - mean; qq += 16.f * d * d; }
      hstore8(stout, ((tt * 64 + tid) * 8 + g) * 8, u32x2{__builtin_bit_cast(unsigned, mean), __builtin_bit_cast(unsigned, qq)});
    }
    STAMP(4, 5);
    sig_done(ws, SY_D + (tt >> 1));
  }
}
DI void d_phase(char* smem, char* ws, char* dout, const int lay_) {
  const int tid = tid_opaque(), lane = tid & 63;
  const int w8 = __builtin_amdgcn_readfirstlane(tid >> 6);
  const int b = blockIdx.x, xcd = b & 7, idx = b >> 3;
  const int tt = 4 * xcd + (idx & 3), g = idx >> 2;
  if (g < 4) d_body<5>(smem, ws, dout, lay_, tt, g, tid, lane, w8);
  else d_body<4>(smem, ws, dout, lay_, tt, g, tid, lane, w8);
}

__global__ __launch_bounds__(512, 2) void k_layers(char* ws, char* dout) {
  __shared__ __attribute__((aligned(16))) char smem[KA_RING * 4096];
#pragma nounroll
  for (int l = 0; l < 2; ++l) {
    ka_phase(smem, ws, l);
    kb_phase(smem, ws, l);
    gu_phase(smem, ws, l);
    d_phase(smem, ws, dout, l);
  }
}

extern "C" void kernel_launch(void* const* d_in, const int* in_sizes, int n_in, void* d_out, int out_size, void* d_ws, size_t ws_size,
                              hipStream_t stream) {
  const unsigned short* x = (const unsigned short*)d_in[0];
  const unsigned short* wqkv = (const unsigned short*)d_in[1];
  const unsigned short* wo = (const unsigned short*)d_in[2];
  const unsigned short* wg = (const unsigned short*)d_in[4];
  const unsigned short* wu = (const unsigned short*)d_in[5];
  const unsigned short* wd = (const unsigned short*)d_in[6];
  char* ws = (char*)d_ws;
  k_prep<<<PB_TOTAL, 512, 0, stream>>>(x, wqkv, wo, wg, wu, wd, ws);
  k_layers<<<256, 512, 0, stream>>>(ws, (char*)d_out);
}
```

```cpp
#include <hip/hip_runtime.h>
#include <hip/hip_bf16.h>
#include <stdint.h>

typedef __attribute__((ext_vector_type(8))) __bf16 bf16x8;
typedef __attribute__((ext_vector_type(2))) __bf16 bf16x2;
typedef __attribute__((ext_vector_type(2))) float f32x2;
typedef __attribute__((ext_vector_type(4))) float f32x4;
typedef __attribute__((ext_vector_type(16))) float f32x16;
typedef __attribute__((ext_vector_type(4))) unsigned u32x4;
typedef __attribute__((ext_vector_type(2))) unsigned u32x2;
#define DI __device__ __forceinline__
#define LDS_AS __attribute__((address_space(3)))

#define STAMP(kid, i) do { } while (0)
constexpr int S = 2048, D = 576, NH = 32, DH = 18, F = 4608;

constexpr size_t SZ_XF = (size_t)S * D * 2;
constexpr size_t OFF_XF0 = 0;
constexpr size_t OFF_XF2 = OFF_XF0 + SZ_XF;
constexpr size_t OFF_XF1 = OFF_XF2 + SZ_XF;
constexpr size_t OFF_ST0 = OFF_XF1 + 2 * SZ_XF;
constexpr size_t OFF_ST2 = OFF_ST0 + (size_t)S * 64;
constexpr size_t SZ_ST1 = (size_t)S * 32;
constexpr size_t OFF_ST1 = OFF_ST2 + (size_t)S * 64;
constexpr size_t SZ_PQF = (size_t)64 * 32 * 1024, SZ_PQL = (size_t)64 * 4 * 1024;
constexpr size_t OFF_PQF = OFF_ST1 + 2 * SZ_ST1;
constexpr size_t OFF_PQL = OFF_PQF + 2 * SZ_PQF;
constexpr size_t KV_DBL = (size_t)NH * 342 + 64;
constexpr size_t OFF_KV = OFF_PQL + 2 * SZ_PQL;
constexpr size_t SZ_KV = 2 * KV_DBL * 8;
constexpr size_t OFF_SYNC = OFF_KV + SZ_KV;
constexpr size_t SZ_ZERO = SZ_KV + 72 * 256;
constexpr int KA_NFRAG = 148, GU_NFRAG = 333;
constexpr size_t OFF_WQKV = OFF_KV + SZ_ZERO;
constexpr size_t OFF_WO = OFF_WQKV + (size_t)16 * KA_NFRAG * 1024;
constexpr size_t OFF_WGU = OFF_WO + (size_t)36 * 18 * 1024;
constexpr size_t OFF_WD = OFF_WGU + (size_t)32 * GU_NFRAG * 1024 + 16384;
constexpr size_t SZ_HF = (size_t)S * F * 2;
constexpr size_t OFF_HF = OFF_WD + (size_t)36 * 144 * 1024;
constexpr size_t WS_END = OFF_HF + 2 * SZ_HF;
static_assert(OFF_WQKV % 1024 == 0 && WS_END < (size_t)200 << 20, "ws map");

DI unsigned pack2(float lo, float hi) {
  f32x2 f = {lo, hi};
  bf16x2 v = __builtin_convertvector(f, bf16x2);
  return __builtin_bit_cast(unsigned, v);
}
DI float asf(unsigned u) { return __builtin_bit_cast(float, u); }
DI float bflo(unsigned u) { return __builtin_bit_cast(float, u << 16); }
DI float bfhi(unsigned u) { return __builtin_bit_cast(float, u & 0xffff0000u); }
DI float rbf(float x) { return bflo(pack2(x, 0.f) & 0xffffu); }
DI bf16x8 asbf(u32x4 v) { return __builtin_bit_cast(bf16x8, v); }
DI f32x16 mfma32(u32x4 a, u32x4 b, f32x16 c) { return __builtin_amdgcn_mfma_f32_32x32x16_bf16(asbf(a), asbf(b), c, 0, 0, 0); }
DI f32x4 mfma16(u32x4 a, u32x4 b, f32x4 c) { return __builtin_amdgcn_mfma_f32_16x16x32_bf16(asbf(a), asbf(b), c, 0, 0, 0); }
DI unsigned lds_addr(const void* p) { return (unsigned)(size_t)(LDS_AS const char*)p; }

DI void glds16(const void* sbase, unsigned voff, unsigned dst) {
  unsigned keep;
  asm volatile("s_mov_b32 %0, m0\n\ts_mov_b32 m0, %3\n\ts_nop 0\n\tglobal_load_lds_dwordx4 %1, %2\n\ts_mov_b32 m0, %0"
               : "=&s"(keep) : "v"(voff), "s"(sbase), "s"(dst) : "memory");
}
template <int IMM> DI void gload16(u32x4& dst, const void* sbase, unsigned voff) {
  asm volatile("global_load_dwordx4 %0, %1, %2 offset:%3" : "=v"(dst) : "v"(voff), "s"(sbase), "n"(IMM));
}
template <int IMM> DI void hload16(u32x4& dst, const void* sbase, unsigned voff) {
  asm volatile("global_load_dwordx4 %0, %1, %2 offset:%3 sc1" : "=v"(dst) : "v"(voff), "s"(sbase), "n"(IMM));
}
template <int IMM> DI void hload8(u32x2& dst, const void* sbase, unsigned voff) {
  asm volatile("global_load_dwordx2 %0, %1, %2 offset:%3 sc1" : "=v"(dst) : "v"(voff), "s"(sbase), "n"(IMM));
}
DI void hstore16(char* sbase, unsigned voff, u32x4 v) {
  asm volatile("s_nop 4\n\tglobal_store_dwordx4 %0, %1, %2 sc1\n\ts_nop 1" ::"v"(voff), "v"(v), "s"(sbase) : "memory");
}
DI void hstore8(char* sbase, unsigned voff, u32x2 v) {
  asm volatile("s_nop 4\n\tglobal_store_dwordx2 %0, %1, %2 sc1" ::"v"(voff), "v"(v), "s"(sbase) : "memory");
}
DI void hstore4(char* sbase, unsigned voff, unsigned v) {
  asm volatile("s_nop 4\n\tglobal_store_dword %0, %1, %2 sc1" ::"v"(voff), "v"(v), "s"(sbase) : "memory");
}
template <int G> DI void hload16_g(u32x4& dst, const void* sbase, unsigned voff, int grp) {
  asm volatile("s_cmp_lg_u32 %3, %4\n\ts_cbranch_scc1 .Lsk%=\n\tglobal_load_dwordx4 %0, %1, %2 sc1\n.Lsk%=:"
               : "+v"(dst) : "v"(voff), "s"(sbase), "s"(grp), "n"(G) : "scc");
}
template <int G> DI void glds16_g(const void* sbase, unsigned voff, unsigned dst, int grp) {
  unsigned keep;
  asm volatile("s_cmp_lg_u32 %4, %5\n\ts_cbranch_scc1 .Lsk%=\n\ts_mov_b32 %0, m0\n\ts_mov_b32 m0, %3\n\ts_nop 0\n\tglobal_load_lds_dwordx4 %1, %2\n\ts_mov_b32 m0, %0\n.Lsk%=:"
               : "=&s"(keep) : "v"(voff), "s"(sbase), "s"(dst), "s"(grp), "n"(G) : "memory", "scc");
}
template <int IMM> DI void gload8(u32x2& dst, const void* sbase, unsigned voff) {
  asm volatile("global_load_dwordx2 %0, %1, %2 offset:%3" : "=v"(dst) : "v"(voff), "s"(sbase), "n"(IMM));
}
DI float sum_x16(float x) {
  auto r = __builtin_amdgcn_permlane16_swap(__builtin_bit_cast(unsigned, x), __builtin_bit_cast(unsigned, x), false, false);
  return __builtin_bit_cast(float, (unsigned)r[0]) + __builtin_bit_cast(float, (unsigned)r[1]);
}
DI float sum_x32(float x) {
  auto r = __builtin_amdgcn_permlane32_swap(__builtin_bit_cast(unsigned, x), __builtin_bit_cast(unsigned, x), false, false);
  return __builtin_bit_cast(float, (unsigned)r[0]) + __builtin_bit_cast(float, (unsigned)r[1]);
}
template <int N> DI void waitv() { asm volatile("s_waitcnt vmcnt(%0)" ::"n"(N) : "memory"); }
template <int N> DI void waitv_pin(u32x4& x) { asm volatile("s_waitcnt vmcnt(%1)" : "+v"(x) : "n"(N)); }
DI void pin(u32x4& x) { asm volatile("" : "+v"(x)); }
DI u32x4 lds_rd(unsigned addr) { return *(const LDS_AS u32x4*)(size_t)addr; }
DI void bar() { asm volatile("" ::: "memory"); __builtin_amdgcn_s_barrier(); asm volatile("" ::: "memory"); }

__host__ __device__ constexpr int dg_start(int g) { return g < 4 ? 5 * g : 20 + 4 * (g - 4); }
__host__ __device__ constexpr int dg_count(int g) { return g < 4 ? 5 : 4; }

DI int tid_opaque() { int t = threadIdx.x; asm volatile("" : "+v"(t)); return t; }

typedef __attribute__((address_space(1))) unsigned gu32;
constexpr int SY_KA = 0, SY_KB = 16, SY_GU = 32, SY_D = 48, SY_TMO = 64;
DI gu32* sync_word(char* ws, int i) { return (gu32*)(ws + OFF_SYNC + (size_t)i * 256); }
DI void sig_done(char* ws, int i) {
  asm volatile("s_waitcnt vmcnt(0) lgkmcnt(0)" ::: "memory");
  bar();
  if (threadIdx.x == 0) __hip_atomic_fetch_add(sync_word(ws, i), 1u, __ATOMIC_RELAXED, __HIP_MEMORY_SCOPE_AGENT);
}
DI void dep_wait(char* ws, int i, int n, unsigned target) {
  if (threadIdx.x < 64) {
    const int lane = threadIdx.x;
    gu32* wd = sync_word(ws, i + (lane < n ? lane : 0));
    for (unsigned spins = 0;; ++spins) {
      const unsigned v = __hip_atomic_load(wd, __ATOMIC_RELAXED, __HIP_MEMORY_SCOPE_AGENT);
      if (__all(v >= target)) break;
      __builtin_amdgcn_s_sleep(1);
      if (spins > (1u << 18)) { if (lane == 0) __hip_atomic_store(sync_word(ws, SY_TMO), 1u, __ATOMIC_RELAXED, __HIP_MEMORY_SCOPE_AGENT); break; }
      if ((spins & 255u) == 255u && __builtin_amdgcn_readfirstlane(__hip_atomic_load(sync_word(ws, SY_TMO), __ATOMIC_RELAXED, __HIP_MEMORY_SCOPE_AGENT))) break;
    }
  }
  bar();
}

#define PLD4(p) __builtin_nontemporal_load(p)
#define PLD2(p) __builtin_nontemporal_load(p)
DI void st16_wt(void* p, u32x4 v) { asm volatile("global_store_dwordx4 %0, %1, off sc1\n\ts_nop 1" ::"v"(p), "v"(v) : "memory"); }
DI void st16_nt(void* p, u32x4 v) { __builtin_nontemporal_store(v, (u32x4*)p); }
#define RST4(p, v) PST4(p, v)
#define PST4(p, v) (*(u32x4*)(p) = (v))
typedef __attribute__((ext_vector_type(4))) short s16x4;
DI u32x2 lds_tr(unsigned addr) {
  return __builtin_bit_cast(u32x2, __builtin_amdgcn_ds_read_tr16_b64_v4i16((LDS_AS s16x4*)(size_t)addr));
}
DI float dot_ones(unsigned u, float acc) {
  return __builtin_amdgcn_fdot2_f32_bf16(__builtin_bit_cast(bf16x2, u), __builtin_bit_cast(bf16x2, 0x3f803f80u), acc, false);
}
DI void put_colsum(char* chunk0, int kq, float cs) {
  unsigned p = pack2(-cs, 0.f) & 0xffffu;
  float hi = bflo(p);
  unsigned lo = pack2(-cs - hi, 0.f) & 0xffffu;
#pragma unroll
  for (int t = 0; t < 3; ++t) {
    int sl = 3 * kq + t;
    *(unsigned short*)(chunk0 + (sl >> 3) * 512 + (sl & 7) * 2) = (unsigned short)(t == 1 ? lo : p);
  }
  if (kq == 3) *(u32x2*)(chunk0 + 512 + 8) = u32x2{0u, 0u};
}
__host__ __device__ constexpr int gu_frag_index(int blk, int ks) { return blk < 3 ? 3 * ks + blk : 111 + 37 * (blk - 3) + ks; }

constexpr int P_RS = 144;
struct RtRegs { u32x4 v[5]; };
DI void rt_load(int u, int tid, const unsigned short* wg, const unsigned short* wu, const unsigned short* wd, RtRegs& R) {
  if (u < 576) {
    const int m = u & 1, kq = (u >> 1) & 3, fq = u >> 3;
    const unsigned short* wsrc = m ? wu : wg;
#pragma unroll
    for (int i = 0; i < 5; ++i) {
      int ch = tid + 256 * i; if (ch > 1151) ch = 1151;
      R.v[i] = PLD4((const u32x4*)(wsrc + (size_t)(144 * kq + (ch >> 3)) * F + 64 * fq + 8 * (ch & 7)));
    }
  } else {
    const int ub = u - 576, cgp = ub % 9, sg = ub / 9;
#pragma unroll
    for (int i = 0; i < 4; ++i) {
      int ch = tid + 256 * i;
      R.v[i] = PLD4((const u32x4*)(wd + (size_t)(128 * sg + (ch >> 3)) * D + 64 * cgp + 8 * (ch & 7)));
    }
    R.v[4] = R.v[3];
  }
}
DI void rt_stage(int u, int tid, char* sm, const RtRegs& R) {
  const int nch = u < 576 ? 1152 : 1024;
#pragma unroll
  for (int i = 0; i < 5; ++i) {
    int ch = tid + 256 * i;
    if (ch < nch) *(u32x4*)(sm + (ch >> 3) * P_RS + (ch & 7) * 16) = R.v[i];
  }
}
DI void rt_emit(int u, int tid, char* sm, char* ws) {
  const int lane = tid & 63, w = tid >> 6;
  const unsigned sb = lds_addr(sm);
  if (u < 576) {
    const int m = u & 1, kq = (u >> 1) & 3, fq = u >> 3;
    const int fb = 4 * fq + w, fs = fb / 9, blk = fb - 9 * fs;
    const int g = lane >> 4, h = g & 1, kpar = g >> 1, qq = (lane >> 2) & 3, pp = lane & 3, i16 = lane & 15;
    const int pq = (pp >> 1) | ((pp & 1) << 1);
    const unsigned rd = sb + (16 * kpar + 8 * h + qq) * P_RS + w * 32 + pq * 8;
    char* dst = ws + OFF_WGU + (size_t)fs * GU_NFRAG * 1024 + (h * 32 + m * 16 + i16) * 16;
    float cs = 0.f;
#pragma unroll
    for (int k2 = 0; k2 < 5; ++k2) {
      const int kl = 2 * k2 + kpar, back = kl < 9 ? 0 : 16;
      u32x2 lo = lds_tr(rd + (2 * k2 * 16 - back) * P_RS), hi = lds_tr(rd + (2 * k2 * 16 + 4 - back) * P_RS);
      u32x4 o = {lo[0], lo[1], hi[0], hi[1]};
      if (kl < 9) {
        cs = dot_ones(o[0], cs); cs = dot_ones(o[1], cs); cs = dot_ones(o[2], cs); cs = dot_ones(o[3], cs);
        RST4(dst + (size_t)gu_frag_index(blk, 9 * kq + kl) * 1024, o);
      }
    }
    cs += __shfl_xor(cs, 16); cs += __shfl_xor(cs, 32);
    if (lane < 16) put_colsum(ws + OFF_WGU + ((size_t)fs * GU_NFRAG + gu_frag_index(blk, 36)) * 1024 + (m * 16 + lane) * 16, kq, cs);
  } else {
    const int ub = u - 576, cgp = ub % 9, sg = ub / 9;
    const int c = 4 * cgp + w;
    const int g = c < 20 ? c / 5 : 4 + (c - 20) / 4;
    const int st = dg_start(g), n = dg_count(g);
    const int q = lane >> 4, qq = (lane >> 2) & 3, pp = lane & 3;
    const unsigned rd = sb + (8 * q + qq) * P_RS + w * 32 + pp * 8;
#pragma unroll
    for (int sl = 0; sl < 4; ++sl) {
      u32x2 lo = lds_tr(rd + sl * 32 * P_RS), hi = lds_tr(rd + (sl * 32 + 4) * P_RS);
      size_t frag = (size_t)st * 144 + (size_t)(4 * sg + sl) * n + (c - st);
      RST4(ws + OFF_WD + frag * 1024 + lane * 16, (u32x4{lo[0], lo[1], hi[0], hi[1]}));
    }
  }
}

constexpr int PB_WQKV = 64, PB_RT = 450, PB_X = 128, PB_WO = 18, PB_Z = 6;
constexpr int PB_TOTAL = PB_WQKV + PB_X + PB_WO + PB_Z + PB_RT;

__global__ __launch_bounds__(512) void k_prep(const unsigned short* __restrict__ x, const unsigned short* __restrict__ wqkv,
                                              const unsigned short* __restrict__ wo, const unsigned short* __restrict__ wg,
                                              const unsigned short* __restrict__ wu, const unsigned short* __restrict__ wd,
                                              char* __restrict__ ws) {
  __shared__ __attribute__((aligned(16))) char smem[2 * 576 * 40];
  int b = blockIdx.x;
  const int tid = threadIdx.x, lane = tid & 63, w8 = tid >> 6;
  {
    unsigned plo, phi_;
    asm volatile("s_getpc_b64 s[40:41]\n\ts_add_u32 s40, s40, _Z8k_layersPcS_@rel32@lo+4\n\ts_addc_u32 s41, s41, _Z8k_layersPcS_@rel32@hi+12\n\t"
                 "s_mov_b32 %0, s40\n\ts_mov_b32 %1, s41" : "=s"(plo), "=s"(phi_) :: "s40", "s41", "scc");
    const unsigned long long pc = ((unsigned long long)phi_ << 32) | plo;
    const int slot = b >> 3;
    if ((slot * 512 + tid) * 16 < 53248) { unsigned v = *(const volatile unsigned*)((const char*)pc + (slot * 512 + tid) * 16); asm volatile("" :: "v"(v)); }
  }
  if (b < PB_WQKV) {
    constexpr int RS = 224;
    const int p = b >> 2, kq = b & 3;
    u32x2 v[8];
#pragma unroll
    for (int i = 0; i < 8; ++i) {
      int ch = tid + 512 * i; if (ch > 3887) ch = 3887;
      int row = ch / 27, rem = ch - 27 * row, m = rem / 9, c4 = rem - 9 * m;
      v[i] = PLD2((const u32x2*)(wqkv + (size_t)(144 * kq + row) * (3 * D) + 576 * m + 36 * p + 4 * c4));
    }
    const int blk = w8 & 3, kh = w8 >> 2, h = lane >> 5, rho = lane & 31;
    int col = 108;
    if (blk == 0) col = 36 + rho;
    else if (blk == 1) {
      if (rho < 18) col = 72 + rho;
      else if (rho >= 19 && rho <= 22) col = 36 + 32 + (rho - 19);
      else if (rho >= 23 && rho <= 26) col = 18 * ((rho - 23) >> 1) + 16 + ((rho - 23) & 1);
    } else if (blk == 2) { if (rho < 18) col = 72 + 18 + rho; }
    else col = 18 * (rho >> 4) + 8 * ((rho >> 2) & 1) + 4 * ((rho >> 3) & 1) + (rho & 3);
#pragma unroll
    for (int i = 0; i < 8; ++i) {
      int ch = tid + 512 * i;
      if (ch < 3888) { int row = ch / 27, rem = ch - 27 * row; *(u32x2*)(smem + row * RS + rem * 8) = v[i]; }
    }
    if (tid < 144) *(u32x2*)(smem + tid * RS + 216) = u32x2{0u, 0u};
    __syncthreads();
    const unsigned short* src = (const unsigned short*)(smem + (80 * kh + 8 * h) * RS) + col;
    char* dst = ws + OFF_WQKV + (size_t)p * KA_NFRAG * 1024 + blk * 1024 + lane * 16;
    float cs = 0.f;
#pragma unroll
    for (int kl = 0; kl < 5; ++kl) {
      if (5 * kh + kl < 9) {
        u32x4 o;
#pragma unroll
        for (int i = 0; i < 4; ++i)
          o[i] = (unsigned)src[(kl * 16 + 2 * i) * (RS / 2)] | ((unsigned)src[(kl * 16 + 2 * i + 1) * (RS / 2)] << 16);
        cs = dot_ones(o[0], cs); cs = dot_ones(o[1], cs); cs = dot_ones(o[2], cs); cs = dot_ones(o[3], cs);
        PST4(dst + (size_t)(9 * kq + 5 * kh + kl) * 4096, o);
      }
    }
    cs += __shfl_xor(cs, 32);
    float* cx = (float*)(smem + 144 * RS);
    if (kh == 1 && lane < 32) cx[blk * 32 + lane] = cs;
    __syncthreads();
    if (kh == 0 && lane < 32) put_colsum(ws + OFF_WQKV + ((size_t)p * KA_NFRAG + 144 + blk) * 1024 + lane * 16, kq, cs + cx[blk * 32 + lane]);
    return;
  }
  b -= PB_WQKV;
  if (b < PB_X) {
    const int ks = lane < 36 ? lane : 35;
    u32x4 v0[2], v1[2];
#pragma unroll
    for (int i = 0; i < 2; ++i) {
      const u32x4* src = (const u32x4*)(x + (size_t)(b * 16 + w8 * 2 + i) * D + 16 * ks);
      v0[i] = PLD4(src); v1[i] = PLD4(src + 1);
    }
#pragma unroll
    for (int i = 0; i < 2; ++i) {
      const int token = b * 16 + w8 * 2 + i, tb = token >> 5, r = token & 31;
      if (lane < 36) {
        char* dst = ws + OFF_XF0 + ((size_t)(tb * 36 + ks) * 64 + r) * 16;
        PST4(dst, v0[i]); PST4(dst + 512, v1[i]);
      }
      float s = 0.f;
#pragma unroll
      for (int j = 0; j < 4; ++j) s += bflo(v0[i][j]) + bfhi(v0[i][j]) + bflo(v1[i][j]) + bfhi(v1[i][j]);
      float m16 = s * (1.f / 16.f), q = 0.f;
#pragma unroll
      for (int j = 0; j < 4; ++j) {
        float a = bflo(v0[i][j]) - m16, c = bfhi(v0[i][j]) - m16, d = bflo(v1[i][j]) - m16, e = bfhi(v1[i][j]) - m16;
        q += a * a + c * c + d * d + e * e;
      }
      int g = lane & 7, st = dg_start(g), n = dg_count(g);
      float ms = 0.f, mm[5], qq = 0.f;
#pragma unroll
      for (int j = 0; j < 5; ++j) {
        int sl = st + (j < n ? j : 0);
        mm[j] = __shfl(m16, sl); float qi = __shfl(q, sl);
        if (j < n) { ms += mm[j]; qq += qi; }
      }
      float mean = ms / n;
#pragma unroll
      for (int j = 0; j < 5; ++j) if (j < n) { float d = mm[j] - mean; qq += 16.f * d * d; }
      if (lane < 8) *(f32x2*)(ws + OFF_ST0 + ((size_t)token * 8 + lane) * 8) = f32x2{mean, qq};
    }
    return;
  }
  b -= PB_X;
  if (b < PB_WO) {
    constexpr int RS = 40;
    const int half = tid >> 8, t = tid & 255, w = w8 & 3, c = 2 * b + half;
    char* sm = smem + half * (576 * RS);
    u32x4 v[5];
#pragma unroll
    for (int i = 0; i < 5; ++i) {
      int ch = t + 256 * i; if (ch > 1151) ch = 1151;
      v[i] = PLD4((const u32x4*)(wo + (size_t)(ch >> 1) * D + 16 * c + 8 * (ch & 1)));
    }
#pragma unroll
    for (int i = 0; i < 5; ++i) {
      int ch = t + 256 * i;
      if (ch < 1152) { char* d = sm + (ch >> 1) * RS + (ch & 1) * 16; *(u32x2*)d = u32x2{v[i][0], v[i][1]}; *(u32x2*)(d + 8) = u32x2{v[i][2], v[i][3]}; }
    }
    __syncthreads();
    const unsigned sb = lds_addr(sm);
    const int q = lane >> 4, qq = (lane >> 2) & 3, pp = lane & 3;
    for (int s = w; s < 18; s += 4) {
      int r0, r1;
      if (s < 16) { r0 = (2 * s + (q >> 1)) * 18 + 8 * (q & 1) + qq; r1 = r0 + 4; }
      else { int w0 = 4 * (s - 16) + (q >> 1); r0 = (4 * w0 + qq) * 18 + 16 + (q & 1); r1 = (4 * (w0 + 2) + qq) * 18 + 16 + (q & 1); }
      u32x2 lo = lds_tr(sb + r0 * RS + pp * 8), hi = lds_tr(sb + r1 * RS + pp * 8);
      PST4(ws + OFF_WO + ((size_t)c * 18 + s) * 1024 + lane * 16, (u32x4{lo[0], lo[1], hi[0], hi[1]}));
    }
    return;
  }
  b -= PB_WO;
  if (b < PB_Z) {
#pragma unroll
    for (int i = 0; i < 4; ++i) {
      size_t o = (((size_t)b * 4 + i) * 512 + tid) * 16;
      if (o < SZ_ZERO) PST4(ws + OFF_KV + o, (u32x4{0, 0, 0, 0}));
    }
    return;
  }
  b -= PB_Z;
  {
    const int half = tid >> 8, t = tid & 255, u = 2 * b + half;
    char* sm = smem + half * (144 * P_RS);
    RtRegs R;
    rt_load(u, t, wg, wu, wd, R);
    rt_stage(u, t, sm, R);
    __syncthreads();
    rt_emit(u, t, sm, ws);
  }
}

DI u32x4 mean_frag(float mean, int h) {
  unsigned mh = pack2(mean, 0.f) & 0xffffu;
  unsigned ml = pack2(mean - bflo(mh), 0.f) & 0xffffu;
  unsigned hh = mh | (mh << 16), lh = ml | (mh << 16), hl = mh | (ml << 16);
  return h == 0 ? u32x4{hh, lh, hl, hh} : u32x4{lh, hl, 0u, 0u};
}

DI float phi2(float a, float rs2) {
  const float t = a * rs2;
  return __builtin_fmaf(fmaxf(t, 0.f), 0.6931471805599453f, __builtin_amdgcn_exp2f(fminf(t, 0.f)) + 1e-6f);
}
#define KA_PRO_ 12
constexpr int KA_PRO = KA_PRO_;
constexpr int KA_RING = 37;
constexpr int KA_PD = 8;
template <int KS>
DI void ka_steps(u32x4 (&X)[37], u32x4 (&A)[KA_PD], f32x16 (&acc)[4], const char* xs, unsigned l16, unsigned rd0, unsigned rd1, float mean, int h) {
  if constexpr (KS == 36) X[36] = mean_frag(mean, h);
  if constexpr (KS % 4 == 0 && KS > 0 && KS <= 32) bar();
  if constexpr (KS < 36) {
    constexpr int issued = KS + KA_PRO < 36 ? KS + KA_PRO : 36;
    waitv_pin<issued - KS - 1>(X[KS]);
    if constexpr (KS + KA_PRO < 36) hload16<0>(X[KS + KA_PRO], xs + (KS + KA_PRO) * 1024, l16);
  }
#pragma unroll
  for (int blk = 0; blk < 4; ++blk) {
    const int i = 4 * KS + blk;
    u32x4 a = A[i % KA_PD];
    if (blk < 3) acc[blk] = mfma32(X[KS], a, acc[blk]);
    else acc[3] = mfma32(a, X[KS], acc[3]);
    if (i + KA_PD < KA_NFRAG) { const int f = (i + KA_PD) % (4 * KA_RING); A[i % KA_PD] = lds_rd((f < 64 ? rd0 : (f < 128 ? rd1 : rd1 + 65536)) + (f & 63) * 1024); }
    __builtin_amdgcn_sched_barrier(0);
  }
  if constexpr (KS + 1 < 37) ka_steps<KS + 1>(X, A, acc, xs, l16, rd0, rd1, mean, h);
}
#define KA_PRE_ 12
constexpr int KA_PRE = KA_PRE_;
constexpr int ka_to(int G) { int t = G == 0 ? KA_PRE : 4 * G + 16; if (G > 0 && t > 4 * (G - 1) + KA_RING) t = 4 * (G - 1) + KA_RING; return t < 37 ? t : 37; }
struct KaFoTable { short v[256][4]; };
constexpr KaFoTable ka_make_fo() {
  KaFoTable T{};
  for (int t = 0; t < 256; ++t)
    for (int i = 0; i < 4; ++i) {
      const int it = t + 256 * i;
      int val = -1;
      if (it < 684) {
        const int hs = it >= 342, sl = it - 342 * hs, e = sl / 18, d = sl - 18 * e;
        val = hs == 0 ? d * 33 + e : (d < 14 ? (18 + d) * 33 + e : (32 + d - 14) * 33 + e);
      }
      T.v[t][i] = (short)val;
    }
  return T;
}
__device__ __attribute__((aligned(16))) const KaFoTable KA_FO = ka_make_fo();
template <int G, int GEND> DI void ka_loader(const char* wsrc, unsigned l16, unsigned dst) {
  constexpr int need = 4 * G + 7 < 36 ? 4 * G + 7 : 36;
  constexpr int from = G == 0 ? KA_PRE : ka_to(G - 1);
  constexpr int to = ka_to(G);
  static_assert(to > need && to >= from, "loader schedule");
#pragma unroll
  for (int ks = from; ks < to; ++ks) glds16(wsrc + ks * 4096, l16, dst + (4 * (ks % KA_RING)) * 1024);
  waitv<to - 1 - need>();
  bar();
  if constexpr (G < GEND) ka_loader<G + 1, GEND>(wsrc, l16, dst);
}

DI void ka_phase(char* smem, char* ws, const int lay_) {
  const char* xf = ws + (lay_ ? OFF_XF2 : OFF_XF0);
  const char* st = ws + (lay_ ? OFF_ST2 : OFF_ST0);
  const char* wqkvf = ws + OFF_WQKV;
  char* pqf = ws + OFF_PQF + lay_ * SZ_PQF;
  char* pql = ws + OFF_PQL + lay_ * SZ_PQL;
  double* kv = (double*)(ws + OFF_KV) + (size_t)lay_ * KV_DBL;
  const int tid = tid_opaque(), lane = tid & 63;
  const int w8 = __builtin_amdgcn_readfirstlane(tid >> 6);
  const int w = w8 & 3;
  const int b = blockIdx.x, xcd = b & 7, idx = b >> 3;
  const int tt = 2 * xcd + (idx & 1), p = idx >> 1;
  const int tb = 4 * tt + w;
  const unsigned sb = lds_addr(smem);
  const unsigned l16 = lane * 16;
  const int h = lane >> 5, r = lane & 31;
  STAMP(1, 0);
  if (w8 >= 4) {
    const char* wsrc = wqkvf + (size_t)p * KA_NFRAG * 1024 + w * 1024;
    u32x2 fot;
    gload8<0>(fot, &KA_FO, (tid - 256) * 8);
#pragma unroll
    for (int ks = 0; ks < KA_PRE; ++ks) glds16(wsrc + ks * 4096, l16, sb + w * 1024 + (4 * ks) * 1024);
    if (lay_) dep_wait(ws, SY_D + tt, 1, 16);
    ka_loader<0, 8>(wsrc, l16, sb + w * 1024);
    asm volatile("" : "+v"(fot));
    int fo[3] = {(short)(fot[0] & 0xffff), (short)(fot[0] >> 16), (short)(fot[1] & 0xffff)};
    bar();
    bar();
    float sum[3];
#pragma unroll
    for (int i = 0; i < 3; ++i) {
      const float* rp = (const float*)smem + (fo[i] >= 0 ? fo[i] : 0);
      sum[i] = (rp[0] + rp[1280]) + (rp[2560] + rp[3840]);
    }
#pragma unroll
    for (int i = 0; i < 3; ++i)
      if (fo[i] >= 0) unsafeAtomicAdd(kv + (size_t)(2 * p) * 342 + (tid - 256) + 256 * i, (double)sum[i]);
  } else {
    u32x4 sv[4], X[37];
    const char* xs = xf + (size_t)tb * 36 * 1024;
    if (lay_) dep_wait(ws, SY_D + tt, 1, 16);
    hload16<0>(X[0], xs, l16); hload16<0>(X[1], xs + 1024, l16);
    {
      const char* sp = st + (size_t)(tb * 32) * 64;
      unsigned so = r * 64;
      hload16<0>(sv[0], sp, so); hload16<16>(sv[1], sp, so); hload16<32>(sv[2], sp, so); hload16<48>(sv[3], sp, so);
    }
#pragma unroll
    for (int k = 2; k < KA_PRO; ++k) hload16<0>(X[k], xs + k * 1024, l16);
    STAMP(1, 1);
    float rstd, mean;
    {
      asm volatile("s_waitcnt vmcnt(%4)" : "+v"(sv[0]), "+v"(sv[1]), "+v"(sv[2]), "+v"(sv[3]) : "n"(KA_PRO - 2));
      float m[8], q[8];
#pragma unroll
      for (int i = 0; i < 4; ++i) {
        m[2 * i] = asf(sv[i][0]); q[2 * i] = asf(sv[i][1]);
        m[2 * i + 1] = asf(sv[i][2]); q[2 * i + 1] = asf(sv[i][3]);
      }
      mean = (80.f * (m[0] + m[1] + m[2] + m[3]) + 64.f * (m[4] + m[5] + m[6] + m[7])) * (1.f / 576.f);
      float M2 = 0.f;
#pragma unroll
      for (int i = 0; i < 8; ++i) { float d = m[i] - mean; M2 += q[i] + (i < 4 ? 80.f : 64.f) * d * d; }
      rstd = rsqrtf(M2 * (1.f / 576.f) + 1e-5f);
    }
    f32x16 acc[4];
#pragma unroll
    for (int i = 0; i < 4; ++i)
#pragma unroll
      for (int j = 0; j < 16; ++j) acc[i][j] = 0.f;
    unsigned rd0 = sb + l16, rd1 = rd0 + 65536;
    asm volatile("" : "+v"(rd0), "+v"(rd1));
    bar();
    STAMP(1, 2);
    u32x4 A[KA_PD];
#pragma unroll
    for (int i = 0; i < KA_PD; ++i) A[i] = lds_rd(rd0 + i * 1024);
    __builtin_amdgcn_sched_barrier(0);
    ka_steps<0>(X, A, acc, xs, l16, rd0, rd1, mean, h);
    STAMP(1, 3);
    bar();
    STAMP(1, 6);

    const int c = r;
    float* rs = (float*)(smem + 32768) + w * 160;
    float* ql = rs + 32;
    if (h == 0) rs[r] = rstd;
    f32x4 rr[4];
#pragma unroll
    for (int g = 0; g < 4; ++g) rr[g] = *(const f32x4*)(rs + 8 * g + 4 * h);
    u32x4 k1[2], v1[2], v2[2];
    f32x4 pv[4];
    const bool ph = c >= 19 && c <= 26, one = c == 18;
#pragma unroll
    for (int s = 0; s < 2; ++s)
#pragma unroll
      for (int t = 0; t < 4; ++t) {
        const int r0 = 8 * s + 2 * t;
        const float s0 = rr[r0 >> 2][r0 & 3], s1 = rr[r0 >> 2][(r0 & 3) + 1];
        const float e0 = s0 * 1.4426950408889634f, e1 = s1 * 1.4426950408889634f;
        k1[s][t] = pack2(phi2(acc[0][r0], e0), phi2(acc[0][r0 + 1], e1));
        float a0 = acc[1][r0] * s0, a1 = acc[1][r0 + 1] * s1;
        float p0 = phi2(acc[1][r0], e0), p1 = phi2(acc[1][r0 + 1], e1);
        a0 = ph ? p0 : (one ? 1.f : a0); a1 = ph ? p1 : (one ? 1.f : a1);
        pv[r0 >> 2][r0 & 3] = p0; pv[r0 >> 2][(r0 & 3) + 1] = p1;
        v1[s][t] = pack2(a0, a1);
        v2[s][t] = one ? 0x3f803f80u : pack2(acc[2][r0] * s0, acc[2][r0 + 1] * s1);
      }
    if (c >= 23 && c <= 26) {
#pragma unroll
      for (int g = 0; g < 4; ++g) *(f32x4*)(ql + (c - 23) * 32 + 8 * g + 4 * h) = pv[g];
    }
    STAMP(1, 7);
    f32x16 R1, R2, R3;
#pragma unroll
    for (int j = 0; j < 16; ++j) { R1[j] = 0.f; R2[j] = 0.f; R3[j] = 0.f; }
#pragma unroll
    for (int s = 0; s < 2; ++s) {
      R1 = mfma32(k1[s], v1[s], R1);
      R2 = mfma32(k1[s], v2[s], R2);
      R3 = mfma32(v1[s], v2[s], R3);
    }
    float* red = (float*)smem + w * 1280;
#pragma unroll
    for (int j = 0; j < 16; ++j) {
      int row = (j & 3) + 8 * (j >> 2) + 4 * h;
      red[row * 33 + c] = row < 18 ? R1[j] : R2[j];
    }
    if (h == 0) red[32 * 33 + c] = R3[11];
    else { red[33 * 33 + c] = R3[8]; red[34 * 33 + c] = R3[9]; red[35 * 33 + c] = R3[10]; }
    STAMP(1, 4);
    asm volatile("s_waitcnt lgkmcnt(0)" ::: "memory");
    bar();
    {
      u32x4 qa, qb;
      const float rse = rstd * 1.4426950408889634f;
#pragma unroll
      for (int t = 0; t < 4; ++t) {
        qa[t] = pack2(phi2(acc[3][2 * t], rse), phi2(acc[3][2 * t + 1], rse));
        qb[t] = pack2(phi2(acc[3][8 + 2 * t], rse), phi2(acc[3][8 + 2 * t + 1], rse));
      }
      char* dst = pqf + ((size_t)tb * 32 + 2 * p) * 1024;
      hstore16(dst, l16, qa); hstore16(dst + 1024, l16, qb);
      float la = ql[h * 32 + r], lb = ql[(2 + h) * 32 + r];
      hstore4(pql + ((size_t)tb * 4 + (p >> 2)) * 1024 + 4 * (p & 3), l16, pack2(la, lb));
    }
  }
  STAMP(1, 8);
  sig_done(ws, SY_KA + 8 * lay_ + xcd);
  STAMP(1, 9);
}

constexpr int KB_LDS = 36 * 1024 + 16 * 1024 + 9 * 32 * 8;
constexpr int KB_KVI = 57344;
struct KbTable { unsigned short v[64][16]; };
constexpr KbTable kb_make_table() {
  KbTable T{};
  for (int l = 0; l < 64; ++l)
    for (int k = 0; k < 16; ++k) {
      const int q = l + 64 * k;
      int val = 4000;
      if (q < 684) { const int hd = q / 171, r = q - 171 * hd, e = r / 9, d2 = r - 9 * e; val = hd * 912 + e * 48 + d2 * 4; }
      T.v[l][k] = (unsigned short)val;
    }
  return T;
}
__device__ __attribute__((aligned(16))) const KbTable KB_TAB = kb_make_table();
DI void kb_phase(char* smem, char* ws, const int lay_) {
  const char* pqf = ws + OFF_PQF + lay_ * SZ_PQF;
  const char* pql = ws + OFF_PQL + lay_ * SZ_PQL;
  const double* kv = (const double*)(ws + OFF_KV) + (size_t)lay_ * KV_DBL;
  const char* wof = ws + OFF_WO;
  const char* xin = ws + (lay_ ? OFF_XF2 : OFF_XF0);
  char* xout = ws + OFF_XF1 + lay_ * SZ_XF;
  char* stout = ws + OFF_ST1 + lay_ * SZ_ST1;
  const int tid = tid_opaque(), lane = tid & 63;
  const int w = __builtin_amdgcn_readfirstlane(tid >> 6);
  const int b = blockIdx.x, xcd = b & 7, idx = b >> 3;
  const int tb = 8 * xcd + (idx & 7), cg = idx >> 3;
  const int ct = 9 * cg + w, ct8 = 9 * cg + 8;
  const int h = lane >> 5, r = lane & 31, q = lane >> 4, t = lane & 15;

  STAMP(2, 0);
  u32x4 A[18], A8[3];
  const char* wp = wof + (size_t)ct * 18 * 1024;
  const char* wp8 = wof + ((size_t)ct8 * 18 + w) * 1024;
  const unsigned l16 = lane * 16;
  u32x2 res[2], res8;
  const unsigned xq = ((q >> 1) * 32 + t) * 16 + (q & 1) * 8;
  const unsigned xo = (tb * 36 + ct) * 1024 + xq;
  const unsigned xo8 = (tb * 36 + ct8) * 1024 + xq + (w & 1) * 256;
#define KB_LDA(s_) gload16<((s_) & 3) * 1024>(A[s_], wp + ((s_) >> 2) * 4096, l16)
  u32x4 tab[2];
  gload16<0>(tab[0], &KB_TAB, lane * 32); gload16<16>(tab[1], &KB_TAB, lane * 32);
  gload16<0>(A8[0], wp8, l16); gload16<0>(A8[1], wp8 + 8192, l16); gload16<0>(A8[2], wp8 + (w < 2 ? 16384 : 0), l16);
  KB_LDA(0); KB_LDA(1); KB_LDA(2); KB_LDA(3); KB_LDA(4); KB_LDA(5); KB_LDA(6); KB_LDA(7); KB_LDA(8);
  KB_LDA(9); KB_LDA(10); KB_LDA(11); KB_LDA(12); KB_LDA(13); KB_LDA(14); KB_LDA(15); KB_LDA(16); KB_LDA(17);
  STAMP(2, 6);
  dep_wait(ws, SY_KA + 8 * lay_, 8, 32);
  STAMP(2, 7);

  u32x4 kd[11];
  u32x4 bq[4], bl;
  {
    const char* src = (const char*)(kv + (size_t)(4 * w) * 342);
#pragma unroll
    for (int k = 0; k < 10; ++k) hload16<0>(kd[k], src + k * 1024, l16);
    hload16<0>(kd[10], src + 10240, l16);
  }
  hload16<0>(bl, pql + ((size_t)tb * 4 + (w >> 1)) * 1024, l16);
#pragma unroll
  for (int i = 0; i < 4; ++i) hload16<0>(bq[i], pqf + ((size_t)tb * 32 + 4 * w + i) * 1024, l16);
  hload8<0>(res[0], xin, xo); hload8<256>(res[1], xin, xo); hload8<0>(res8, xin, xo8);
  STAMP(2, 1);
  char* kvi = smem + KB_KVI + w * 4096;
  asm volatile("s_waitcnt vmcnt(8)" : "+v"(kd[0]), "+v"(kd[1]), "+v"(kd[2]), "+v"(kd[3]), "+v"(kd[4]), "+v"(kd[5]), "+v"(kd[6]), "+v"(kd[7]),
               "+v"(kd[8]), "+v"(kd[9]), "+v"(kd[10]), "+v"(tab[0]), "+v"(tab[1]));
#pragma unroll
  for (int k = 0; k < 11; ++k) {
    const double d0 = __builtin_bit_cast(double, u32x2{kd[k][0], kd[k][1]}), d1 = __builtin_bit_cast(double, u32x2{kd[k][2], kd[k][3]});
    const unsigned tw = tab[k >> 3][(k >> 1) & 3];
    const unsigned off = (k & 1) ? (tw >> 16) : (tw & 0xffffu);
    *(unsigned*)(kvi + off) = pack2((float)d0, (float)d1);
  }
  const bool valid = r < 18 || r == 20 || r == 21;
  const int er = r < 16 ? 8 * ((r >> 2) & 1) + 4 * (r >> 3) + (r & 3) : (r == 16 ? 16 : (r == 20 ? 17 : (valid ? 18 : 0)));
  asm volatile("s_waitcnt vmcnt(3)" : "+v"(bl), "+v"(bq[0]), "+v"(bq[1]), "+v"(bq[2]), "+v"(bq[3]));
  float lo[4];
#pragma unroll
  for (int i = 0; i < 4; ++i) {
    const char* row = kvi + i * 912 + er * 48;
    u32x4 a1 = *(const u32x4*)(row + 16 * h), a2;
    const unsigned l16v = *(const unsigned short*)(row + 32 + 2 * h);
    if (!valid) a1 = u32x4{0u, 0u, 0u, 0u};
    const unsigned lv = valid ? ((i & 1) ? (l16v << 16) : l16v) : 0u;
    const int dw = 2 * (w & 1) + (i >> 1);
#pragma unroll
    for (int k = 0; k < 4; ++k) a2[k] = (k == dw) ? lv : 0u;
    f32x16 n;
#pragma unroll
    for (int j = 0; j < 16; ++j) n[j] = 0.f;
    n = mfma32(a1, bq[i], n);
    n = mfma32(a2, bl, n);
    float rd = __builtin_amdgcn_rcpf(n[9] + 1e-6f);
    u32x4 o;
#pragma unroll
    for (int k = 0; k < 4; ++k) o[k] = pack2(n[2 * k] * rd, n[2 * k + 1] * rd);
    lo[i] = n[8] * rd;
    const int head = 4 * w + i, s = head >> 1, qq = 2 * (head & 1) + h;
    *(u32x4*)(smem + (((r >> 4) * 18 + s) * 64 + qq * 16 + (r & 15)) * 16) = o;
  }
  {
    const int s = 16 + (w >> 2), qq = 2 * (w & 1) + h, half = (w >> 1) & 1;
    *(u32x2*)(smem + (((r >> 4) * 18 + s) * 64 + qq * 16 + (r & 15)) * 16 + half * 8) = u32x2{pack2(lo[0], lo[1]), pack2(lo[2], lo[3])};
  }
  STAMP(2, 2);
  asm volatile("s_waitcnt lgkmcnt(0)" ::: "memory");
  bar();
  STAMP(2, 3);
  {
    asm volatile("" : "+v"(A8[0]), "+v"(A8[1]), "+v"(A8[2]));
    f32x4 p8[2] = {{0.f, 0.f, 0.f, 0.f}, {0.f, 0.f, 0.f, 0.f}};
#pragma unroll
    for (int j = 0; j < 3; ++j) {
      const int s = w + 8 * j;
      if (j < 2 || w < 2) {
        u32x4 b0 = *(const u32x4*)(smem + (s * 64 + lane) * 16);
        u32x4 b1 = *(const u32x4*)(smem + ((18 + s) * 64 + lane) * 16);
        p8[0] = mfma16(A8[j], b0, p8[0]);
        p8[1] = mfma16(A8[j], b1, p8[1]);
      }
    }
    *(f32x4*)(smem + 36 * 1024 + ((w * 2 + 0) * 64 + lane) * 16) = p8[0];
    *(f32x4*)(smem + 36 * 1024 + ((w * 2 + 1) * 64 + lane) * 16) = p8[1];
  }
  f32x4 acc[2][2] = {{{0.f, 0.f, 0.f, 0.f}, {0.f, 0.f, 0.f, 0.f}}, {{0.f, 0.f, 0.f, 0.f}, {0.f, 0.f, 0.f, 0.f}}};
  {
    u32x4 b0[18], b1[18];
#pragma unroll
    for (int s = 0; s < 18; ++s) {
      b0[s] = *(const u32x4*)(smem + (s * 64 + lane) * 16);
      b1[s] = *(const u32x4*)(smem + ((18 + s) * 64 + lane) * 16);
    }
    __builtin_amdgcn_sched_barrier(0);
#pragma unroll
    for (int s = 0; s < 18; ++s) pin(A[s]);
#pragma unroll
    for (int s = 0; s < 18; ++s) {
      acc[0][s & 1] = mfma16(A[s], b0[s], acc[0][s & 1]);
      acc[1][s & 1] = mfma16(A[s], b1[s], acc[1][s & 1]);
    }
#pragma unroll
    for (int j = 0; j < 4; ++j) { acc[0][0][j] += acc[0][1][j]; acc[1][0][j] += acc[1][1][j]; }
  }
  STAMP(2, 4);
  asm volatile("s_waitcnt vmcnt(0) lgkmcnt(0)" : "+v"(res[0]), "+v"(res[1]), "+v"(res8) :: "memory");
  bar();
  f32x2* sred = (f32x2*)(smem + 52 * 1024);
  auto finish = [&](const f32x4& a, u32x2 rs, unsigned off, int slot, int tok) {
    float v0 = bflo(rs[0]) + a[0], v1 = bfhi(rs[0]) + a[1], v2 = bflo(rs[1]) + a[2], v3 = bfhi(rs[1]) + a[3];
    u32x2 o = {pack2(v0, v1), pack2(v2, v3)};
    hstore8(xout, off, o);
    v0 = bflo(o[0]); v1 = bfhi(o[0]); v2 = bflo(o[1]); v3 = bfhi(o[1]);
    float s = sum_x32(sum_x16((v0 + v1) + (v2 + v3)));
    float m = s * (1.f / 16.f);
    float d0 = v0 - m, d1 = v1 - m, d2 = v2 - m, d3 = v3 - m;
    float qq = sum_x32(sum_x16(d0 * d0 + d1 * d1 + d2 * d2 + d3 * d3));
    if (q == 0) sred[slot * 32 + tok] = f32x2{m, qq};
  };
  finish(acc[0][0], res[0], xo, w, t);
  finish(acc[1][0], res[1], xo + 256, w, 16 + t);
  if (w < 2) {
    f32x4 a = {0.f, 0.f, 0.f, 0.f};
#pragma unroll
    for (int i = 0; i < 8; ++i) {
      f32x4 pv = *(const f32x4*)(smem + 36 * 1024 + ((i * 2 + (w & 1)) * 64 + lane) * 16);
      a[0] += pv[0]; a[1] += pv[1]; a[2] += pv[2]; a[3] += pv[3];
    }
    finish(a, res8, xo8, 8, 16 * (w & 1) + t);
  }
  asm volatile("s_waitcnt lgkmcnt(0)" ::: "memory");
  bar();
  if (tid < 32) {
    float mm[9], ms = 0.f, qq = 0.f;
#pragma unroll
    for (int i = 0; i < 9; ++i) { f32x2 v = sred[i * 32 + tid]; mm[i] = v[0]; ms += v[0]; qq += v[1]; }
    float mean = ms * (1.f / 9.f);
#pragma unroll
    for (int i = 0; i < 9; ++i) { float d = mm[i] - mean; qq += 16.f * d * d; }
    hstore8(stout, ((tb * 32 + tid) * 4 + cg) * 8, u32x2{__builtin_bit_cast(unsigned, mean), __builtin_bit_cast(unsigned, qq)});
  }
  STAMP(2, 5);
  sig_done(ws, SY_KB + 8 * lay_ + xcd);
  STAMP(2, 9);
}

#define GU_STAGE_ 32
#define GU_RING_ 128
constexpr int GU_STAGE = GU_STAGE_, GU_RING = GU_RING_, GU_P1 = 111;
constexpr int GU_PD = 5;
constexpr int GU_NSLOT = GU_RING / GU_STAGE, GU_NPIECE = (GU_NFRAG + 7) / 8;
constexpr int GU_NST = (GU_NFRAG + GU_STAGE - 1) / GU_STAGE;
struct GuSched { int n; int kind[96]; int arg[96]; int step[96]; };
#define GU_PRO_X_ 10
#define GU_PRO_P_ 12
#define GU_PACE 5
#define GU_LAG_ 1
constexpr int GU_PRO_X = GU_PRO_X_;
constexpr int GU_PRO_P = GU_PRO_P_;
constexpr int gu_pstage(int p) { return 8 * p / GU_STAGE; }
constexpr int gu_pdead(int p) { int st = gu_pstage(p); return st == 0 ? 0 : st * GU_STAGE - GU_PD; }
constexpr int gu_pearly(int p) { int st = gu_pstage(p); return st < GU_NSLOT ? 0 : (st - GU_NSLOT + 2) * GU_STAGE - GU_PD + 2; }
constexpr GuSched gu_make_sched() {
  GuSched s{};
  int n = 0;
  auto push = [&](int k, int a, int st) { s.kind[n] = k; s.arg[n] = a; s.step[n] = st; ++n; };
  int kx = 0, pc = 0;
  while (pc < GU_PRO_P) push(1, pc++, -2);
  push(2, 0, -1); push(2, 1, -1);
  while (kx < GU_PRO_X) push(0, kx++, -1);
  int m = 0, last = 0;
  while (kx < 36 || pc < GU_NPIECE) {
    int dx = kx < 36 ? 3 * kx : 100000, ds = pc < GU_NPIECE ? gu_pdead(pc) : 100000;
    bool takep = ds <= dx;
    int st = kx < 36 ? (m * GU_PACE) / 2 : last + 5;
    if (takep && st < gu_pearly(pc)) { if (kx < 36) { takep = false; } else st = gu_pearly(pc); }
    if (st < last) st = last;
    if (takep) push(1, pc++, st); else push(0, kx++, st);
    last = st; ++m;
  }
  s.n = n;
  return s;
}
constexpr GuSched GUS = gu_make_sched();
constexpr int GU_LAG = GU_LAG_;
constexpr int gu_cnt_raw(int step) { int c = 0; for (int i = 0; i < GUS.n; ++i) if (GUS.step[i] < step) ++c; return c; }
constexpr int gu_cnt_before(int step) { return gu_cnt_raw(step - GU_LAG < 0 ? 0 : step - GU_LAG); }
constexpr int gu_idx(int kind, int arg) { for (int i = 0; i < GUS.n; ++i) if (GUS.kind[i] == kind && GUS.arg[i] == arg) return i; return -1; }
constexpr int gu_clamp(int n) { return n > 63 ? 63 : (n < 0 ? 0 : n); }
constexpr int gu_wait_for(int idx, int step) { return gu_clamp(gu_cnt_before(step) - idx - 1); }
constexpr int gu_last_piece(int st) { int p = (st + 1) * GU_STAGE / 8 - 1; return gu_idx(1, p < GU_NPIECE ? p : GU_NPIECE - 1); }
constexpr bool gu_sched_ok() {
  for (int i = 0; i < GUS.n; ++i) {
    if (GUS.kind[i] == 1) { if (GUS.step[i] + GU_LAG >= gu_pdead(GUS.arg[i]) && gu_pdead(GUS.arg[i]) > 0) return false;
                            if (gu_pdead(GUS.arg[i]) == 0 && GUS.step[i] >= 0) return false;
                            if (gu_pearly(GUS.arg[i]) > 0 && GUS.step[i] < gu_pearly(GUS.arg[i]) - 2) return false; }
    if (GUS.kind[i] == 0 && GUS.step[i] >= 0 && GUS.step[i] + GU_LAG >= 3 * GUS.arg[i]) return false;
    if (i > 0 && GUS.step[i] < GUS.step[i - 1]) return false;
  }
  return true;
}
static_assert(gu_sched_ok(), "GU schedule");
static_assert(GU_PRO_P >= GU_STAGE / 8, "stage 0 must be requested in the prologue");

struct GuCtx {
  const char* wsrc; const char* xs; const char* sp; unsigned l16, sb, so; int w, grp, h; unsigned rd0, rd1, rd2; float c1, c3, cinv; char* hbase; unsigned hvoff; int fb0;
};
template <int N, int G> DI void gu_issue(const GuCtx& c, u32x4 (&X)[37], u32x4 (&sv)[2]) {
  constexpr int kind = GUS.kind[N], arg = GUS.arg[N];
  if constexpr (kind == 0) {
    if constexpr (G < 0) hload16<0>(X[arg], c.xs + arg * 1024, c.l16);
    else hload16_g<G>(X[arg], c.xs + arg * 1024, c.l16, c.grp);
  }
  if constexpr (kind == 1) {
    constexpr int piece = arg * 8;
    if constexpr (G < 0) glds16(c.wsrc + piece * 1024, c.l16, c.sb + (piece % GU_RING) * 1024 + c.w * 1024);
    else glds16_g<G>(c.wsrc + piece * 1024, c.l16, c.sb + (piece % GU_RING) * 1024 + c.w * 1024, c.grp);
  }
  if constexpr (kind == 2) hload16<16 * arg>(sv[arg], c.sp, c.so);
}
template <int N, int STEP, int G> DI void gu_issue_step(const GuCtx& c, u32x4 (&X)[37], u32x4 (&sv)[2]) {
  if constexpr (N < GUS.n) {
    if constexpr (GUS.step[N] == STEP) gu_issue<N, G>(c, X, sv);
    if constexpr (GUS.step[N] <= STEP) gu_issue_step<N + 1, STEP, G>(c, X, sv);
  }
}
constexpr int gu_epi_start(int j) { int a = GU_P1 + 2 + 24 * j, e = (j < 3 ? GU_P1 : GU_P1 + 37 * (j - 2)) + 2; return a > e ? a : e; }
constexpr int gu_epi_block(int I) { for (int j = 0; j < 8; ++j) if (I >= gu_epi_start(j) && I < gu_epi_start(j) + 24) return j; return -1; }
struct GuEpi { float x, ab; unsigned pk[4]; float olo; };
template <int E> DI void gu_epi_0(const f32x16& a, const GuCtx& c, GuEpi& e) {
  float g = a[E];
  e.x = g * __builtin_fmaf(g * g, c.c3, c.c1);
}
template <int E> DI void gu_epi_1(const f32x16& a, const GuCtx& c, GuEpi& e) {
  float ex = __builtin_amdgcn_exp2f(e.x);
  e.ab = a[E] * a[E + 8];
  e.x = __builtin_fmaf(ex, c.cinv, c.cinv);
}
template <int E> DI void gu_epi_2(const GuCtx& c, GuEpi& e, int fb) {
  float o = e.ab * __builtin_amdgcn_rcpf(e.x);
  if constexpr ((E & 1) == 0) e.olo = o; else e.pk[E >> 1] = pack2(e.olo, o);
  if constexpr (E == 7) hstore16(c.hbase + (size_t)(fb >> 1) * 4096 + (fb & 1) * 512, c.hvoff, u32x4{e.pk[0], e.pk[1], e.pk[2], e.pk[3]});
}
template <int I>
DI void gu_steps(GuCtx& c, u32x4 (&A)[GU_PD], u32x4 (&X)[37], u32x4 (&sv)[2], f32x16 (&acc)[4], GuEpi& ep) {
  if constexpr ((I + GU_PD) % GU_STAGE == 0 && (I + GU_PD) / GU_STAGE < GU_NST) {
    constexpr int s = (I + GU_PD) / GU_STAGE;
    waitv<gu_wait_for(gu_last_piece(s), I)>();
    bar();
  }
  constexpr int blk = I < GU_P1 ? I % 3 : 3 + (I - GU_P1) / 37;
  constexpr int ks = I < GU_P1 ? I / 3 : (I - GU_P1) % 37;
  constexpr int slot = blk & 3;
  if constexpr (I < 108 && I % 3 == 0 && ks >= 2) waitv_pin<gu_wait_for(gu_idx(0, ks), I)>(X[ks]);
  gu_issue_step<gu_cnt_raw(I), I, 0>(c, X, sv);
  if constexpr (I >= GU_LAG) gu_issue_step<gu_cnt_raw(I - GU_LAG), I - GU_LAG, 1>(c, X, sv);
  if constexpr (I >= GU_P1 && ks == 0) {
    f32x16 z;
#pragma unroll
    for (int j = 0; j < 16; ++j) z[j] = 0.f;
    acc[slot] = mfma32(A[I % GU_PD], X[ks], z);
  } else {
    acc[slot] = mfma32(A[I % GU_PD], X[ks], acc[slot]);
  }
  if constexpr (I + GU_PD < GU_NFRAG) {
    constexpr int fr = (I + GU_PD) % GU_RING;
    A[I % GU_PD] = lds_rd((fr < 64 ? c.rd0 : (fr < 128 ? c.rd1 : c.rd2)) + (fr & 63) * 1024);
  }
  __builtin_amdgcn_sched_barrier(0);
  {
    constexpr int j = gu_epi_block(I);
    if constexpr (j >= 0) {
      constexpr int d = I - gu_epi_start(j);
      if constexpr (d % 3 == 0) gu_epi_0<d / 3>(acc[j & 3], c, ep);
      if constexpr (d % 3 == 1) gu_epi_1<d / 3>(acc[j & 3], c, ep);
      if constexpr (d % 3 == 2) gu_epi_2<d / 3>(c, ep, c.fb0 + j);
    }
  }
  __builtin_amdgcn_sched_barrier(0);
  if constexpr (I + 1 < GU_NFRAG) gu_steps<I + 1>(c, A, X, sv, acc, ep);
}

DI void gu_phase(char* smem, char* ws, const int lay_) {
  const char* xf = ws + OFF_XF1 + lay_ * SZ_XF;
  const char* st = ws + OFF_ST1 + lay_ * SZ_ST1;
  const char* wguf = ws + OFF_WGU;
  char* hf = ws + OFF_HF + lay_ * SZ_HF;
  const int tid = tid_opaque(), lane = tid & 63;
  const int w = __builtin_amdgcn_readfirstlane(tid >> 6);
  const int b = blockIdx.x, xcd = b & 7, idx = b >> 3;
  const int tt = idx & 7, fs = 4 * xcd + (idx >> 3);
  const int tb32 = 8 * tt + w;
  const unsigned sb = lds_addr(smem), l16 = lane * 16;
  const int h = lane >> 5, r = lane & 31;

  STAMP(3, 0);
  GuCtx c;
  c.wsrc = wguf + (size_t)fs * GU_NFRAG * 1024 + w * 1024;
  c.xs = xf + (size_t)tb32 * 36 * 1024;
  c.sp = st + (size_t)(tb32 * 32) * 32; c.so = r * 32;
  c.l16 = l16; c.sb = sb; c.w = w; c.grp = w >> 2; c.h = h; c.fb0 = 9 * fs;
  c.c1 = 0.f; c.c3 = 0.f; c.cinv = 0.f;
  u32x4 sv[2], X[37];
  gu_issue_step<0, -2, -1>(c, X, sv);
  STAMP(3, 8);
  dep_wait(ws, SY_KB + 8 * lay_ + tt, 1, 32);
  STAMP(3, 9);
  gu_issue_step<gu_cnt_raw(-1), -1, -1>(c, X, sv);
#pragma unroll
  for (int k = GU_PRO_X; k < 36; ++k) asm volatile("" : "=v"(X[k]));
  STAMP(3, 1);
  f32x16 acc[4];
#pragma unroll
  for (int i = 0; i < 3; ++i)
#pragma unroll
    for (int j = 0; j < 16; ++j) acc[i][j] = 0.f;
  {
    asm volatile("s_waitcnt vmcnt(%2)" : "+v"(sv[0]), "+v"(sv[1]) : "n"(gu_wait_for(gu_idx(2, 1), 0)));
    float m0 = asf(sv[0][0]), q0 = asf(sv[0][1]), m1 = asf(sv[0][2]), q1 = asf(sv[0][3]);
    float m2 = asf(sv[1][0]), q2 = asf(sv[1][1]), m3 = asf(sv[1][2]), q3 = asf(sv[1][3]);
    float mean = 0.25f * ((m0 + m1) + (m2 + m3));
    float d0 = m0 - mean, d1 = m1 - mean, d2 = m2 - mean, d3 = m3 - mean;
    float M2 = (q0 + q1) + (q2 + q3) + 144.f * (d0 * d0 + d1 * d1 + d2 * d2 + d3 * d3);
    c.cinv = M2 * (1.f / 576.f) + 1e-5f;
    float rstd = rsqrtf(c.cinv);
    c.c1 = -2.3022081985f * rstd;
    c.c3 = -0.1029432397f * rstd * rstd * rstd;
    X[36] = mean_frag(mean, h);
  }
  waitv<gu_wait_for(gu_last_piece(0) > gu_idx(0, 1) ? gu_last_piece(0) : gu_idx(0, 1), 0)>();
  pin(X[0]); pin(X[1]);
  bar();
  STAMP(3, 2);
  c.rd0 = sb + l16; c.rd1 = sb + l16 + 65536; c.rd2 = sb + l16 + 131072;
  asm volatile("" : "+v"(c.rd0), "+v"(c.rd1));
  if (GU_RING > 128) asm volatile("" : "+v"(c.rd2));
  c.hbase = hf + (size_t)(tb32 >> 1) * 144 * 4096;
  c.hvoff = (2 * (tb32 & 1) + (r >> 4)) * 1024 + h * 256 + (r & 15) * 16;
  u32x4 A[GU_PD];
#pragma unroll
  for (int i = 0; i < GU_PD; ++i) A[i] = lds_rd(c.rd0 + i * 1024);
  GuEpi ep;
  __builtin_amdgcn_sched_barrier(0);
  gu_steps<0>(c, A, X, sv, acc, ep);
  STAMP(3, 3);
  {
    float o[8];
#pragma unroll
    for (int e = 0; e < 8; ++e) {
      float g = acc[0][e];
      float ex = __builtin_amdgcn_exp2f(g * __builtin_fmaf(g * g, c.c3, c.c1));
      o[e] = g * acc[0][e + 8] * __builtin_amdgcn_rcpf(__builtin_fmaf(ex, c.cinv, c.cinv));
    }
    const int fb = c.fb0 + 8;
    hstore16(c.hbase + (size_t)(fb >> 1) * 4096 + (fb & 1) * 512, c.hvoff,
                u32x4{pack2(o[0], o[1]), pack2(o[2], o[3]), pack2(o[4], o[5]), pack2(o[6], o[7])});
  }
  STAMP(3, 4);
  sig_done(ws, SY_GU + 8 * lay_ + tt);
  STAMP(3, 10);
}

#define D_DEPTH_ 4
constexpr int D_DEPTH = D_DEPTH_;
template <int NC, int Sx> DI void d_load_b(u32x4 (&Bf)[D_DEPTH][4], const char* hs, unsigned l16) {
  constexpr int sl = Sx % D_DEPTH;
  const char* hp = hs + Sx * 4096;
  hload16<0>(Bf[sl][0], hp, l16); hload16<1024>(Bf[sl][1], hp, l16); hload16<2048>(Bf[sl][2], hp, l16); hload16<3072>(Bf[sl][3], hp, l16);
}
template <int NC, int Sx> DI void d_load_a(u32x4 (&Af)[D_DEPTH][5], const char* ws_, unsigned l16) {
  constexpr int sl = Sx % D_DEPTH;
  const char* wp = ws_ + Sx * NC * 1024;
  gload16<0>(Af[sl][0], wp, l16); gload16<1024>(Af[sl][1], wp, l16); gload16<2048>(Af[sl][2], wp, l16); gload16<3072>(Af[sl][3], wp, l16);
  if constexpr (NC == 5) gload16<0>(Af[sl][4], wp + 4096, l16);
}
struct DRes { const char* x1; unsigned off; u32x4 v[2]; };
constexpr int d_wait_count(int NC, int s) {
  const int per = 4 + NC;
  const int later = s < 36 - D_DEPTH ? s : 36 - D_DEPTH;
  const int issued = D_DEPTH * per + later * per;
  const int last = s < D_DEPTH ? D_DEPTH * NC + 4 * (s + 1) - 1 : D_DEPTH * per + (s - D_DEPTH + 1) * per - 1;
  return issued - 1 - last;
}
template <int NC, int Sx>
DI void d_steps(u32x4 (&Bf)[D_DEPTH][4], u32x4 (&Af)[D_DEPTH][5], f32x4 (&acc)[4][5], const char* hs, const char* ws_, unsigned l16, DRes& rs) {
  constexpr int sl = Sx % D_DEPTH;
  constexpr int n = d_wait_count(NC, Sx);
  if constexpr (NC == 5)
    asm volatile("s_waitcnt vmcnt(%9)" : "+v"(Bf[sl][0]), "+v"(Bf[sl][1]), "+v"(Bf[sl][2]), "+v"(Bf[sl][3]), "+v"(Af[sl][0]), "+v"(Af[sl][1]),
                 "+v"(Af[sl][2]), "+v"(Af[sl][3]), "+v"(Af[sl][4]) : "n"(n));
  else
    asm volatile("s_waitcnt vmcnt(%8)" : "+v"(Bf[sl][0]), "+v"(Bf[sl][1]), "+v"(Bf[sl][2]), "+v"(Bf[sl][3]), "+v"(Af[sl][0]), "+v"(Af[sl][1]),
                 "+v"(Af[sl][2]), "+v"(Af[sl][3]) : "n"(n));
#pragma unroll
  for (int c = 0; c < NC; ++c)
#pragma unroll
    for (int t = 0; t < 4; ++t) acc[t][c] = mfma16(Af[sl][c], Bf[sl][t], acc[t][c]);
  if constexpr (Sx + D_DEPTH < 36) {
    __builtin_amdgcn_sched_barrier(0);
    d_load_b<NC, Sx + D_DEPTH>(Bf, hs, l16);
    d_load_a<NC, Sx + D_DEPTH>(Af, ws_, l16);
  }
  if constexpr (Sx == 36 - D_DEPTH + 1) {
    __builtin_amdgcn_sched_barrier(0);
    hload16<0>(rs.v[0], rs.x1, rs.off); hload16<512>(rs.v[1], rs.x1, rs.off);
  }
  if constexpr (Sx + 1 < 36) d_steps<NC, Sx + 1>(Bf, Af, acc, hs, ws_, l16, rs);
}
constexpr int D_STRIDE = 84;
template <int NC>
DI void d_body(char* smem, char* ws, char* dout, const int lay_, int tt, int g, int tid, int lane, int w8) {
  const char* hf = ws + OFF_HF + lay_ * SZ_HF;
  const char* wdf = ws + OFF_WD;
  char* xout = ws + OFF_XF2;
  char* stout = ws + OFF_ST2;
  const unsigned l16 = lane * 16;
  const int st = dg_start(g);
  DRes rs;
  rs.x1 = ws + OFF_XF1 + lay_ * SZ_XF;
  const bool has = tid < 64 * NC;
  const int it = has ? tid : 0;
  int token, kl;
  if (lay_) { token = it / NC; kl = it - token * NC; } else { token = it & 63; kl = it >> 6; }
  const int gt = tt * 64 + token, ks = st + kl;
  rs.off = (((gt >> 5) * 36 + ks) * 64 + (gt & 31)) * 16;
  float* red = (float*)smem;
  STAMP(4, 0);
  if (w8 < 4) {
    const char* hs = hf + ((size_t)(tt * 144 + 36 * w8) * 4) * 1024;
    const char* ws_ = wdf + ((size_t)st * 144 + (size_t)36 * w8 * NC) * 1024;
    u32x4 Bf[D_DEPTH][4], Af[D_DEPTH][5];
    f32x4 acc[4][5];
#pragma unroll
    for (int t = 0; t < 4; ++t)
#pragma unroll
      for (int c = 0; c < 5; ++c) acc[t][c] = f32x4{0.f, 0.f, 0.f, 0.f};
    d_load_a<NC, 0>(Af, ws_, l16); d_load_a<NC, 1>(Af, ws_, l16); d_load_a<NC, 2>(Af, ws_, l16);
    if constexpr (D_DEPTH > 3) d_load_a<NC, 3>(Af, ws_, l16);
    STAMP(4, 6);
    dep_wait(ws, SY_GU + 8 * lay_ + (tt >> 2), 1, 32);
    STAMP(4, 7);
    d_load_b<NC, 0>(Bf, hs, l16); d_load_b<NC, 1>(Bf, hs, l16); d_load_b<NC, 2>(Bf, hs, l16);
    if constexpr (D_DEPTH > 3) d_load_b<NC, 3>(Bf, hs, l16);
    STAMP(4, 1);
    d_steps<NC, 0>(Bf, Af, acc, hs, ws_, l16, rs);
    STAMP(4, 2);
    const int q = lane >> 4, t = lane & 15;
    float* rp = red + (size_t)w8 * 64 * D_STRIDE + t * D_STRIDE + 4 * q;
#pragma unroll
    for (int tb = 0; tb < 4; ++tb)
#pragma unroll
      for (int c = 0; c < NC; ++c) *(f32x4*)(rp + tb * 16 * D_STRIDE + c * 16) = acc[tb][c];
  } else {
    dep_wait(ws, SY_GU + 8 * lay_ + (tt >> 2), 1, 32);
    hload16<0>(rs.v[0], rs.x1, rs.off); hload16<512>(rs.v[1], rs.x1, rs.off);
  }
  asm volatile("s_waitcnt vmcnt(0) lgkmcnt(0)" : "+v"(rs.v[0]), "+v"(rs.v[1]) :: "memory");
  bar();
  STAMP(4, 3);
  f32x2* sred = (f32x2*)(smem + 4 * 64 * D_STRIDE * 4);
  if (has) {
    const float* rp = red + token * D_STRIDE + kl * 16;
    float v[16];
#pragma unroll
    for (int i = 0; i < 4; ++i) {
      f32x4 a = *(const f32x4*)(rp + 4 * i), b = *(const f32x4*)(rp + 64 * D_STRIDE + 4 * i);
      f32x4 c = *(const f32x4*)(rp + 2 * 64 * D_STRIDE + 4 * i), d = *(const f32x4*)(rp + 3 * 64 * D_STRIDE + 4 * i);
#pragma unroll
      for (int j = 0; j < 4; ++j) v[4 * i + j] = (a[j] + b[j]) + (c[j] + d[j]);
    }
    const u32x4 r0 = rs.v[0], r1 = rs.v[1];
    u32x4 o0, o1;
#pragma unroll
    for (int i = 0; i < 4; ++i) {
      o0[i] = pack2(bflo(r0[i]) + v[2 * i], bfhi(r0[i]) + v[2 * i + 1]);
      o1[i] = pack2(bflo(r1[i]) + v[8 + 2 * i], bfhi(r1[i]) + v[8 + 2 * i + 1]);
    }
    if (lay_) {
      char* dst = dout + ((size_t)gt * D + 16 * ks) * 2;
      *(u32x4*)dst = o0; *(u32x4*)(dst + 16) = o1;
    } else {
      hstore16(xout, rs.off, o0); hstore16(xout + 512, rs.off, o1);
      float s = 0.f;
#pragma unroll
      for (int i = 0; i < 4; ++i) s += bflo(o0[i]) + bfhi(o0[i]) + bflo(o1[i]) + bfhi(o1[i]);
      float m = s * (1.f / 16.f), qq = 0.f;
#pragma unroll
      for (int i = 0; i < 4; ++i) {
        float a = bflo(o0[i]) - m, b = bfhi(o0[i]) - m, c = bflo(o1[i]) - m, d = bfhi(o1[i]) - m;
        qq += a * a + b * b + c * c + d * d;
      }
      sred[kl * 64 + token] = f32x2{m, qq};
    }
  }
  STAMP(4, 4);
  if (!lay_) {
    asm volatile("s_waitcnt lgkmcnt(0)" ::: "memory");
    bar();
    if (tid < 64) {
      float mm[NC], ms = 0.f, qq = 0.f;
#pragma unroll
      for (int i = 0; i < NC; ++i) { f32x2 v = sred[i * 64 + tid]; mm[i] = v[0]; ms += v[0]; qq += v[1]; }
      float mean = ms * (1.f / NC);
#pragma unroll
      for (int i = 0; i < NC; ++i) { float d = mm[i] - mean; qq += 16.f * d * d; }
      hstore8(stout, ((tt * 64 + tid) * 8 + g) * 8, u32x2{__builtin_bit_cast(unsigned, mean), __builtin_bit_cast(unsigned, qq)});
    }
    STAMP(4, 5);
    sig_done(ws, SY_D + (tt >> 1));
  }
}
DI void d_phase(char* smem, char* ws, char* dout, const int lay_) {
  const int tid = tid_opaque(), lane = tid & 63;
  const int w8 = __builtin_amdgcn_readfirstlane(tid >> 6);
  const int b = blockIdx.x, xcd = b & 7, idx = b >> 3;
  const int tt = 4 * xcd + (idx & 3), g = idx >> 2;
  if (g < 4) d_body<5>(smem, ws, dout, lay_, tt, g, tid, lane, w8);
  else d_body<4>(smem, ws, dout, lay_, tt, g, tid, lane, w8);
}

__global__ __launch_bounds__(512, 2) void k_layers(char* ws, char* dout) {
  __shared__ __attribute__((aligned(16))) char smem[KA_RING * 4096];
#pragma nounroll
  for (int l = 0; l < 2; ++l) {
    ka_phase(smem, ws, l);
    kb_phase(smem, ws, l);
    gu_phase(smem, ws, l);
    d_phase(smem, ws, dout, l);
  }
}

extern "C" void kernel_launch(void* const* d_in, const int* in_sizes, int n_in, void* d_out, int out_size, void* d_ws, size_t ws_size,
                              hipStream_t stream) {
  const unsigned short* x = (const unsigned short*)d_in[0];
  const unsigned short* wqkv = (const unsigned short*)d_in[1];
  const unsigned short* wo = (const unsigned short*)d_in[2];
  const unsigned short* wg = (const unsigned short*)d_in[4];
  const unsigned short* wu = (const unsigned short*)d_in[5];
  const unsigned short* wd = (const unsigned short*)d_in[6];
  char* ws = (char*)d_ws;
  k_prep<<<PB_TOTAL, 512, 0, stream>>>(x, wqkv, wo, wg, wu, wd, ws);
  k_layers<<<256, 512, 0, stream>>>(ws, (char*)d_out);
}
```

```cpp
#include <hip/hip_runtime.h>
#include <hip/hip_bf16.h>
#include <stdint.h>

typedef __attribute__((ext_vector_type(8))) __bf16 bf16x8;
typedef __attribute__((ext_vector_type(2))) __bf16 bf16x2;
typedef __attribute__((ext_vector_type(2))) float f32x2;
typedef __attribute__((ext_vector_type(4))) float f32x4;
typedef __attribute__((ext_vector_type(16))) float f32x16;
typedef __attribute__((ext_vector_type(4))) unsigned u32x4;
typedef __attribute__((ext_vector_type(2))) unsigned u32x2;
#define DI __device__ __forceinline__
#define LDS_AS __attribute__((address_space(3)))

#define STAMP(kid, i) do { } while (0)
constexpr int S = 2048, D = 576, NH = 32, DH = 18, F = 4608;

constexpr size_t SZ_XF = (size_t)S * D * 2;
constexpr size_t OFF_XF0 = 0;
constexpr size_t OFF_XF2 = OFF_XF0 + SZ_XF;
constexpr size_t OFF_XF1 = OFF_XF2 + SZ_XF;
constexpr size_t OFF_ST0 = OFF_XF1 + 2 * SZ_XF;
constexpr size_t OFF_ST2 = OFF_ST0 + (size_t)S * 64;
constexpr size_t SZ_ST1 = (size_t)S * 32;
constexpr size_t OFF_ST1 = OFF_ST2 + (size_t)S * 64;
constexpr size_t SZ_PQF = (size_t)64 * 32 * 1024, SZ_PQL = (size_t)64 * 4 * 1024;
constexpr size_t OFF_PQF = OFF_ST1 + 2 * SZ_ST1;
constexpr size_t OFF_PQL = OFF_PQF + 2 * SZ_PQF;
constexpr size_t KV_DBL = (size_t)NH * 342 + 64;
constexpr size_t OFF_KV = OFF_PQL + 2 * SZ_PQL;
constexpr size_t SZ_KV = 2 * KV_DBL * 8;
constexpr size_t OFF_SYNC = OFF_KV + SZ_KV;
constexpr size_t SZ_ZERO = SZ_KV + 116 * 256;
constexpr int KA_NFRAG = 148, GU_NFRAG = 333;
constexpr size_t OFF_WQKV = OFF_KV + SZ_ZERO;
constexpr size_t OFF_WO = OFF_WQKV + (size_t)16 * KA_NFRAG * 1024;
constexpr size_t OFF_WGU = OFF_WO + (size_t)36 * 18 * 1024;
constexpr size_t OFF_WD = OFF_WGU + (size_t)32 * GU_NFRAG * 1024 + 16384;
constexpr size_t SZ_HF = (size_t)S * F * 2;
constexpr size_t OFF_HF = OFF_WD + (size_t)36 * 144 * 1024;
constexpr size_t WS_END = OFF_HF + 2 * SZ_HF;
static_assert(OFF_WQKV % 1024 == 0 && WS_END < (size_t)200 << 20, "ws map");

DI unsigned pack2(float lo, float hi) {
  f32x2 f = {lo, hi};
  bf16x2 v = __builtin_convertvector(f, bf16x2);
  return __builtin_bit_cast(unsigned, v);
}
DI float asf(unsigned u) { return __builtin_bit_cast(float, u); }
DI float bflo(unsigned u) { return __builtin_bit_cast(float, u << 16); }
DI float bfhi(unsigned u) { return __builtin_bit_cast(float, u & 0xffff0000u); }
DI bf16x8 asbf(u32x4 v) { return __builtin_bit_cast(bf16x8, v); }
DI f32x16 mfma32(u32x4 a, u32x4 b, f32x16 c) { return __builtin_amdgcn_mfma_f32_32x32x16_bf16(asbf(a), asbf(b), c, 0, 0, 0); }
DI f32x4 mfma16(u32x4 a, u32x4 b, f32x4 c) { return __builtin_amdgcn_mfma_f32_16x16x32_bf16(asbf(a), asbf(b), c, 0, 0, 0); }
DI unsigned lds_addr(const void* p) { return (unsigned)(size_t)(LDS_AS const char*)p; }

DI void glds16(const void* sbase, unsigned voff, unsigned dst) {
  unsigned keep;
  asm volatile("s_mov_b32 %0, m0\n\ts_mov_b32 m0, %3\n\ts_nop 0\n\tglobal_load_lds_dwordx4 %1, %2\n\ts_mov_b32 m0, %0"
               : "=&s"(keep) : "v"(voff), "s"(sbase), "s"(dst) : "memory");
}
template <int IMM> DI void gload16(u32x4& dst, const void* sbase, unsigned voff) {
  asm volatile("global_load_dwordx4 %0, %1, %2 offset:%3" : "=v"(dst) : "v"(voff), "s"(sbase), "n"(IMM));
}
template <int IMM> DI void hload16(u32x4& dst, const void* sbase, unsigned voff) {
  asm volatile("global_load_dwordx4 %0, %1, %2 offset:%3 sc1" : "=v"(dst) : "v"(voff), "s"(sbase), "n"(IMM));
}
template <int IMM> DI void hload8(u32x2& dst, const void* sbase, unsigned voff) {
  asm volatile("global_load_dwordx2 %0, %1, %2 offset:%3 sc1" : "=v"(dst) : "v"(voff), "s"(sbase), "n"(IMM));
}
DI void hstore16(char* sbase, unsigned voff, u32x4 v) {
  asm volatile("s_nop 4\n\tglobal_store_dwordx4 %0, %1, %2 sc1\n\ts_nop 1" ::"v"(voff), "v"(v), "s"(sbase) : "memory");
}
DI void hstore8(char* sbase, unsigned voff, u32x2 v) {
  asm volatile("s_nop 4\n\tglobal_store_dwordx2 %0, %1, %2 sc1" ::"v"(voff), "v"(v), "s"(sbase) : "memory");
}
DI void hstore4(char* sbase, unsigned voff, unsigned v) {
  asm volatile("s_nop 4\n\tglobal_store_dword %0, %1, %2 sc1" ::"v"(voff), "v"(v), "s"(sbase) : "memory");
}
template <int G> DI void hload16_g(u32x4& dst, const void* sbase, unsigned voff, int grp) {
  asm volatile("s_cmp_lg_u32 %3, %4\n\ts_cbranch_scc1 .Lsk%=\n\tglobal_load_dwordx4 %0, %1, %2 sc1\n.Lsk%=:"
               : "+v"(dst) : "v"(voff), "s"(sbase), "s"(grp), "n"(G) : "scc");
}
template <int G> DI void glds16_g(const void* sbase, unsigned voff, unsigned dst, int grp) {
  unsigned keep;
  asm volatile("s_cmp_lg_u32 %4, %5\n\ts_cbranch_scc1 .Lsk%=\n\ts_mov_b32 %0, m0\n\ts_mov_b32 m0, %3\n\ts_nop 0\n\tglobal_load_lds_dwordx4 %1, %2\n\ts_mov_b32 m0, %0\n.Lsk%=:"
               : "=&s"(keep) : "v"(voff), "s"(sbase), "s"(dst), "s"(grp), "n"(G) : "memory", "scc");
}
template <int IMM> DI void gload8(u32x2& dst, const void* sbase, unsigned voff) {
  asm volatile("global_load_dwordx2 %0, %1, %2 offset:%3" : "=v"(dst) : "v"(voff), "s"(sbase), "n"(IMM));
}
DI float sum_x16(float x) {
  auto r = __builtin_amdgcn_permlane16_swap(__builtin_bit_cast(unsigned, x), __builtin_bit_cast(unsigned, x), false, false);
  return __builtin_bit_cast(float, (unsigned)r[0]) + __builtin_bit_cast(float, (unsigned)r[1]);
}
DI float sum_x32(float x) {
  auto r = __builtin_amdgcn_permlane32_swap(__builtin_bit_cast(unsigned, x), __builtin_bit_cast(unsigned, x), false, false);
  return __builtin_bit_cast(float, (unsigned)r[0]) + __builtin_bit_cast(float, (unsigned)r[1]);
}
template <int N> DI void waitv() { asm volatile("s_waitcnt vmcnt(%0)" ::"n"(N) : "memory"); }
template <int N> DI void waitv_pin(u32x4& x) { asm volatile("s_waitcnt vmcnt(%1)" : "+v"(x) : "n"(N)); }
DI void pin(u32x4& x) { asm volatile("" : "+v"(x)); }
DI u32x4 lds_rd(unsigned addr) { return *(const LDS_AS u32x4*)(size_t)addr; }
DI void bar() { asm volatile("" ::: "memory"); __builtin_amdgcn_s_barrier(); asm volatile("" ::: "memory"); }

__host__ __device__ constexpr int dg_start(int g) { return g < 4 ? 5 * g : 20 + 4 * (g - 4); }
__host__ __device__ constexpr int dg_count(int g) { return g < 4 ? 5 : 4; }

DI int tid_opaque() { int t = threadIdx.x; asm volatile("" : "+v"(t)); return t; }

typedef __attribute__((address_space(1))) unsigned gu32;
constexpr int KA_REP = 4;
constexpr int SY_KA = 0, SY_KB = 64, SY_GU = 80, SY_D = 96, SY_TMO = 112;
DI gu32* sync_word(char* ws, int i) { return (gu32*)(ws + OFF_SYNC + (size_t)i * 256); }
DI void sig_done(char* ws, int i, int nrep = 1, int stride = 0) {
  asm volatile("s_waitcnt vmcnt(0) lgkmcnt(0)" ::: "memory");
  bar();
  if ((int)threadIdx.x < nrep) __hip_atomic_fetch_add(sync_word(ws, i + (int)threadIdx.x * stride), 1u, __ATOMIC_RELAXED, __HIP_MEMORY_SCOPE_AGENT);
}
#define POLL_SLEEP 8
DI void dep_wait(char* ws, int i, int n, unsigned target) {
  if (threadIdx.x < 64) {
    const int lane = threadIdx.x;
    gu32* wd = sync_word(ws, i + (lane < n ? lane : 0));
    for (unsigned spins = 0;; ++spins) {
      const unsigned v = __hip_atomic_load(wd, __ATOMIC_RELAXED, __HIP_MEMORY_SCOPE_AGENT);
      if (__all(v >= target)) break;
      __builtin_amdgcn_s_sleep(POLL_SLEEP);
      if (spins > (1u << 18)) { if (lane == 0) __hip_atomic_store(sync_word(ws, SY_TMO), 1u, __ATOMIC_RELAXED, __HIP_MEMORY_SCOPE_AGENT); break; }
      if ((spins & 255u) == 255u && __builtin_amdgcn_readfirstlane(__hip_atomic_load(sync_word(ws, SY_TMO), __ATOMIC_RELAXED, __HIP_MEMORY_SCOPE_AGENT))) break;
    }
  }
  bar();
}

#define PLD4(p) __builtin_nontemporal_load(p)
#define PLD2(p) __builtin_nontemporal_load(p)
#define PST4(p, v) (*(u32x4*)(p) = (v))
#define RST4(p, v) PST4(p, v)
typedef __attribute__((ext_vector_type(4))) short s16x4;
DI u32x2 lds_tr(unsigned addr) {
  return __builtin_bit_cast(u32x2, __builtin_amdgcn_ds_read_tr16_b64_v4i16((LDS_AS s16x4*)(size_t)addr));
}
DI float dot_ones(unsigned u, float acc) {
  return __builtin_amdgcn_fdot2_f32_bf16(__builtin_bit_cast(bf16x2, u), __builtin_bit_cast(bf16x2, 0x3f803f80u), acc, false);
}
DI void put_colsum(char* chunk0, int kq, float cs) {
  unsigned p = pack2(-cs, 0.f) & 0xffffu;
  float hi = bflo(p);
  unsigned lo = pack2(-cs - hi, 0.f) & 0xffffu;
#pragma unroll
  for (int t = 0; t < 3; ++t) {
    int sl = 3 * kq + t;
    *(unsigned short*)(chunk0 + (sl >> 3) * 512 + (sl & 7) * 2) = (unsigned short)(t == 1 ? lo : p);
  }
  if (kq == 3) *(u32x2*)(chunk0 + 512 + 8) = u32x2{0u, 0u};
}
__host__ __device__ constexpr int gu_frag_index(int blk, int ks) { return blk < 3 ? 3 * ks + blk : 111 + 37 * (blk - 3) + ks; }

constexpr int P_RS = 144;
struct RtRegs { u32x4 v[5]; };
DI void rt_load(int u, int tid, const unsigned short* wg, const unsigned short* wu, const unsigned short* wd, RtRegs& R) {
  if (u < 576) {
    const int m = u & 1, kq = (u >> 1) & 3, fq = u >> 3;
    const unsigned short* wsrc = m ? wu : wg;
#pragma unroll
    for (int i = 0; i < 5; ++i) {
      int ch = tid + 256 * i; if (ch > 1151) ch = 1151;
      R.v[i] = PLD4((const u32x4*)(wsrc + (size_t)(144 * kq + (ch >> 3)) * F + 64 * fq + 8 * (ch & 7)));
    }
  } else {
    const int ub = u - 576, cgp = ub % 9, sg = ub / 9;
#pragma unroll
    for (int i = 0; i < 4; ++i) {
      int ch = tid + 256 * i;
      R.v[i] = PLD4((const u32x4*)(wd + (size_t)(128 * sg + (ch >> 3)) * D + 64 * cgp + 8 * (ch & 7)));
    }
    R.v[4] = R.v[3];
  }
}
DI void rt_stage(int u, int tid, char* sm, const RtRegs& R) {
  const int nch = u < 576 ? 1152 : 1024;
#pragma unroll
  for (int i = 0; i < 5; ++i) {
    int ch = tid + 256 * i;
    if (ch < nch) *(u32x4*)(sm + (ch >> 3) * P_RS + (ch & 7) * 16) = R.v[i];
  }
}
DI void rt_emit(int u, int tid, char* sm, char* ws) {
  const int lane = tid & 63, w = tid >> 6;
  const unsigned sb = lds_addr(sm);
  if (u < 576) {
    const int m = u & 1, kq = (u >> 1) & 3, fq = u >> 3;
    const int fb = 4 * fq + w, fs = fb / 9, blk = fb - 9 * fs;
    const int g = lane >> 4, h = g & 1, kpar = g >> 1, qq = (lane >> 2) & 3, pp = lane & 3, i16 = lane & 15;
    const int pq = (pp >> 1) | ((pp & 1) << 1);
    const unsigned rd = sb + (16 * kpar + 8 * h + qq) * P_RS + w * 32 + pq * 8;
    char* dst = ws + OFF_WGU + (size_t)fs * GU_NFRAG * 1024 + (h * 32 + m * 16 + i16) * 16;
    float cs = 0.f;
#pragma unroll
    for (int k2 = 0; k2 < 5; ++k2) {
      const int kl = 2 * k2 + kpar, back = kl < 9 ? 0 : 16;
      u32x2 lo = lds_tr(rd + (2 * k2 * 16 - back) * P_RS), hi = lds_tr(rd + (2 * k2 * 16 + 4 - back) * P_RS);
      u32x4 o = {lo[0], lo[1], hi[0], hi[1]};
      if (kl < 9) {
        cs = dot_ones(o[0], cs); cs = dot_ones(o[1], cs); cs = dot_ones(o[2], cs); cs = dot_ones(o[3], cs);
        RST4(dst + (size_t)gu_frag_index(blk, 9 * kq + kl) * 1024, o);
      }
    }
    cs += __shfl_xor(cs, 16); cs += __shfl_xor(cs, 32);
    if (lane < 16) put_colsum(ws + OFF_WGU + ((size_t)fs * GU_NFRAG + gu_frag_index(blk, 36)) * 1024 + (m * 16 + lane) * 16, kq, cs);
  } else {
    const int ub = u - 576, cgp = ub % 9, sg = ub / 9;
    const int c = 4 * cgp + w;
    const int g = c < 20 ? c / 5 : 4 + (c - 20) / 4;
    const int st = dg_start(g), n = dg_count(g);
    const int q = lane >> 4, qq = (lane >> 2) & 3, pp = lane & 3;
    const unsigned rd = sb + (8 * q + qq) * P_RS + w * 32 + pp * 8;
#pragma unroll
    for (int sl = 0; sl < 4; ++sl) {
      u32x2 lo = lds_tr(rd + sl * 32 * P_RS), hi = lds_tr(rd + (sl * 32 + 4) * P_RS);
      size_t frag = (size_t)st * 144 + (size_t)(4 * sg + sl) * n + (c - st);
      RST4(ws + OFF_WD + frag * 1024 + lane * 16, (u32x4{lo[0], lo[1], hi[0], hi[1]}));
    }
  }
}

constexpr int PB_WQKV = 64, PB_RT = 450, PB_X = 128, PB_WO = 18, PB_Z = 7;
constexpr int PB_TOTAL = PB_WQKV + PB_X + PB_WO + PB_Z + PB_RT;

__global__ __launch_bounds__(512) void k_prep(const unsigned short* __restrict__ x, const unsigned short* __restrict__ wqkv,
                                              const unsigned short* __restrict__ wo, const unsigned short* __restrict__ wg,
                                              const unsigned short* __restrict__ wu, const unsigned short* __restrict__ wd,
                                              char* __restrict__ ws) {
  __shared__ __attribute__((aligned(16))) char smem[2 * 576 * 40];
  int b = blockIdx.x;
  const int tid = threadIdx.x, lane = tid & 63, w8 = tid >> 6;
  {
    unsigned plo, phi_;
    asm volatile("s_getpc_b64 s[40:41]\n\ts_add_u32 s40, s40, _Z8k_layersPcS_@rel32@lo+4\n\ts_addc_u32 s41, s41, _Z8k_layersPcS_@rel32@hi+12\n\t"
                 "s_mov_b32 %0, s40\n\ts_mov_b32 %1, s41" : "=s"(plo), "=s"(phi_) :: "s40", "s41", "scc");
    const unsigned long long pc = ((unsigned long long)phi_ << 32) | plo;
    const int slot = b >> 3;
    if ((slot * 512 + tid) * 16 < 53248) { unsigned v = *(const volatile unsigned*)((const char*)pc + (slot * 512 + tid) * 16); asm volatile("" :: "v"(v)); }
  }
  if (b < PB_WQKV) {
    constexpr int RS = 224;
    const int p = b >> 2, kq = b & 3;
    u32x2 v[8];
#pragma unroll
    for (int i = 0; i < 8; ++i) {
      int ch = tid + 512 * i; if (ch > 3887) ch = 3887;
      int row = ch / 27, rem = ch - 27 * row, m = rem / 9, c4 = rem - 9 * m;
      v[i] = PLD2((const u32x2*)(wqkv + (size_t)(144 * kq + row) * (3 * D) + 576 * m + 36 * p + 4 * c4));
    }
    const int blk = w8 & 3, kh = w8 >> 2, h = lane >> 5, rho = lane & 31;
    int col = 108;
    if (blk == 0) col = 36 + rho;
    else if (blk == 1) {
      if (rho < 18) col = 72 + rho;
      else if (rho >= 19 && rho <= 22) col = 36 + 32 + (rho - 19);
      else if (rho >= 23 && rho <= 26) col = 18 * ((rho - 23) >> 1) + 16 + ((rho - 23) & 1);
    } else if (blk == 2) { if (rho < 18) col = 72 + 18 + rho; }
    else col = 18 * (rho >> 4) + 8 * ((rho >> 2) & 1) + 4 * ((rho >> 3) & 1) + (rho & 3);
#pragma unroll
    for (int i = 0; i < 8; ++i) {
      int ch = tid + 512 * i;
      if (ch < 3888) { int row = ch / 27, rem = ch - 27 * row; *(u32x2*)(smem + row * RS + rem * 8) = v[i]; }
    }
    if (tid < 144) *(u32x2*)(smem + tid * RS + 216) = u32x2{0u, 0u};
    __syncthreads();
    const unsigned short* src = (const unsigned short*)(smem + (80 * kh + 8 * h) * RS) + col;
    char* dst = ws + OFF_WQKV + (size_t)p * KA_NFRAG * 1024 + blk * 1024 + lane * 16;
    float cs = 0.f;
#pragma unroll
    for (int kl = 0; kl < 5; ++kl) {
      if (5 * kh + kl < 9) {
        u32x4 o;
#pragma unroll
        for (int i = 0; i < 4; ++i)
          o[i] = (unsigned)src[(kl * 16 + 2 * i) * (RS / 2)] | ((unsigned)src[(kl * 16 + 2 * i + 1) * (RS / 2)] << 16);
        cs = dot_ones(o[0], cs); cs = dot_ones(o[1], cs); cs = dot_ones(o[2], cs); cs = dot_ones(o[3], cs);
        PST4(dst + (size_t)(9 * kq + 5 * kh + kl) * 4096, o);
      }
    }
    cs += __shfl_xor(cs, 32);
    float* cx = (float*)(smem + 144 * RS);
    if (kh == 1 && lane < 32) cx[blk * 32 + lane] = cs;
    __syncthreads();
    if (kh == 0 && lane < 32) put_colsum(ws + OFF_WQKV + ((size_t)p * KA_NFRAG + 144 + blk) * 1024 + lane * 16, kq, cs + cx[blk * 32 + lane]);
    return;
  }
  b -= PB_WQKV;
  if (b < PB_X) {
    const int ks = lane < 36 ? lane : 35;
    u32x4 v0[2], v1[2];
#pragma unroll
    for (int i = 0; i < 2; ++i) {
      const u32x4* src = (const u32x4*)(x + (size_t)(b * 16 + w8 * 2 + i) * D + 16 * ks);
      v0[i] = PLD4(src); v1[i] = PLD4(src + 1);
    }
#pragma unroll
    for (int i = 0; i < 2; ++i) {
      const int token = b * 16 + w8 * 2 + i, tb = token >> 5, r = token & 31;
      if (lane < 36) {
        char* dst = ws + OFF_XF0 + ((size_t)(tb * 36 + ks) * 64 + r) * 16;
        PST4(dst, v0[i]); PST4(dst + 512, v1[i]);
      }
      float s = 0.f;
#pragma unroll
      for (int j = 0; j < 4; ++j) s += bflo(v0[i][j]) + bfhi(v0[i][j]) + bflo(v1[i][j]) + bfhi(v1[i][j]);
      float m16 = s * (1.f / 16.f), q = 0.f;
#pragma unroll
      for (int j = 0; j < 4; ++j) {
        float a = bflo(v0[i][j]) - m16, c = bfhi(v0[i][j]) - m16, d = bflo(v1[i][j]) - m16, e = bfhi(v1[i][j]) - m16;
        q += a * a + c * c + d * d + e * e;
      }
      int g = lane & 7, st = dg_start(g), n = dg_count(g);
      float ms = 0.f, mm[5], qq = 0.f;
#pragma unroll
      for (int j = 0; j < 5; ++j) {
        int sl = st + (j < n ? j : 0);
        mm[j] = __shfl(m16, sl); float qi = __shfl(q, sl);
        if (j < n) { ms += mm[j]; qq += qi; }
      }
      float mean = ms / n;
#pragma unroll
      for (int j = 0; j < 5; ++j) if (j < n) { float d = mm[j] - mean; qq += 16.f * d * d; }
      if (lane < 8) *(f32x2*)(ws + OFF_ST0 + ((size_t)token * 8 + lane) * 8) = f32x2{mean, qq};
    }
    return;
  }
  b -= PB_X;
  if (b < PB_WO) {
    constexpr int RS = 40;
    const int half = tid >> 8, t = tid & 255, w = w8 & 3, c = 2 * b + half;
    char* sm = smem + half * (576 * RS);
    u32x4 v[5];
#pragma unroll
    for (int i = 0; i < 5; ++i) {
      int ch = t + 256 * i; if (ch > 1151) ch = 1151;
      v[i] = PLD4((const u32x4*)(wo + (size_t)(ch >> 1) * D + 16 * c + 8 * (ch & 1)));
    }
#pragma unroll
    for (int i = 0; i < 5; ++i) {
      int ch = t + 256 * i;
      if (ch < 1152) { char* d = sm + (ch >> 1) * RS + (ch & 1) * 16; *(u32x2*)d = u32x2{v[i][0], v[i][1]}; *(u32x2*)(d + 8) = u32x2{v[i][2], v[i][3]}; }
    }
    __syncthreads();
    const unsigned sb = lds_addr(sm);
    const int q = lane >> 4, qq = (lane >> 2) & 3, pp = lane & 3;
    for (int s = w; s < 18; s += 4) {
      int r0, r1;
      if (s < 16) { r0 = (2 * s + (q >> 1)) * 18 + 8 * (q & 1) + qq; r1 = r0 + 4; }
      else { int w0 = 4 * (s - 16) + (q >> 1); r0 = (4 * w0 + qq) * 18 + 16 + (q & 1); r1 = (4 * (w0 + 2) + qq) * 18 + 16 + (q & 1); }
      u32x2 lo = lds_tr(sb + r0 * RS + pp * 8), hi = lds_tr(sb + r1 * RS + pp * 8);
      PST4(ws + OFF_WO + ((size_t)c * 18 + s) * 1024 + lane * 16, (u32x4{lo[0], lo[1], hi[0], hi[1]}));
    }
    return;
  }
  b -= PB_WO;
  if (b < PB_Z) {
#pragma unroll
    for (int i = 0; i < 4; ++i) {
      size_t o = (((size_t)b * 4 + i) * 512 + tid) * 16;
      if (o < SZ_ZERO) PST4(ws + OFF_KV + o, (u32x4{0, 0, 0, 0}));
    }
    return;
  }
  b -= PB_Z;
  {
    const int half = tid >> 8, t = tid & 255, u = 2 * b + half;
    char* sm = smem + half * (144 * P_RS);
    RtRegs R;
    rt_load(u, t, wg, wu, wd, R);
    rt_stage(u, t, sm, R);
    __syncthreads();
    rt_emit(u, t, sm, ws);
  }
}

DI u32x4 mean_frag(float mean, int h) {
  unsigned mh = pack2(mean, 0.f) & 0xffffu;
  unsigned ml = pack2(mean - bflo(mh), 0.f) & 0xffffu;
  unsigned hh = mh | (mh << 16), lh = ml | (mh << 16), hl = mh | (ml << 16);
  return h == 0 ? u32x4{hh, lh, hl, hh} : u32x4{lh, hl, 0u, 0u};
}

DI float phi2(float a, float rs2) {
  const float t = a * rs2;
  return __builtin_fmaf(fmaxf(t, 0.f), 0.6931471805599453f, __builtin_amdgcn_exp2f(fminf(t, 0.f)) + 1e-6f);
}
#define KA_PRO_ 12
constexpr int KA_PRO = KA_PRO_;
constexpr int KA_RING = 37;
constexpr int KA_PD = 8;
template <int KS>
DI void ka_steps(u32x4 (&X)[37], u32x4 (&A)[KA_PD], f32x16 (&acc)[4], const char* xs, unsigned l16, unsigned rd0, unsigned rd1, float mean, int h) {
  if constexpr (KS == 36) X[36] = mean_frag(mean, h);
  if constexpr (KS % 4 == 0 && KS > 0 && KS <= 32) bar();
  if constexpr (KS < 36) {
    constexpr int issued = KS + KA_PRO < 36 ? KS + KA_PRO : 36;
    waitv_pin<issued - KS - 1>(X[KS]);
    if constexpr (KS + KA_PRO < 36) hload16<0>(X[KS + KA_PRO], xs + (KS + KA_PRO) * 1024, l16);
  }
#pragma unroll
  for (int blk = 0; blk < 4; ++blk) {
    const int i = 4 * KS + blk;
    u32x4 a = A[i % KA_PD];
    if (blk < 3) acc[blk] = mfma32(X[KS], a, acc[blk]);
    else acc[3] = mfma32(a, X[KS], acc[3]);
    if (i + KA_PD < KA_NFRAG) { const int f = (i + KA_PD) % (4 * KA_RING); A[i % KA_PD] = lds_rd((f < 64 ? rd0 : (f < 128 ? rd1 : rd1 + 65536)) + (f & 63) * 1024); }
    __builtin_amdgcn_sched_barrier(0);
  }
  if constexpr (KS + 1 < 37) ka_steps<KS + 1>(X, A, acc, xs, l16, rd0, rd1, mean, h);
}
#define KA_PRE_ 12
constexpr int KA_PRE = KA_PRE_;
constexpr int ka_to(int G) { int t = G == 0 ? KA_PRE : 4 * G + 16; if (G > 0 && t > 4 * (G - 1) + KA_RING) t = 4 * (G - 1) + KA_RING; return t < 37 ? t : 37; }
struct KaFoTable { short v[256][4]; };
constexpr KaFoTable ka_make_fo() {
  KaFoTable T{};
  for (int t = 0; t < 256; ++t)
    for (int i = 0; i < 4; ++i) {
      const int it = t + 256 * i;
      int val = -1;
      if (it < 684) {
        const int hs = it >= 342, sl = it - 342 * hs, e = sl / 18, d = sl - 18 * e;
        val = hs == 0 ? d * 33 + e : (d < 14 ? (18 + d) * 33 + e : (32 + d - 14) * 33 + e);
      }
      T.v[t][i] = (short)val;
    }
  return T;
}
__device__ __attribute__((aligned(16))) const KaFoTable KA_FO = ka_make_fo();
template <int G, int GEND> DI void ka_loader(const char* wsrc, unsigned l16, unsigned dst) {
  constexpr int need = 4 * G + 7 < 36 ? 4 * G + 7 : 36;
  constexpr int from = G == 0 ? KA_PRE : ka_to(G - 1);
  constexpr int to = ka_to(G);
  static_assert(to > need && to >= from, "loader schedule");
#pragma unroll
  for (int ks = from; ks < to; ++ks) glds16(wsrc + ks * 4096, l16, dst + (4 * (ks % KA_RING)) * 1024);
  waitv<to - 1 - need>();
  bar();
  if constexpr (G < GEND) ka_loader<G + 1, GEND>(wsrc, l16, dst);
}

DI void ka_phase(char* smem, char* ws, const int lay_) {
  const char* xf = ws + (lay_ ? OFF_XF2 : OFF_XF0);
  const char* st = ws + (lay_ ? OFF_ST2 : OFF_ST0);
  const char* wqkvf = ws + OFF_WQKV;
  char* pqf = ws + OFF_PQF + lay_ * SZ_PQF;
  char* pql = ws + OFF_PQL + lay_ * SZ_PQL;
  double* kv = (double*)(ws + OFF_KV) + (size_t)lay_ * KV_DBL;
  const int tid = tid_opaque(), lane = tid & 63;
  const int w8 = __builtin_amdgcn_readfirstlane(tid >> 6);
  const int w = w8 & 3;
  const int b = blockIdx.x, xcd = b & 7, idx = b >> 3;
  const int tt = 2 * xcd + (idx & 1), p = idx >> 1;
  const int tb = 4 * tt + w;
  const unsigned sb = lds_addr(smem);
  const unsigned l16 = lane * 16;
  const int h = lane >> 5, r = lane & 31;
  STAMP(1, 0);
  if (w8 >= 4) {
    const char* wsrc = wqkvf + (size_t)p * KA_NFRAG * 1024 + w * 1024;
    u32x2 fot;
    gload8<0>(fot, &KA_FO, (tid - 256) * 8);
#pragma unroll
    for (int ks = 0; ks < KA_PRE; ++ks) glds16(wsrc + ks * 4096, l16, sb + w * 1024 + (4 * ks) * 1024);
    if (lay_) dep_wait(ws, SY_D + tt, 1, 16);
    ka_loader<0, 8>(wsrc, l16, sb + w * 1024);
    asm volatile("" : "+v"(fot));
    int fo[3] = {(short)(fot[0] & 0xffff), (short)(fot[0] >> 16), (short)(fot[1] & 0xffff)};
    bar();
    bar();
    float sum[3];
#pragma unroll
    for (int i = 0; i < 3; ++i) {
      const float* rp = (const float*)smem + (fo[i] >= 0 ? fo[i] : 0);
      sum[i] = (rp[0] + rp[1280]) + (rp[2560] + rp[3840]);
    }
#pragma unroll
    for (int i = 0; i < 3; ++i)
      if (fo[i] >= 0) unsafeAtomicAdd(kv + (size_t)(2 * p) * 342 + (tid - 256) + 256 * i, (double)sum[i]);
  } else {
    u32x4 sv[4], X[37];
    const char* xs = xf + (size_t)tb * 36 * 1024;
    if (lay_) dep_wait(ws, SY_D + tt, 1, 16);
    hload16<0>(X[0], xs, l16); hload16<0>(X[1], xs + 1024, l16);
    {
      const char* sp = st + (size_t)(tb * 32) * 64;
      unsigned so = r * 64;
      hload16<0>(sv[0], sp, so); hload16<16>(sv[1], sp, so); hload16<32>(sv[2], sp, so); hload16<48>(sv[3], sp, so);
    }
#pragma unroll
    for (int k = 2; k < KA_PRO; ++k) hload16<0>(X[k], xs + k * 1024, l16);
    STAMP(1, 1);
    float rstd, mean;
    {
      asm volatile("s_waitcnt vmcnt(%4)" : "+v"(sv[0]), "+v"(sv[1]), "+v"(sv[2]), "+v"(sv[3]) : "n"(KA_PRO - 2));
      float m[8], q[8];
#pragma unroll
      for (int i = 0; i < 4; ++i) {
        m[2 * i] = asf(sv[i][0]); q[2 * i] = asf(sv[i][1]);
        m[2 * i + 1] = asf(sv[i][2]); q[2 * i + 1] = asf(sv[i][3]);
      }
      mean = (80.f * (m[0] + m[1] + m[2] + m[3]) + 64.f * (m[4] + m[5] + m[6] + m[7])) * (1.f / 576.f);
      float M2 = 0.f;
#pragma unroll
      for (int i = 0; i < 8; ++i) { float d = m[i] - mean; M2 += q[i] + (i < 4 ? 80.f : 64.f) * d * d; }
      rstd = rsqrtf(M2 * (1.f / 576.f) + 1e-5f);
    }
    f32x16 acc[4];
#pragma unroll
    for (int i = 0; i < 4; ++i)
#pragma unroll
      for (int j = 0; j < 16; ++j) acc[i][j] = 0.f;
    unsigned rd0 = sb + l16, rd1 = rd0 + 65536;
    asm volatile("" : "+v"(rd0), "+v"(rd1));
    bar();
    STAMP(1, 2);
    u32x4 A[KA_PD];
#pragma unroll
    for (int i = 0; i < KA_PD; ++i) A[i] = lds_rd(rd0 + i * 1024);
    __builtin_amdgcn_sched_barrier(0);
    ka_steps<0>(X, A, acc, xs, l16, rd0, rd1, mean, h);
    STAMP(1, 3);
    bar();
    STAMP(1, 6);

    const int c = r;
    float* rs = (float*)(smem + 32768) + w * 160;
    float* ql = rs + 32;
    if (h == 0) rs[r] = rstd;
    f32x4 rr[4];
#pragma unroll
    for (int g = 0; g < 4; ++g) rr[g] = *(const f32x4*)(rs + 8 * g + 4 * h);
    u32x4 k1[2], v1[2], v2[2];
    f32x4 pv[4];
    const bool ph = c >= 19 && c <= 26, one = c == 18;
#pragma unroll
    for (int s = 0; s < 2; ++s)
#pragma unroll
      for (int t = 0; t < 4; ++t) {
        const int r0 = 8 * s + 2 * t;
        const float s0 = rr[r0 >> 2][r0 & 3], s1 = rr[r0 >> 2][(r0 & 3) + 1];
        const float e0 = s0 * 1.4426950408889634f, e1 = s1 * 1.4426950408889634f;
        k1[s][t] = pack2(phi2(acc[0][r0], e0), phi2(acc[0][r0 + 1], e1));
        float a0 = acc[1][r0] * s0, a1 = acc[1][r0 + 1] * s1;
        float p0 = phi2(acc[1][r0], e0), p1 = phi2(acc[1][r0 + 1], e1);
        a0 = ph ? p0 : (one ? 1.f : a0); a1 = ph ? p1 : (one ? 1.f : a1);
        pv[r0 >> 2][r0 & 3] = p0; pv[r0 >> 2][(r0 & 3) + 1] = p1;
        v1[s][t] = pack2(a0, a1);
        v2[s][t] = one ? 0x3f803f80u : pack2(acc[2][r0] * s0, acc[2][r0 + 1] * s1);
      }
    if (c >= 23 && c <= 26) {
#pragma unroll
      for (int g = 0; g < 4; ++g) *(f32x4*)(ql + (c - 23) * 32 + 8 * g + 4 * h) = pv[g];
    }
    STAMP(1, 7);
    f32x16 R1, R2, R3;
#pragma unroll
    for (int j = 0; j < 16; ++j) { R1[j] = 0.f; R2[j] = 0.f; R3[j] = 0.f; }
#pragma unroll
    for (int s = 0; s < 2; ++s) {
      R1 = mfma32(k1[s], v1[s], R1);
      R2 = mfma32(k1[s], v2[s], R2);
      R3 = mfma32(v1[s], v2[s], R3);
    }
    float* red = (float*)smem + w * 1280;
#pragma unroll
    for (int j = 0; j < 16; ++j) {
      int row = (j & 3) + 8 * (j >> 2) + 4 * h;
      red[row * 33 + c] = row < 18 ? R1[j] : R2[j];
    }
    if (h == 0) red[32 * 33 + c] = R3[11];
    else { red[33 * 33 + c] = R3[8]; red[34 * 33 + c] = R3[9]; red[35 * 33 + c] = R3[10]; }
    STAMP(1, 4);
    asm volatile("s_waitcnt lgkmcnt(0)" ::: "memory");
    bar();
    {
      u32x4 qa, qb;
      const float rse = rstd * 1.4426950408889634f;
#pragma unroll
      for (int t = 0; t < 4; ++t) {
        qa[t] = pack2(phi2(acc[3][2 * t], rse), phi2(acc[3][2 * t + 1], rse));
        qb[t] = pack2(phi2(acc[3][8 + 2 * t], rse), phi2(acc[3][8 + 2 * t + 1], rse));
      }
      char* dst = pqf + ((size_t)tb * 32 + 2 * p) * 1024;
      hstore16(dst, l16, qa); hstore16(dst + 1024, l16, qb);
      float la = ql[h * 32 + r], lb = ql[(2 + h) * 32 + r];
      hstore4(pql + ((size_t)tb * 4 + (p >> 2)) * 1024 + 4 * (p & 3), l16, pack2(la, lb));
    }
  }
  STAMP(1, 8);
  sig_done(ws, SY_KA + 8 * KA_REP * lay_ + xcd, KA_REP, 8);
  STAMP(1, 9);
}

constexpr int KB_LDS = 36 * 1024 + 16 * 1024 + 9 * 32 * 8;
constexpr int KB_KVI = 57344;
struct KbTable { unsigned short v[64][16]; };
constexpr KbTable kb_make_table() {
  KbTable T{};
  for (int l = 0; l < 64; ++l)
    for (int k = 0; k < 16; ++k) {
      const int q = l + 64 * k;
      int val = 4000;
      if (q < 684) { const int hd = q / 171, r = q - 171 * hd, e = r / 9, d2 = r - 9 * e; val = hd * 912 + e * 48 + d2 * 4; }
      T.v[l][k] = (unsigned short)val;
    }
  return T;
}
__device__ __attribute__((aligned(16))) const KbTable KB_TAB = kb_make_table();
DI void kb_phase(char* smem, char* ws, const int lay_) {
  const char* pqf = ws + OFF_PQF + lay_ * SZ_PQF;
  const char* pql = ws + OFF_PQL + lay_ * SZ_PQL;
  const double* kv = (const double*)(ws + OFF_KV) + (size_t)lay_ * KV_DBL;
  const char* wof = ws + OFF_WO;
  const char* xin = ws + (lay_ ? OFF_XF2 : OFF_XF0);
  char* xout = ws + OFF_XF1 + lay_ * SZ_XF;
  char* stout = ws + OFF_ST1 + lay_ * SZ_ST1;
  const int tid = tid_opaque(), lane = tid & 63;
  const int w = __builtin_amdgcn_readfirstlane(tid >> 6);
  const int b = blockIdx.x, xcd = b & 7, idx = b >> 3;
  const int tb = 8 * xcd + (idx & 7), cg = idx >> 3;
  const int ct = 9 * cg + w, ct8 = 9 * cg + 8;
  const int h = lane >> 5, r = lane & 31, q = lane >> 4, t = lane & 15;

  STAMP(2, 0);
  u32x4 A[18], A8[3];
  const char* wp = wof + (size_t)ct * 18 * 1024;
  const char* wp8 = wof + ((size_t)ct8 * 18 + w) * 1024;
  const unsigned l16 = lane * 16;
  u32x2 res[2], res8;
  const unsigned xq = ((q >> 1) * 32 + t) * 16 + (q & 1) * 8;
  const unsigned xo = (tb * 36 + ct) * 1024 + xq;
  const unsigned xo8 = (tb * 36 + ct8) * 1024 + xq + (w & 1) * 256;
#define KB_LDA(s_) gload16<((s_) & 3) * 1024>(A[s_], wp + ((s_) >> 2) * 4096, l16)
  u32x4 tab[2];
  gload16<0>(tab[0], &KB_TAB, lane * 32); gload16<16>(tab[1], &KB_TAB, lane * 32);
  gload16<0>(A8[0], wp8, l16); gload16<0>(A8[1], wp8 + 8192, l16); gload16<0>(A8[2], wp8 + (w < 2 ? 16384 : 0), l16);
  KB_LDA(0); KB_LDA(1); KB_LDA(2); KB_LDA(3); KB_LDA(4); KB_LDA(5); KB_LDA(6); KB_LDA(7); KB_LDA(8);
  KB_LDA(9); KB_LDA(10); KB_LDA(11); KB_LDA(12); KB_LDA(13); KB_LDA(14); KB_LDA(15); KB_LDA(16); KB_LDA(17);
  STAMP(2, 6);
  dep_wait(ws, SY_KA + 8 * KA_REP * lay_ + 8 * (idx & (KA_REP - 1)), 8, 32);
  STAMP(2, 7);

  u32x4 kd[11];
  u32x4 bq[4], bl;
  {
    const char* src = (const char*)(kv + (size_t)(4 * w) * 342);
#pragma unroll
    for (int k = 0; k < 10; ++k) hload16<0>(kd[k], src + k * 1024, l16);
    hload16<0>(kd[10], src + 10240, l16);
  }
  hload16<0>(bl, pql + ((size_t)tb * 4 + (w >> 1)) * 1024, l16);
#pragma unroll
  for (int i = 0; i < 4; ++i) hload16<0>(bq[i], pqf + ((size_t)tb * 32 + 4 * w + i) * 1024, l16);
  hload8<0>(res[0], xin, xo); hload8<256>(res[1], xin, xo); hload8<0>(res8, xin, xo8);
  STAMP(2, 1);
  char* kvi = smem + KB_KVI + w * 4096;
  asm volatile("s_waitcnt vmcnt(8)" : "+v"(kd[0]), "+v"(kd[1]), "+v"(kd[2]), "+v"(kd[3]), "+v"(kd[4]), "+v"(kd[5]), "+v"(kd[6]), "+v"(kd[7]),
               "+v"(kd[8]), "+v"(kd[9]), "+v"(kd[10]), "+v"(tab[0]), "+v"(tab[1]));
#pragma unroll
  for (int k = 0; k < 11; ++k) {
    const double d0 = __builtin_bit_cast(double, u32x2{kd[k][0], kd[k][1]}), d1 = __builtin_bit_cast(double, u32x2{kd[k][2], kd[k][3]});
    const unsigned tw = tab[k >> 3][(k >> 1) & 3];
    const unsigned off = (k & 1) ? (tw >> 16) : (tw & 0xffffu);
    *(unsigned*)(kvi + off) = pack2((float)d0, (float)d1);
  }
  const bool valid = r < 18 || r == 20 || r == 21;
  const int er = r < 16 ? 8 * ((r >> 2) & 1) + 4 * (r >> 3) + (r & 3) : (r == 16 ? 16 : (r == 20 ? 17 : (valid ? 18 : 0)));
  asm volatile("s_waitcnt vmcnt(3)" : "+v"(bl), "+v"(bq[0]), "+v"(bq[1]), "+v"(bq[2]), "+v"(bq[3]));
  float lo[4];
#pragma unroll
  for (int i = 0; i < 4; ++i) {
    const char* row = kvi + i * 912 + er * 48;
    u32x4 a1 = *(const u32x4*)(row + 16 * h), a2;
    const unsigned l16v = *(const unsigned short*)(row + 32 + 2 * h);
    if (!valid) a1 = u32x4{0u, 0u, 0u, 0u};
    const unsigned lv = valid ? ((i & 1) ? (l16v << 16) : l16v) : 0u;
    const int dw = 2 * (w & 1) + (i >> 1);
#pragma unroll
    for (int k = 0; k < 4; ++k) a2[k] = (k == dw) ? lv : 0u;
    f32x16 n;
#pragma unroll
    for (int j = 0; j < 16; ++j) n[j] = 0.f;
    n = mfma32(a1, bq[i], n);
    n = mfma32(a2, bl, n);
    float rd = __builtin_amdgcn_rcpf(n[9] + 1e-6f);
    u32x4 o;
#pragma unroll
    for (int k = 0; k < 4; ++k) o[k] = pack2(n[2 * k] * rd, n[2 * k + 1] * rd);
    lo[i] = n[8] * rd;
    const int head = 4 * w + i, s = head >> 1, qq = 2 * (head & 1) + h;
    *(u32x4*)(smem + (((r >> 4) * 18 + s) * 64 + qq * 16 + (r & 15)) * 16) = o;
  }
  {
    const int s = 16 + (w >> 2), qq = 2 * (w & 1) + h, half = (w >> 1) & 1;
    *(u32x2*)(smem + (((r >> 4) * 18 + s) * 64 + qq * 16 + (r & 15)) * 16 + half * 8) = u32x2{pack2(lo[0], lo[1]), pack2(lo[2], lo[3])};
  }
  STAMP(2, 2);
  asm volatile("s_waitcnt lgkmcnt(0)" ::: "memory");
  bar();
  STAMP(2, 3);
  {
    asm volatile("" : "+v"(A8[0]), "+v"(A8[1]), "+v"(A8[2]));
    f32x4 p8[2] = {{0.f, 0.f, 0.f, 0.f}, {0.f, 0.f, 0.f, 0.f}};
#pragma unroll
    for (int j = 0; j < 3; ++j) {
      const int s = w + 8 * j;
      if (j < 2 || w < 2) {
        u32x4 b0 = *(const u32x4*)(smem + (s * 64 + lane) * 16);
        u32x4 b1 = *(const u32x4*)(smem + ((18 + s) * 64 + lane) * 16);
        p8[0] = mfma16(A8[j], b0, p8[0]);
        p8[1] = mfma16(A8[j], b1, p8[1]);
      }
    }
    *(f32x4*)(smem + 36 * 1024 + ((w * 2 + 0) * 64 + lane) * 16) = p8[0];
    *(f32x4*)(smem + 36 * 1024 + ((w * 2 + 1) * 64 + lane) * 16) = p8[1];
  }
  f32x4 acc[2][2] = {{{0.f, 0.f, 0.f, 0.f}, {0.f, 0.f, 0.f, 0.f}}, {{0.f, 0.f, 0.f, 0.f}, {0.f, 0.f, 0.f, 0.f}}};
  {
    u32x4 b0[18], b1[18];
#pragma unroll
    for (int s = 0; s < 18; ++s) {
      b0[s] = *(const u32x4*)(smem + (s * 64 + lane) * 16);
      b1[s] = *(const u32x4*)(smem + ((18 + s) * 64 + lane) * 16);
    }
    __builtin_amdgcn_sched_barrier(0);
#pragma unroll
    for (int s = 0; s < 18; ++s) pin(A[s]);
#pragma unroll
    for (int s = 0; s < 18; ++s) {
      acc[0][s & 1] = mfma16(A[s], b0[s], acc[0][s & 1]);
      acc[1][s & 1] = mfma16(A[s], b1[s], acc[1][s & 1]);
    }
#pragma unroll
    for (int j = 0; j < 4; ++j) { acc[0][0][j] += acc[0][1][j]; acc[1][0][j] += acc[1][1][j]; }
  }
  STAMP(2, 4);
  asm volatile("s_waitcnt vmcnt(0)" : "+v"(res[0]), "+v"(res[1]), "+v"(res8));
  f32x2* sred = (f32x2*)(smem + 52 * 1024);
  auto finish = [&](const f32x4& a, u32x2 rs, unsigned off) -> f32x2 {
    float v0 = bflo(rs[0]) + a[0], v1 = bfhi(rs[0]) + a[1], v2 = bflo(rs[1]) + a[2], v3 = bfhi(rs[1]) + a[3];
    u32x2 o = {pack2(v0, v1), pack2(v2, v3)};
    hstore8(xout, off, o);
    v0 = bflo(o[0]); v1 = bfhi(o[0]); v2 = bflo(o[1]); v3 = bfhi(o[1]);
    float s = sum_x32(sum_x16((v0 + v1) + (v2 + v3)));
    float m = s * (1.f / 16.f);
    float d0 = v0 - m, d1 = v1 - m, d2 = v2 - m, d3 = v3 - m;
    float qq = sum_x32(sum_x16(d0 * d0 + d1 * d1 + d2 * d2 + d3 * d3));
    return f32x2{m, qq};
  };
  {
    const f32x2 s0 = finish(acc[0][0], res[0], xo), s1 = finish(acc[1][0], res[1], xo + 256);
    if (q == 0) { sred[w * 32 + t] = s0; sred[w * 32 + 16 + t] = s1; }
  }
  asm volatile("s_waitcnt lgkmcnt(0)" ::: "memory");
  bar();
  if (w < 2) {
    f32x4 a = {0.f, 0.f, 0.f, 0.f};
#pragma unroll
    for (int i = 0; i < 8; ++i) {
      f32x4 pv = *(const f32x4*)(smem + 36 * 1024 + ((i * 2 + w) * 64 + lane) * 16);
      a[0] += pv[0]; a[1] += pv[1]; a[2] += pv[2]; a[3] += pv[3];
    }
    f32x2 pp[8];
#pragma unroll
    for (int i = 0; i < 8; ++i) pp[i] = sred[i * 32 + 16 * w + t];
    const f32x2 s8 = finish(a, res8, xo8);
    float ms = s8[0], qq = s8[1];
#pragma unroll
    for (int i = 0; i < 8; ++i) { ms += pp[i][0]; qq += pp[i][1]; }
    const float mean = ms * (1.f / 9.f);
    { float d = s8[0] - mean; qq += 16.f * d * d; }
#pragma unroll
    for (int i = 0; i < 8; ++i) { float d = pp[i][0] - mean; qq += 16.f * d * d; }
    if (q == 0) hstore8(stout, ((tb * 32 + 16 * w + t) * 4 + cg) * 8, u32x2{__builtin_bit_cast(unsigned, mean), __builtin_bit_cast(unsigned, qq)});
  }
  STAMP(2, 5);
  sig_done(ws, SY_KB + 8 * lay_ + xcd);
  STAMP(2, 9);
}

#define GU_STAGE_ 32
#define GU_RING_ 128
constexpr int GU_STAGE = GU_STAGE_, GU_RING = GU_RING_, GU_P1 = 111;
constexpr int GU_PD = 5;
constexpr int GU_NSLOT = GU_RING / GU_STAGE, GU_NPIECE = (GU_NFRAG + 7) / 8;
constexpr int GU_NST = (GU_NFRAG + GU_STAGE - 1) / GU_STAGE;
struct GuSched { int n; int kind[96]; int arg[96]; int step[96]; };
#define GU_PRO_X_ 10
#define GU_PRO_P_ 12
#define GU_PACE 5
#define GU_LAG_ 1
constexpr int GU_PRO_X = GU_PRO_X_;
constexpr int GU_PRO_P = GU_PRO_P_;
constexpr int gu_pstage(int p) { return 8 * p / GU_STAGE; }
constexpr int gu_pdead(int p) { int st = gu_pstage(p); return st == 0 ? 0 : st * GU_STAGE - GU_PD; }
constexpr int gu_pearly(int p) { int st = gu_pstage(p); return st < GU_NSLOT ? 0 : (st - GU_NSLOT + 2) * GU_STAGE - GU_PD + 2; }
constexpr GuSched gu_make_sched() {
  GuSched s{};
  int n = 0;
  auto push = [&](int k, int a, int st) { s.kind[n] = k; s.arg[n] = a; s.step[n] = st; ++n; };
  int kx = 0, pc = 0;
  while (pc < GU_PRO_P) push(1, pc++, -2);
  push(2, 0, -1); push(2, 1, -1);
  while (kx < GU_PRO_X) push(0, kx++, -1);
  int m = 0, last = 0;
  while (kx < 36 || pc < GU_NPIECE) {
    int dx = kx < 36 ? 3 * kx : 100000, ds = pc < GU_NPIECE ? gu_pdead(pc) : 100000;
    bool takep = ds <= dx;
    int st = kx < 36 ? (m * GU_PACE) / 2 : last + 5;
    if (takep && st < gu_pearly(pc)) { if (kx < 36) { takep = false; } else st = gu_pearly(pc); }
    if (st < last) st = last;
    if (takep) push(1, pc++, st); else push(0, kx++, st);
    last = st; ++m;
  }
  s.n = n;
  return s;
}
constexpr GuSched GUS = gu_make_sched();
constexpr int GU_LAG = GU_LAG_;
constexpr int gu_cnt_raw(int step) { int c = 0; for (int i = 0; i < GUS.n; ++i) if (GUS.step[i] < step) ++c; return c; }
constexpr int gu_cnt_before(int step) { return gu_cnt_raw(step - GU_LAG < 0 ? 0 : step - GU_LAG); }
constexpr int gu_idx(int kind, int arg) { for (int i = 0; i < GUS.n; ++i) if (GUS.kind[i] == kind && GUS.arg[i] == arg) return i; return -1; }
constexpr int gu_clamp(int n) { return n > 63 ? 63 : (n < 0 ? 0 : n); }
constexpr int gu_wait_for(int idx, int step) { return gu_clamp(gu_cnt_before(step) - idx - 1); }
constexpr int gu_last_piece(int st) { int p = (st + 1) * GU_STAGE / 8 - 1; return gu_idx(1, p < GU_NPIECE ? p : GU_NPIECE - 1); }
constexpr bool gu_sched_ok() {
  for (int i = 0; i < GUS.n; ++i) {
    if (GUS.kind[i] == 1) { if (GUS.step[i] + GU_LAG >= gu_pdead(GUS.arg[i]) && gu_pdead(GUS.arg[i]) > 0) return false;
                            if (gu_pdead(GUS.arg[i]) == 0 && GUS.step[i] >= 0) return false;
                            if (gu_pearly(GUS.arg[i]) > 0 && GUS.step[i] < gu_pearly(GUS.arg[i]) - 2) return false; }
    if (GUS.kind[i] == 0 && GUS.step[i] >= 0 && GUS.step[i] + GU_LAG >= 3 * GUS.arg[i]) return false;
    if (i > 0 && GUS.step[i] < GUS.step[i - 1]) return false;
  }
  return true;
}
static_assert(gu_sched_ok(), "GU schedule");
static_assert(GU_PRO_P >= GU_STAGE / 8, "stage 0 must be requested in the prologue");

struct GuCtx {
  const char* wsrc; const char* xs; const char* sp; unsigned l16, sb, so; int w, grp, h; unsigned rd0, rd1, rd2; float c1, c3, cinv; char* hbase; unsigned hvoff; int fb0;
};
template <int N, int G> DI void gu_issue(const GuCtx& c, u32x4 (&X)[37], u32x4 (&sv)[2]) {
  constexpr int kind = GUS.kind[N], arg = GUS.arg[N];
  if constexpr (kind == 0) {
    if constexpr (G < 0) hload16<0>(X[arg], c.xs + arg * 1024, c.l16);
    else hload16_g<G>(X[arg], c.xs + arg * 1024, c.l16, c.grp);
  }
  if constexpr (kind == 1) {
    constexpr int piece = arg * 8;
    if constexpr (G < 0) glds16(c.wsrc + piece * 1024, c.l16, c.sb + (piece % GU_RING) * 1024 + c.w * 1024);
    else glds16_g<G>(c.wsrc + piece * 1024, c.l16, c.sb + (piece % GU_RING) * 1024 + c.w * 1024, c.grp);
  }
  if constexpr (kind == 2) hload16<16 * arg>(sv[arg], c.sp, c.so);
}
template <int N, int STEP, int G> DI void gu_issue_step(const GuCtx& c, u32x4 (&X)[37], u32x4 (&sv)[2]) {
  if constexpr (N < GUS.n) {
    if constexpr (GUS.step[N] == STEP) gu_issue<N, G>(c, X, sv);
    if constexpr (GUS.step[N] <= STEP) gu_issue_step<N + 1, STEP, G>(c, X, sv);
  }
}
constexpr int gu_epi_start(int j) { int a = GU_P1 + 2 + 24 * j, e = (j < 3 ? GU_P1 : GU_P1 + 37 * (j - 2)) + 2; return a > e ? a : e; }
constexpr int gu_epi_block(int I) { for (int j = 0; j < 8; ++j) if (I >= gu_epi_start(j) && I < gu_epi_start(j) + 24) return j; return -1; }
struct GuEpi { float x, ab; unsigned pk[4]; float olo; };
template <int E> DI void gu_epi_0(const f32x16& a, const GuCtx& c, GuEpi& e) {
  float g = a[E];
  e.x = g * __builtin_fmaf(g * g, c.c3, c.c1);
}
template <int E> DI void gu_epi_1(const f32x16& a, const GuCtx& c, GuEpi& e) {
  float ex = __builtin_amdgcn_exp2f(e.x);
  e.ab = a[E] * a[E + 8];
  e.x = __builtin_fmaf(ex, c.cinv, c.cinv);
}
template <int E> DI void gu_epi_2(const GuCtx& c, GuEpi& e, int fb) {
  float o = e.ab * __builtin_amdgcn_rcpf(e.x);
  if constexpr ((E & 1) == 0) e.olo = o; else e.pk[E >> 1] = pack2(e.olo, o);
  if constexpr (E == 7) hstore16(c.hbase + (size_t)(fb >> 1) * 4096 + (fb & 1) * 512, c.hvoff, u32x4{e.pk[0], e.pk[1], e.pk[2], e.pk[3]});
}
template <int I>
DI void gu_steps(GuCtx& c, u32x4 (&A)[GU_PD], u32x4 (&X)[37], u32x4 (&sv)[2], f32x16 (&acc)[4], GuEpi& ep) {
  if constexpr ((I + GU_PD) % GU_STAGE == 0 && (I + GU_PD) / GU_STAGE < GU_NST) {
    constexpr int s = (I + GU_PD) / GU_STAGE;
    waitv<gu_wait_for(gu_last_piece(s), I)>();
    bar();
  }
  constexpr int blk = I < GU_P1 ? I % 3 : 3 + (I - GU_P1) / 37;
  constexpr int ks = I < GU_P1 ? I / 3 : (I - GU_P1) % 37;
  constexpr int slot = blk & 3;
  if constexpr (I < 108 && I % 3 == 0 && ks >= 2) waitv_pin<gu_wait_for(gu_idx(0, ks), I)>(X[ks]);
  gu_issue_step<gu_cnt_raw(I), I, 0>(c, X, sv);
  if constexpr (I >= GU_LAG) gu_issue_step<gu_cnt_raw(I - GU_LAG), I - GU_LAG, 1>(c, X, sv);
  if constexpr (I >= GU_P1 && ks == 0) {
    f32x16 z;
#pragma unroll
    for (int j = 0; j < 16; ++j) z[j] = 0.f;
    acc[slot] = mfma32(A[I % GU_PD], X[ks], z);
  } else {
    acc[slot] = mfma32(A[I % GU_PD], X[ks], acc[slot]);
  }
  if constexpr (I + GU_PD < GU_NFRAG) {
    constexpr int fr = (I + GU_PD) % GU_RING;
    A[I % GU_PD] = lds_rd((fr < 64 ? c.rd0 : (fr < 128 ? c.rd1 : c.rd2)) + (fr & 63) * 1024);
  }
  __builtin_amdgcn_sched_barrier(0);
  {
    constexpr int j = gu_epi_block(I);
    if constexpr (j >= 0) {
      constexpr int d = I - gu_epi_start(j);
      if constexpr (d % 3 == 0) gu_epi_0<d / 3>(acc[j & 3], c, ep);
      if constexpr (d % 3 == 1) gu_epi_1<d / 3>(acc[j & 3], c, ep);
      if constexpr (d % 3 == 2) gu_epi_2<d / 3>(c, ep, c.fb0 + j);
    }
  }
  __builtin_amdgcn_sched_barrier(0);
  if constexpr (I + 1 < GU_NFRAG) gu_steps<I + 1>(c, A, X, sv, acc, ep);
}

DI void gu_phase(char* smem, char* ws, const int lay_) {
  const char* xf = ws + OFF_XF1 + lay_ * SZ_XF;
  const char* st = ws + OFF_ST1 + lay_ * SZ_ST1;
  const char* wguf = ws + OFF_WGU;
  char* hf = ws + OFF_HF + lay_ * SZ_HF;
  const int tid = tid_opaque(), lane = tid & 63;
  const int w = __builtin_amdgcn_readfirstlane(tid >> 6);
  const int b = blockIdx.x, xcd = b & 7, idx = b >> 3;
  const int tt = idx & 7, fs = 4 * xcd + (idx >> 3);
  const int tb32 = 8 * tt + w;
  const unsigned sb = lds_addr(smem), l16 = lane * 16;
  const int h = lane >> 5, r = lane & 31;

  STAMP(3, 0);
  GuCtx c;
  c.wsrc = wguf + (size_t)fs * GU_NFRAG * 1024 + w * 1024;
  c.xs = xf + (size_t)tb32 * 36 * 1024;
  c.sp = st + (size_t)(tb32 * 32) * 32; c.so = r * 32;
  c.l16 = l16; c.sb = sb; c.w = w; c.grp = w >> 2; c.h = h; c.fb0 = 9 * fs;
  c.c1 = 0.f; c.c3 = 0.f; c.cinv = 0.f;
  u32x4 sv[2], X[37];
  gu_issue_step<0, -2, -1>(c, X, sv);
  STAMP(3, 8);
  dep_wait(ws, SY_KB + 8 * lay_ + tt, 1, 32);
  STAMP(3, 9);
  gu_issue_step<gu_cnt_raw(-1), -1, -1>(c, X, sv);
#pragma unroll
  for (int k = GU_PRO_X; k < 36; ++k) asm volatile("" : "=v"(X[k]));
  STAMP(3, 1);
  f32x16 acc[4];
#pragma unroll
  for (int i = 0; i < 3; ++i)
#pragma unroll
    for (int j = 0; j < 16; ++j) acc[i][j] = 0.f;
  {
    asm volatile("s_waitcnt vmcnt(%2)" : "+v"(sv[0]), "+v"(sv[1]) : "n"(gu_wait_for(gu_idx(2, 1), 0)));
    float m0 = asf(sv[0][0]), q0 = asf(sv[0][1]), m1 = asf(sv[0][2]), q1 = asf(sv[0][3]);
    float m2 = asf(sv[1][0]), q2 = asf(sv[1][1]), m3 = asf(sv[1][2]), q3 = asf(sv[1][3]);
    float mean = 0.25f * ((m0 + m1) + (m2 + m3));
    float d0 = m0 - mean, d1 = m1 - mean, d2 = m2 - mean, d3 = m3 - mean;
    float M2 = (q0 + q1) + (q2 + q3) + 144.f * (d0 * d0 + d1 * d1 + d2 * d2 + d3 * d3);
    c.cinv = M2 * (1.f / 576.f) + 1e-5f;
    float rstd = rsqrtf(c.cinv);
    c.c1 = -2.3022081985f * rstd;
    c.c3 = -0.1029432397f * rstd * rstd * rstd;
    X[36] = mean_frag(mean, h);
  }
  waitv<gu_wait_for(gu_last_piece(0) > gu_idx(0, 1) ? gu_last_piece(0) : gu_idx(0, 1), 0)>();
  pin(X[0]); pin(X[1]);
  bar();
  STAMP(3, 2);
  c.rd0 = sb + l16; c.rd1 = sb + l16 + 65536; c.rd2 = sb + l16 + 131072;
  asm volatile("" : "+v"(c.rd0), "+v"(c.rd1));
  if (GU_RING > 128) asm volatile("" : "+v"(c.rd2));
  c.hbase = hf + (size_t)(tb32 >> 1) * 144 * 4096;
  c.hvoff = (2 * (tb32 & 1) + (r >> 4)) * 1024 + h * 256 + (r & 15) * 16;
  u32x4 A[GU_PD];
#pragma unroll
  for (int i = 0; i < GU_PD; ++i) A[i] = lds_rd(c.rd0 + i * 1024);
  GuEpi ep;
  __builtin_amdgcn_sched_barrier(0);
  gu_steps<0>(c, A, X, sv, acc, ep);
  STAMP(3, 3);
  {
    float o[8];
#pragma unroll
    for (int e = 0; e < 8; ++e) {
      float g = acc[0][e];
      float ex = __builtin_amdgcn_exp2f(g * __builtin_fmaf(g * g, c.c3, c.c1));
      o[e] = g * acc[0][e + 8] * __builtin_amdgcn_rcpf(__builtin_fmaf(ex, c.cinv, c.cinv));
    }
    const int fb = c.fb0 + 8;
    hstore16(c.hbase + (size_t)(fb >> 1) * 4096 + (fb & 1) * 512, c.hvoff,
                u32x4{pack2(o[0], o[1]), pack2(o[2], o[3]), pack2(o[4], o[5]), pack2(o[6], o[7])});
  }
  STAMP(3, 4);
  sig_done(ws, SY_GU + 8 * lay_ + tt);
  STAMP(3, 10);
}

#define D_DEPTH_ 4
constexpr int D_DEPTH = D_DEPTH_;
template <int NC, int Sx> DI void d_load_b(u32x4 (&Bf)[D_DEPTH][4], const char* hs, unsigned l16) {
  constexpr int sl = Sx % D_DEPTH;
  const char* hp = hs + Sx * 4096;
  hload16<0>(Bf[sl][0], hp, l16); hload16<1024>(Bf[sl][1], hp, l16); hload16<2048>(Bf[sl][2], hp, l16); hload16<3072>(Bf[sl][3], hp, l16);
}
template <int NC, int Sx> DI void d_load_a(u32x4 (&Af)[D_DEPTH][5], const char* ws_, unsigned l16) {
  constexpr int sl = Sx % D_DEPTH;
  const char* wp = ws_ + Sx * NC * 1024;
  gload16<0>(Af[sl][0], wp, l16); gload16<1024>(Af[sl][1], wp, l16); gload16<2048>(Af[sl][2], wp, l16); gload16<3072>(Af[sl][3], wp, l16);
  if constexpr (NC == 5) gload16<0>(Af[sl][4], wp + 4096, l16);
}
struct DRes { const char* x1; unsigned off; u32x4 v[2]; };
constexpr int d_wait_count(int NC, int s) {
  const int per = 4 + NC;
  const int later = s < 36 - D_DEPTH ? s : 36 - D_DEPTH;
  const int issued = D_DEPTH * per + later * per;
  const int last = s < D_DEPTH ? D_DEPTH * NC + 4 * (s + 1) - 1 : D_DEPTH * per + (s - D_DEPTH + 1) * per - 1;
  return issued - 1 - last;
}
template <int NC, int Sx>
DI void d_steps(u32x4 (&Bf)[D_DEPTH][4], u32x4 (&Af)[D_DEPTH][5], f32x4 (&acc)[4][5], const char* hs, const char* ws_, unsigned l16, DRes& rs) {
  constexpr int sl = Sx % D_DEPTH;
  constexpr int n = d_wait_count(NC, Sx);
  if constexpr (NC == 5)
    asm volatile("s_waitcnt vmcnt(%9)" : "+v"(Bf[sl][0]), "+v"(Bf[sl][1]), "+v"(Bf[sl][2]), "+v"(Bf[sl][3]), "+v"(Af[sl][0]), "+v"(Af[sl][1]),
                 "+v"(Af[sl][2]), "+v"(Af[sl][3]), "+v"(Af[sl][4]) : "n"(n));
  else
    asm volatile("s_waitcnt vmcnt(%8)" : "+v"(Bf[sl][0]), "+v"(Bf[sl][1]), "+v"(Bf[sl][2]), "+v"(Bf[sl][3]), "+v"(Af[sl][0]), "+v"(Af[sl][1]),
                 "+v"(Af[sl][2]), "+v"(Af[sl][3]) : "n"(n));
#pragma unroll
  for (int c = 0; c < NC; ++c)
#pragma unroll
    for (int t = 0; t < 4; ++t) acc[t][c] = mfma16(Af[sl][c], Bf[sl][t], acc[t][c]);
  if constexpr (Sx + D_DEPTH < 36) {
    __builtin_amdgcn_sched_barrier(0);
    d_load_b<NC, Sx + D_DEPTH>(Bf, hs, l16);
    d_load_a<NC, Sx + D_DEPTH>(Af, ws_, l16);
  }
  if constexpr (Sx == 36 - D_DEPTH + 1) {
    __builtin_amdgcn_sched_barrier(0);
    hload16<0>(rs.v[0], rs.x1, rs.off); hload16<512>(rs.v[1], rs.x1, rs.off);
  }
  if constexpr (Sx + 1 < 36) d_steps<NC, Sx + 1>(Bf, Af, acc, hs, ws_, l16, rs);
}
constexpr int D_STRIDE = 84;
template <int NC>
DI void d_body(char* smem, char* ws, char* dout, const int lay_, int tt, int g, int tid, int lane, int w8) {
  const char* hf = ws + OFF_HF + lay_ * SZ_HF;
  const char* wdf = ws + OFF_WD;
  char* xout = ws + OFF_XF2;
  char* stout = ws + OFF_ST2;
  const unsigned l16 = lane * 16;
  const int st = dg_start(g);
  DRes rs;
  rs.x1 = ws + OFF_XF1 + lay_ * SZ_XF;
  const bool has = tid < 64 * NC;
  const int it = has ? tid : 0;
  int token, kl;
  if (lay_) { token = it / NC; kl = it - token * NC; } else { token = it & 63; kl = it >> 6; }
  const int gt = tt * 64 + token, ks = st + kl;
  rs.off = (((gt >> 5) * 36 + ks) * 64 + (gt & 31)) * 16;
  float* red = (float*)smem;
  STAMP(4, 0);
  if (w8 < 4) {
    const char* hs = hf + ((size_t)(tt * 144 + 36 * w8) * 4) * 1024;
    const char* ws_ = wdf + ((size_t)st * 144 + (size_t)36 * w8 * NC) * 1024;
    u32x4 Bf[D_DEPTH][4], Af[D_DEPTH][5];
    f32x4 acc[4][5];
#pragma unroll
    for (int t = 0; t < 4; ++t)
#pragma unroll
      for (int c = 0; c < 5; ++c) acc[t][c] = f32x4{0.f, 0.f, 0.f, 0.f};
    d_load_a<NC, 0>(Af, ws_, l16); d_load_a<NC, 1>(Af, ws_, l16); d_load_a<NC, 2>(Af, ws_, l16);
    if constexpr (D_DEPTH > 3) d_load_a<NC, 3>(Af, ws_, l16);
    STAMP(4, 6);
    dep_wait(ws, SY_GU + 8 * lay_ + (tt >> 2), 1, 32);
    STAMP(4, 7);
    d_load_b<NC, 0>(Bf, hs, l16); d_load_b<NC, 1>(Bf, hs, l16); d_load_b<NC, 2>(Bf, hs, l16);
    if constexpr (D_DEPTH > 3) d_load_b<NC, 3>(Bf, hs, l16);
    STAMP(4, 1);
    d_steps<NC, 0>(Bf, Af, acc, hs, ws_, l16, rs);
    STAMP(4, 2);
    const int q = lane >> 4, t = lane & 15;
    float* rp = red + (size_t)w8 * 64 * D_STRIDE + t * D_STRIDE + 4 * q;
#pragma unroll
    for (int tb = 0; tb < 4; ++tb)
#pragma unroll
      for (int c = 0; c < NC; ++c) *(f32x4*)(rp + tb * 16 * D_STRIDE + c * 16) = acc[tb][c];
  } else {
    dep_wait(ws, SY_GU + 8 * lay_ + (tt >> 2), 1, 32);
    hload16<0>(rs.v[0], rs.x1, rs.off); hload16<512>(rs.v[1], rs.x1, rs.off);
  }
  asm volatile("s_waitcnt vmcnt(0) lgkmcnt(0)" : "+v"(rs.v[0]), "+v"(rs.v[1]) :: "memory");
  bar();
  STAMP(4, 3);
  f32x2* sred = (f32x2*)(smem + 4 * 64 * D_STRIDE * 4);
  if (has) {
    const float* rp = red + token * D_STRIDE + kl * 16;
    float v[16];
#pragma unroll
    for (int i = 0; i < 4; ++i) {
      f32x4 a = *(const f32x4*)(rp + 4 * i), b = *(const f32x4*)(rp + 64 * D_STRIDE + 4 * i);
      f32x4 c = *(const f32x4*)(rp + 2 * 64 * D_STRIDE + 4 * i), d = *(const f32x4*)(rp + 3 * 64 * D_STRIDE + 4 * i);
#pragma unroll
      for (int j = 0; j < 4; ++j) v[4 * i + j] = (a[j] + b[j]) + (c[j] + d[j]);
    }
    const u32x4 r0 = rs.v[0], r1 = rs.v[1];
    u32x4 o0, o1;
#pragma unroll
    for (int i = 0; i < 4; ++i) {
      o0[i] = pack2(bflo(r0[i]) + v[2 * i], bfhi(r0[i]) + v[2 * i + 1]);
      o1[i] = pack2(bflo(r1[i]) + v[8 + 2 * i], bfhi(r1[i]) + v[8 + 2 * i + 1]);
    }
    if (lay_) {
      char* dst = dout + ((size_t)gt * D + 16 * ks) * 2;
      *(u32x4*)dst = o0; *(u32x4*)(dst + 16) = o1;
    } else {
      hstore16(xout, rs.off, o0); hstore16(xout + 512, rs.off, o1);
      float s = 0.f;
#pragma unroll
      for (int i = 0; i < 4; ++i) s += bflo(o0[i]) + bfhi(o0[i]) + bflo(o1[i]) + bfhi(o1[i]);
      float m = s * (1.f / 16.f), qq = 0.f;
#pragma unroll
      for (int i = 0; i < 4; ++i) {
        float a = bflo(o0[i]) - m, b = bfhi(o0[i]) - m, c = bflo(o1[i]) - m, d = bfhi(o1[i]) - m;
        qq += a * a + b * b + c * c + d * d;
      }
      sred[kl * 64 + token] = f32x2{m, qq};
    }
  }
  STAMP(4, 4);
  if (!lay_) {
    asm volatile("s_waitcnt lgkmcnt(0)" ::: "memory");
    bar();
    if (tid < 64) {
      float mm[NC], ms = 0.f, qq = 0.f;
#pragma unroll
      for (int i = 0; i < NC; ++i) { f32x2 v = sred[i * 64 + tid]; mm[i] = v[0]; ms += v[0]; qq += v[1]; }
      float mean = ms * (1.f / NC);
#pragma unroll
      for (int i = 0; i < NC; ++i) { float d = mm[i] - mean; qq += 16.f * d * d; }
      hstore8(stout, ((tt * 64 + tid) * 8 + g) * 8, u32x2{__builtin_bit_cast(unsigned, mean), __builtin_bit_cast(unsigned, qq)});
    }
    STAMP(4, 5);
    sig_done(ws, SY_D + (tt >> 1));
  }
}
DI void d_phase(char* smem, char* ws, char* dout, const int lay_) {
  const int tid = tid_opaque(), lane = tid & 63;
  const int w8 = __builtin_amdgcn_readfirstlane(tid >> 6);
  const int b = blockIdx.x, xcd = b & 7, idx = b >> 3;
  const int tt = 4 * xcd + (idx & 3), g = idx >> 2;
  if (g < 4) d_body<5>(smem, ws, dout, lay_, tt, g, tid, lane, w8);
  else d_body<4>(smem, ws, dout, lay_, tt, g, tid, lane, w8);
}

__global__ __launch_bounds__(512, 2) void k_layers(char* ws, char* dout) {
  __shared__ __attribute__((aligned(16))) char smem[KA_RING * 4096];
#pragma nounroll
  for (int l = 0; l < 2; ++l) {
    ka_phase(smem, ws, l);
    kb_phase(smem, ws, l);
    gu_phase(smem, ws, l);
    d_phase(smem, ws, dout, l);
  }
}

extern "C" void kernel_launch(void* const* d_in, const int* in_sizes, int n_in, void* d_out, int out_size, void* d_ws, size_t ws_size,
                              hipStream_t stream) {
  const unsigned short* x = (const unsigned short*)d_in[0];
  const unsigned short* wqkv = (const unsigned short*)d_in[1];
  const unsigned short* wo = (const unsigned short*)d_in[2];
  const unsigned short* wg = (const unsigned short*)d_in[4];
  const unsigned short* wu = (const unsigned short*)d_in[5];
  const unsigned short* wd = (const unsigned short*)d_in[6];
  char* ws = (char*)d_ws;
  k_prep<<<PB_TOTAL, 512, 0, stream>>>(x, wqkv, wo, wg, wu, wd, ws);
  k_layers<<<256, 512, 0, stream>>>(ws, (char*)d_out);
}
```
